```python
import torch

def _softmax(x, axis=-1):
    return torch.softmax(x, dim=axis)
def _gelu(x):
    return torch.nn.functional.gelu(x, approximate="tanh")
def _silu(x):
    return torch.nn.functional.silu(x)
def _sigmoid(x):
    return torch.sigmoid(x)
def _layernorm(x, axis=-1, eps=1e-5):
    m = x.mean(dim=axis, keepdim=True)
    v = x.var(dim=axis, keepdim=True, unbiased=False)
    return (x - m) / torch.sqrt(v + eps)
def _scatter_add(tgt, idx, src, axis=0):
    return tgt.clone().index_add_(axis, idx, src)
def _segment_sum(src, seg, n):
    out = torch.zeros((n,) + src.shape[1:], dtype=src.dtype)
    return out.index_add_(0, seg, src)
def _pad(x, pw):
    flat = tuple(v for pair in reversed(pw) for v in pair)
    return torch.nn.functional.pad(x, flat)

kernel_name = 'standalone__gqa_block_cached__near_ridge'

def setup_inputs():

    _w = (lambda a: (lambda t: t.to(torch.bfloat16) if t.is_floating_point() else t)(torch.as_tensor(a)))
    B, Sq, D = 8, 192, 640
    Hq, Hkv, Sk, Dh = 32, 32, 2048, 128
    assert Hq % Hkv == 0, (Hq, Hkv)
    return {
        "x": _w((lambda *s: torch.randn(*s, dtype=torch.bfloat16) * 0.8)(B, Sq, D)),
        "k_cache": _w((lambda *s: torch.randn(*s, dtype=torch.bfloat16) * 0.8)(B, Hkv, Sk, Dh)),
        "v_cache": _w((lambda *s: torch.randn(*s, dtype=torch.bfloat16) * 0.05)(B, Hkv, Sk, Dh)),
        "wq": _w((lambda *s: torch.randn(*s, dtype=torch.bfloat16) * 0.8)(D, Hq * Dh) * (D ** -0.5)),
        "wo": _w((lambda *s: torch.randn(*s, dtype=torch.bfloat16) * 0.05)(Hq * Dh, D) * ((Hq * Dh) ** -0.5)),
    }

def reference(x, k_cache, v_cache, wq, wo):
    B, Sq, D = x.shape
    Hq, Dh = 32, 128
    Hkv = k_cache.shape[1]
    rep = Hq // Hkv
    q = (lambda x, *d: x.permute(*d))((x @ wq).reshape(B, Sq, Hq, Dh), 0, 2, 1, 3)
    k = (lambda x, n, axis: torch.repeat_interleave(x, n, dim=axis))(k_cache, rep, axis=1)
    v = (lambda x, n, axis: torch.repeat_interleave(x, n, dim=axis))(v_cache, rep, axis=1)
    scores = torch.einsum("bhqd,bhkd->bhqk", q, k) * (Dh ** -0.5)
    p = _softmax(scores, axis=-1)
    o = torch.einsum("bhqk,bhkd->bhqd", p, v)
    return (lambda x, *d: x.permute(*d))(o, 0, 2, 1, 3).reshape(B, Sq, Hq * Dh) @ wo
```

```cpp
#include <hip/hip_runtime.h>
#include <hip/hip_bf16.h>

typedef unsigned short u16;
__device__ __forceinline__ float bf2f(u16 v) { return __uint_as_float(((unsigned)v) << 16); }
__device__ __forceinline__ u16 f2bf(float f) { __hip_bfloat16 b = __float2bfloat16(f); return *(u16*)&b; }

__global__ void gemm_naive(const u16* A, const u16* B, u16* C, int M, int N, int K) {
  __shared__ float As[16][17], Bs[16][17];
  int tx = threadIdx.x & 15, ty = threadIdx.x >> 4;
  int row = blockIdx.y * 16 + ty, col = blockIdx.x * 16 + tx;
  float acc = 0.f;
  for (int k0 = 0; k0 < K; k0 += 16) {
    As[ty][tx] = bf2f(A[(size_t)row * K + k0 + tx]);
    Bs[ty][tx] = bf2f(B[(size_t)(k0 + ty) * N + col]);
    __syncthreads();
    for (int k = 0; k < 16; ++k) acc += As[ty][k] * Bs[k][tx];
    __syncthreads();
  }
  C[(size_t)row * N + col] = f2bf(acc);
}

__global__ void attn_naive(const u16* Q, const u16* K, const u16* V, u16* O) {
  __shared__ float q[128], p[2048], red[256];
  int row = blockIdx.x % 192, bh = blockIdx.x / 192, h = bh % 32, b = bh / 32;
  int t = threadIdx.x;
  if (t < 128) q[t] = bf2f(Q[(size_t)(b * 192 + row) * 4096 + h * 128 + t]);
  __syncthreads();
  const u16* Kp = K + (size_t)bh * 2048 * 128;
  const u16* Vp = V + (size_t)bh * 2048 * 128;
  float mx = -1e30f;
  for (int k = t; k < 2048; k += 256) {
    float s = 0.f;
    for (int d = 0; d < 128; ++d) s += q[d] * bf2f(Kp[k * 128 + d]);
    s *= 0.08838834764831845f;
    p[k] = s; mx = fmaxf(mx, s);
  }
  red[t] = mx; __syncthreads();
  for (int o = 128; o > 0; o >>= 1) { if (t < o) red[t] = fmaxf(red[t], red[t + o]); __syncthreads(); }
  mx = red[0]; __syncthreads();
  float sum = 0.f;
  for (int k = t; k < 2048; k += 256) { float e = __expf(p[k] - mx); p[k] = e; sum += e; }
  red[t] = sum; __syncthreads();
  for (int o = 128; o > 0; o >>= 1) { if (t < o) red[t] += red[t + o]; __syncthreads(); }
  sum = red[0];
  if (t < 128) {
    float acc = 0.f;
    for (int k = 0; k < 2048; ++k) acc += p[k] * bf2f(Vp[k * 128 + t]);
    O[(size_t)(b * 192 + row) * 4096 + h * 128 + t] = f2bf(acc / sum);
  }
}

extern "C" void kernel_launch(void* const* d_in, const int* in_sizes, int n_in,
                              void* d_out, int out_size, void* d_ws, size_t ws_size,
                              hipStream_t stream) {
  const u16* x = (const u16*)d_in[0]; const u16* k = (const u16*)d_in[1]; const u16* v = (const u16*)d_in[2];
  const u16* wq = (const u16*)d_in[3]; const u16* wo = (const u16*)d_in[4];
  u16* Q = (u16*)d_ws; u16* O = Q + 1536 * 4096;
  gemm_naive<<<dim3(4096 / 16, 1536 / 16), 256, 0, stream>>>(x, wq, Q, 1536, 4096, 640);
  attn_naive<<<8 * 32 * 192, 256, 0, stream>>>(Q, k, v, O);
  gemm_naive<<<dim3(640 / 16, 1536 / 16), 256, 0, stream>>>(O, wo, (u16*)d_out, 1536, 640, 4096);
}
```

```cpp
#include <hip/hip_runtime.h>
#include <hip/hip_bf16.h>

__global__ __attribute__((aligned(4096))) __launch_bounds__(256, 1) void attn_kernel(const void* x, const void* kc, const void* vc, const void* wq, const void* wo, void* out, void* ws) {
  const unsigned wg = blockIdx.x;
  const unsigned wid = __builtin_amdgcn_readfirstlane(threadIdx.x >> 6);
  asm volatile(
    "s_nop 4\n\t"
    "s_memrealtime s[98:99]\n\t"
    "s_memtime s[18:19]\n\t"
    "s_load_dwordx8 s[4:11], %0, 0x0\n\t"
    "s_load_dwordx4 s[12:15], %0, 0x20\n\t"
    "s_load_dwordx2 s[16:17], %0, 0x30\n\t"
    "s_mov_b32 s73, %1\n\t"
    "s_mov_b32 s64, %2\n\t"
    "v_mbcnt_lo_u32_b32 v244, -1, 0\n\t"
    "v_mbcnt_hi_u32_b32 v244, -1, v244\n\t"
    "v_and_b32 v245, 15, v244\n\t"
    "v_lshrrev_b32 v246, 4, v244\n\t"
    "s_bfe_u32 s75, s73, 0x20003\n\t"
    "s_lshr_b32 s76, s73, 5\n\t"
    "s_and_b32 s74, s73, 7\n\t"
    "s_lshl2_add_u32 s65, s74, s75\n\t"
    "s_mov_b32 s66, s76\n\t"
    "s_add_u32 s76, s76, s73\n\t"
    "s_and_b32 s76, s76, 7\n\t"
    "s_mul_i32 s76, s76, 5\n\t"
    "s_add_u32 s76, s76, 2\n\t"
    "s_lshr_b32 s76, s76, 2\n\t"
    "s_and_b32 s75, s75, 0\n\t"
    "s_mul_i32 s75, s75, 5\n\t"
    "s_add_u32 s75, s75, s76\n\t"
    "s_sub_u32 s76, s75, 10\n\t"
    "s_min_u32 s75, s75, s76\n\t"
    "s_lshl_b32 s69, s75, 7\n\t"
    "s_lshl_b32 s67, s75, 19\n\t"
    "s_lshl_b32 s68, s64, 11\n\t"
    "s_lshl_b32 s74, s65, 8\n\t"
    "s_sub_u32 s75, 0x500000, s74\n\t"
    "s_mov_b32 s50, s75\n\t"
    "s_mov_b32 s51, 0x20000\n\t"
    "s_mov_b32 s48, s74\n\t"
    "s_lshl_b32 s76, s64, 3\n\t"
    "v_add_u32 v247, s76, v246\n\t"
    "s_and_b32 s76, s64, 1\n\t"
    "v_lshl_add_u32 v248, v246, 2, s76\n\t"
    "v_xor_b32 v248, v248, v245\n\t"
    "v_lshlrev_b32 v248, 4, v248\n\t"
    "v_lshl_add_u32 v140, v247, 13, v248\n\t"
    "v_add_u32 v141, 0x7c00, v140\n\t"
    "s_getpc_b64 s[84:85]\n\t"
    "Lpc%=:\n\t"
    "s_and_b32 s85, s85, 0xffff\n\t"
    "s_mov_b32 s86, Lend%=-Lpc%=\n\t"
    "s_mov_b32 s87, 0x20000\n\t"
    "s_lshr_b32 s74, s73, 3\n\t"
    "s_lshl2_add_u32 s74, s74, s64\n\t"
    "s_lshl_b32 s74, s74, 7\n\t"
    "v_lshl_add_u32 v254, v244, 14, s74\n\t"
    "buffer_load_dword v254, v254, s[84:87], 0 offen\n\t"
    "s_mul_i32 s74, s66, 0x3c000\n\t"
    "s_mov_b32 s38, 0x3c000\n\t"
    "s_mov_b32 s39, 0x20000\n\t"
    "s_mov_b32 s36, s74\n\t"
    "s_mul_i32 s77, s64, 48\n\t"
    "v_lshrrev_b32 v248, 3, v244\n\t"
    "v_add_u32 v248, s77, v248\n\t"
    "v_mul_u32_u24 v248, 0x500, v248\n\t"
    "v_and_b32 v249, 7, v244\n\t"
    "v_xor_b32 v249, v249, v246\n\t"
    "v_lshl_add_u32 v132, v249, 4, v248\n\t"
    "v_xor_b32 v133, 64, v132\n\t"
    "v_add_u32 v133, 0x2400, v133\n\t"
    "v_add_u32 v134, 0x5000, v132\n\t"
    "v_add_u32 v135, 0x5000, v133\n\t"
    "v_add_u32 v136, 0xa000, v132\n\t"
    "v_add_u32 v137, 0xa000, v133\n\t"
    "s_mul_i32 s23, s64, 0xf000\n\t"
    "s_add_u32 s23, s23, s36\n\t"
    "s_lshl_b32 s21, s66, 5\n\t"
    "s_add_u32 s21, s21, s65\n\t"
    "s_lshl_b32 s21, s21, 2\n\t"
    "s_add_u32 s21, s21, s64\n\t"
    "s_lshl_b32 s21, s21, 17\n\t"
    "v_mul_u32_u24 v108, 0x9e3775, v244\n\t"
    "v_mad_u32_u24 v109, v108, 61, v244\n\t"
    "v_mad_u32_u24 v110, v109, 61, v244\n\t"
    "v_mad_u32_u24 v111, v110, 61, v244\n\t"
    "v_mad_u32_u24 v112, v111, 61, v244\n\t"
    "v_mad_u32_u24 v113, v112, 61, v244\n\t"
    "v_mad_u32_u24 v114, v113, 61, v244\n\t"
    "v_mad_u32_u24 v115, v114, 61, v244\n\t"
    "v_mad_u32_u24 v116, v115, 61, v244\n\t"
    "v_mad_u32_u24 v117, v116, 61, v244\n\t"
    "v_mad_u32_u24 v118, v117, 61, v244\n\t"
    "v_mad_u32_u24 v119, v118, 61, v244\n\t"
    "v_mad_u32_u24 v120, v119, 61, v244\n\t"
    "v_mad_u32_u24 v121, v120, 61, v244\n\t"
    "v_mad_u32_u24 v122, v121, 61, v244\n\t"
    "v_mad_u32_u24 v123, v122, 61, v244\n\t"
    "v_mad_u32_u24 v124, v123, 61, v244\n\t"
    "v_mad_u32_u24 v125, v124, 61, v244\n\t"
    "v_mad_u32_u24 v126, v125, 61, v244\n\t"
    "v_mad_u32_u24 v127, v126, 61, v244\n\t"
    "v_mad_u32_u24 v128, v127, 61, v244\n\t"
    "v_mad_u32_u24 v129, v128, 61, v244\n\t"
    "v_mad_u32_u24 v130, v129, 61, v244\n\t"
    "v_mad_u32_u24 v131, v130, 61, v244\n\t"
    "s_mov_b32 s77, 0x807f807f\n\t"
    "v_mov_b32 v250, 0x3f003f00\n\t"
    "v_and_or_b32 v108, v108, s77, v250\n\t"
    "v_and_or_b32 v109, v109, s77, v250\n\t"
    "v_and_or_b32 v110, v110, s77, v250\n\t"
    "v_and_or_b32 v111, v111, s77, v250\n\t"
    "v_and_or_b32 v112, v112, s77, v250\n\t"
    "v_and_or_b32 v113, v113, s77, v250\n\t"
    "v_and_or_b32 v114, v114, s77, v250\n\t"
    "v_and_or_b32 v115, v115, s77, v250\n\t"
    "v_and_or_b32 v116, v116, s77, v250\n\t"
    "v_and_or_b32 v117, v117, s77, v250\n\t"
    "v_and_or_b32 v118, v118, s77, v250\n\t"
    "v_and_or_b32 v119, v119, s77, v250\n\t"
    "v_and_or_b32 v120, v120, s77, v250\n\t"
    "v_and_or_b32 v121, v121, s77, v250\n\t"
    "v_and_or_b32 v122, v122, s77, v250\n\t"
    "v_and_or_b32 v123, v123, s77, v250\n\t"
    "v_and_or_b32 v124, v124, s77, v250\n\t"
    "v_and_or_b32 v125, v125, s77, v250\n\t"
    "v_and_or_b32 v126, v126, s77, v250\n\t"
    "v_and_or_b32 v127, v127, s77, v250\n\t"
    "v_and_or_b32 v128, v128, s77, v250\n\t"
    "v_and_or_b32 v129, v129, s77, v250\n\t"
    "v_and_or_b32 v130, v130, s77, v250\n\t"
    "v_and_or_b32 v131, v131, s77, v250\n\t"
    "v_mfma_f32_16x16x32_bf16 v[0:3], v[108:111], v[120:123], 0\n\t"
    "v_mfma_f32_16x16x32_bf16 v[32:35], v[112:115], v[120:123], 0\n\t"
    "v_mfma_f32_16x16x32_bf16 v[64:67], v[116:119], v[124:127], 0\n\t"
    "v_mfma_f32_16x16x32_bf16 v[4:7], v[108:111], v[124:127], 0\n\t"
    "v_mfma_f32_16x16x32_bf16 v[36:39], v[112:115], v[128:131], 0\n\t"
    "v_mfma_f32_16x16x32_bf16 v[68:71], v[116:119], v[128:131], 0\n\t"
    "v_mfma_f32_16x16x32_bf16 v[8:11], v[108:111], v[120:123], 0\n\t"
    "v_mfma_f32_16x16x32_bf16 v[40:43], v[112:115], v[120:123], 0\n\t"
    "v_mfma_f32_16x16x32_bf16 v[72:75], v[116:119], v[124:127], 0\n\t"
    "v_mfma_f32_16x16x32_bf16 v[12:15], v[108:111], v[124:127], 0\n\t"
    "v_mfma_f32_16x16x32_bf16 v[44:47], v[112:115], v[128:131], 0\n\t"
    "v_mfma_f32_16x16x32_bf16 v[76:79], v[116:119], v[128:131], 0\n\t"
    "v_mfma_f32_16x16x32_bf16 v[16:19], v[108:111], v[120:123], 0\n\t"
    "v_mfma_f32_16x16x32_bf16 v[48:51], v[112:115], v[120:123], 0\n\t"
    "v_mfma_f32_16x16x32_bf16 v[80:83], v[116:119], v[124:127], 0\n\t"
    "v_mfma_f32_16x16x32_bf16 v[20:23], v[108:111], v[124:127], 0\n\t"
    "v_mfma_f32_16x16x32_bf16 v[52:55], v[112:115], v[128:131], 0\n\t"
    "v_mfma_f32_16x16x32_bf16 v[84:87], v[116:119], v[128:131], 0\n\t"
    "v_mfma_f32_16x16x32_bf16 v[24:27], v[108:111], v[120:123], 0\n\t"
    "v_mfma_f32_16x16x32_bf16 v[56:59], v[112:115], v[120:123], 0\n\t"
    "s_waitcnt lgkmcnt(0)\n\t"
    "s_load_dword s22, s[4:5], s23\n\t"
    "s_load_dword s22, s[6:7], s21\n\t"
    "s_load_dword s22, s[8:9], s21\n\t"
    "s_add_u32 s48, s10, s48\n\t"
    "s_addc_u32 s49, s11, 0\n\t"
    "s_and_b32 s49, s49, 0xffff\n\t"
    "s_add_u32 m0, s68, 0x20000\n\t"
    "s_nop 0\n\t"
    "buffer_load_dwordx4 v140, s[48:51], s67 offen lds\n\t"
    "v_mfma_f32_16x16x32_bf16 v[88:91], v[116:119], v[124:127], 0\n\t"
    "v_mfma_f32_16x16x32_bf16 v[28:31], v[108:111], v[124:127], 0\n\t"
    "v_mfma_f32_16x16x32_bf16 v[60:63], v[112:115], v[128:131], 0\n\t"
    "v_mfma_f32_16x16x32_bf16 v[92:95], v[116:119], v[128:131], 0\n\t"
    "buffer_load_dwordx4 v141, s[48:51], s67 offen offset:1024 lds\n\t"
    "s_add_u32 s67, s67, 0x40000\n\t"
    "v_mfma_f32_16x16x32_bf16 v[0:3], v[108:111], v[120:123], v[0:3]\n\t"
    "v_mfma_f32_16x16x32_bf16 v[32:35], v[112:115], v[120:123], v[32:35]\n\t"
    "v_mfma_f32_16x16x32_bf16 v[64:67], v[116:119], v[124:127], v[64:67]\n\t"
    "v_mfma_f32_16x16x32_bf16 v[4:7], v[108:111], v[124:127], v[4:7]\n\t"
    "s_add_u32 s36, s4, s36\n\t"
    "s_addc_u32 s37, s5, 0\n\t"
    "s_and_b32 s37, s37, 0xffff\n\t"
    "s_add_u32 m0, s68, 0x10000\n\t"
    "s_nop 0\n\t"
    "buffer_load_dwordx4 v132, s[36:39], s69 offen lds\n\t"
    "v_mfma_f32_16x16x32_bf16 v[36:39], v[112:115], v[128:131], v[36:39]\n\t"
    "v_mfma_f32_16x16x32_bf16 v[68:71], v[116:119], v[128:131], v[68:71]\n\t"
    "v_mfma_f32_16x16x32_bf16 v[8:11], v[108:111], v[120:123], v[8:11]\n\t"
    "v_mfma_f32_16x16x32_bf16 v[40:43], v[112:115], v[120:123], v[40:43]\n\t"
    "buffer_load_dwordx4 v133, s[36:39], s69 offen offset:1024 lds\n\t"
    "v_mfma_f32_16x16x32_bf16 v[72:75], v[116:119], v[124:127], v[72:75]\n\t"
    "v_mfma_f32_16x16x32_bf16 v[12:15], v[108:111], v[124:127], v[12:15]\n\t"
    "v_mfma_f32_16x16x32_bf16 v[44:47], v[112:115], v[128:131], v[44:47]\n\t"
    "v_mfma_f32_16x16x32_bf16 v[76:79], v[116:119], v[128:131], v[76:79]\n\t"
    "s_add_u32 m0, s68, 0x12000\n\t"
    "s_nop 0\n\t"
    "buffer_load_dwordx4 v134, s[36:39], s69 offen lds\n\t"
    "v_mfma_f32_16x16x32_bf16 v[16:19], v[108:111], v[120:123], v[16:19]\n\t"
    "v_mfma_f32_16x16x32_bf16 v[48:51], v[112:115], v[120:123], v[48:51]\n\t"
    "v_mfma_f32_16x16x32_bf16 v[80:83], v[116:119], v[124:127], v[80:83]\n\t"
    "v_mfma_f32_16x16x32_bf16 v[20:23], v[108:111], v[124:127], v[20:23]\n\t"
    "buffer_load_dwordx4 v135, s[36:39], s69 offen offset:1024 lds\n\t"
    "v_mfma_f32_16x16x32_bf16 v[52:55], v[112:115], v[128:131], v[52:55]\n\t"
    "v_mfma_f32_16x16x32_bf16 v[84:87], v[116:119], v[128:131], v[84:87]\n\t"
    "v_mfma_f32_16x16x32_bf16 v[24:27], v[108:111], v[120:123], v[24:27]\n\t"
    "v_mfma_f32_16x16x32_bf16 v[56:59], v[112:115], v[120:123], v[56:59]\n\t"
    "s_add_u32 m0, s68, 0x14000\n\t"
    "s_nop 0\n\t"
    "buffer_load_dwordx4 v136, s[36:39], s69 offen lds\n\t"
    "v_mfma_f32_16x16x32_bf16 v[88:91], v[116:119], v[124:127], v[88:91]\n\t"
    "v_mfma_f32_16x16x32_bf16 v[28:31], v[108:111], v[124:127], v[28:31]\n\t"
    "v_mfma_f32_16x16x32_bf16 v[60:63], v[112:115], v[128:131], v[60:63]\n\t"
    "v_mfma_f32_16x16x32_bf16 v[92:95], v[116:119], v[128:131], v[92:95]\n\t"
    "buffer_load_dwordx4 v137, s[36:39], s69 offen offset:1024 lds\n\t"
    "s_add_u32 s69, s69, 0x80\n\t"
    "s_cmp_eq_u32 s69, 0x500\n\t"
    "s_cselect_b32 s69, 0, s69\n\t"
    "v_mfma_f32_16x16x32_bf16 v[0:3], v[108:111], v[120:123], v[0:3]\n\t"
    "v_mfma_f32_16x16x32_bf16 v[32:35], v[112:115], v[120:123], v[32:35]\n\t"
    "v_mfma_f32_16x16x32_bf16 v[64:67], v[116:119], v[124:127], v[64:67]\n\t"
    "v_mfma_f32_16x16x32_bf16 v[4:7], v[108:111], v[124:127], v[4:7]\n\t"
    "s_add_u32 m0, s68, 0x22000\n\t"
    "s_nop 0\n\t"
    "buffer_load_dwordx4 v140, s[48:51], s67 offen lds\n\t"
    "v_mfma_f32_16x16x32_bf16 v[36:39], v[112:115], v[128:131], v[36:39]\n\t"
    "v_mfma_f32_16x16x32_bf16 v[68:71], v[116:119], v[128:131], v[68:71]\n\t"
    "v_mfma_f32_16x16x32_bf16 v[8:11], v[108:111], v[120:123], v[8:11]\n\t"
    "v_mfma_f32_16x16x32_bf16 v[40:43], v[112:115], v[120:123], v[40:43]\n\t"
    "buffer_load_dwordx4 v141, s[48:51], s67 offen offset:1024 lds\n\t"
    "s_add_u32 s67, s67, 0x40000\n\t"
    "s_cmp_eq_u32 s67, 0x500000\n\t"
    "s_cselect_b32 s67, 0, s67\n\t"
    "v_mfma_f32_16x16x32_bf16 v[72:75], v[116:119], v[124:127], v[72:75]\n\t"
    "v_mfma_f32_16x16x32_bf16 v[12:15], v[108:111], v[124:127], v[12:15]\n\t"
    "v_mfma_f32_16x16x32_bf16 v[44:47], v[112:115], v[128:131], v[44:47]\n\t"
    "v_mfma_f32_16x16x32_bf16 v[76:79], v[116:119], v[128:131], v[76:79]\n\t"
    "v_mfma_f32_16x16x32_bf16 v[16:19], v[108:111], v[120:123], v[16:19]\n\t"
    "s_lshl_b32 s74, s66, 5\n\t"
    "s_add_u32 s74, s74, s65\n\t"
    "s_lshr_b32 s75, s74, 13\n\t"
    "v_mfma_f32_16x16x32_bf16 v[48:51], v[112:115], v[120:123], v[48:51]\n\t"
    "s_lshl_b32 s74, s74, 19\n\t"
    "s_mov_b32 s42, 0x80000\n\t"
    "s_mov_b32 s43, 0x20000\n\t"
    "v_mfma_f32_16x16x32_bf16 v[80:83], v[116:119], v[124:127], v[80:83]\n\t"
    "s_add_u32 s40, s6, s74\n\t"
    "s_addc_u32 s41, s7, s75\n\t"
    "s_and_b32 s41, s41, 0xffff\n\t"
    "v_mfma_f32_16x16x32_bf16 v[20:23], v[108:111], v[124:127], v[20:23]\n\t"
    "s_mov_b32 s46, 0x80000\n\t"
    "s_mov_b32 s47, 0x20000\n\t"
    "s_add_u32 s44, s8, s74\n\t"
    "v_mfma_f32_16x16x32_bf16 v[52:55], v[112:115], v[128:131], v[52:55]\n\t"
    "s_addc_u32 s45, s9, s75\n\t"
    "s_and_b32 s45, s45, 0xffff\n\t"
    "v_add_u32 v248, 0, v247\n\t"
    "v_mfma_f32_16x16x32_bf16 v[84:87], v[116:119], v[128:131], v[84:87]\n\t"
    "v_and_b32 v248, 15, v248\n\t"
    "v_xor_b32 v248, v248, v245\n\t"
    "v_lshlrev_b32 v248, 4, v248\n\t"
    "v_mfma_f32_16x16x32_bf16 v[24:27], v[108:111], v[120:123], v[24:27]\n\t"
    "v_lshl_add_u32 v192, v247, 8, v248\n\t"
    "v_add_u32 v248, 4, v247\n\t"
    "v_and_b32 v248, 15, v248\n\t"
    "v_mfma_f32_16x16x32_bf16 v[56:59], v[112:115], v[120:123], v[56:59]\n\t"
    "v_xor_b32 v248, v248, v245\n\t"
    "v_lshlrev_b32 v248, 4, v248\n\t"
    "v_lshl_add_u32 v193, v247, 8, v248\n\t"
    "v_mfma_f32_16x16x32_bf16 v[88:91], v[116:119], v[124:127], v[88:91]\n\t"
    "v_add_u32 v248, 0, v246\n\t"
    "v_lshlrev_b32 v248, 1, v248\n\t"
    "v_xor_b32 v248, v248, v245\n\t"
    "v_mfma_f32_16x16x32_bf16 v[28:31], v[108:111], v[124:127], v[28:31]\n\t"
    "v_lshlrev_b32 v248, 4, v248\n\t"
    "v_lshl_add_u32 v194, v247, 8, v248\n\t"
    "v_add_u32 v248, 4, v246\n\t"
    "v_mfma_f32_16x16x32_bf16 v[60:63], v[112:115], v[128:131], v[60:63]\n\t"
    "v_lshlrev_b32 v248, 1, v248\n\t"
    "v_xor_b32 v248, v248, v245\n\t"
    "v_lshlrev_b32 v248, 4, v248\n\t"
    "v_mfma_f32_16x16x32_bf16 v[92:95], v[116:119], v[128:131], v[92:95]\n\t"
    "v_lshl_add_u32 v195, v247, 8, v248\n\t"
    "v_bfe_u32 v247, v244, 2, 2\n\t"
    "v_lshl_add_u32 v248, v246, 3, v247\n\t"
    "v_mfma_f32_16x16x32_bf16 v[0:3], v[108:111], v[120:123], v[0:3]\n\t"
    "v_lshlrev_b32 v248, 8, v248\n\t"
    "v_lshl_add_u32 v248, v247, 6, v248\n\t"
    "v_and_b32 v249, 1, v246\n\t"
    "v_mfma_f32_16x16x32_bf16 v[32:35], v[112:115], v[120:123], v[32:35]\n\t"
    "v_xor_b32 v249, v249, v244\n\t"
    "v_and_b32 v249, 3, v249\n\t"
    "v_lshl_add_u32 v248, v249, 4, v248\n\t"
    "v_mfma_f32_16x16x32_bf16 v[64:67], v[116:119], v[124:127], v[64:67]\n\t"
    "v_and_b32 v249, 1, v244\n\t"
    "v_lshl_add_u32 v248, v249, 3, v248\n\t"
    "v_add_u32 v144, 0x20000, v248\n\t"
    "v_mfma_f32_16x16x32_bf16 v[4:7], v[108:111], v[124:127], v[4:7]\n\t"
    "v_xor_b32 v145, 8, v144\n\t"
    "v_xor_b32 v146, 64, v144\n\t"
    "v_xor_b32 v147, 0x48, v144\n\t"
    "v_mfma_f32_16x16x32_bf16 v[36:39], v[112:115], v[128:131], v[36:39]\n\t"
    "v_xor_b32 v148, 0x80, v144\n\t"
    "v_xor_b32 v149, 0x88, v144\n\t"
    "v_xor_b32 v150, 0xc0, v144\n\t"
    "v_mfma_f32_16x16x32_bf16 v[68:71], v[116:119], v[128:131], v[68:71]\n\t"
    "v_xor_b32 v151, 0xc8, v144\n\t"
    "s_add_u32 s76, s68, 0x10000\n\t"
    "v_lshrrev_b32 v248, 1, v245\n\t"
    "v_mfma_f32_16x16x32_bf16 v[8:11], v[108:111], v[120:123], v[8:11]\n\t"
    "v_xor_b32 v248, v248, v246\n\t"
    "v_lshlrev_b32 v248, 4, v248\n\t"
    "v_lshl_add_u32 v248, v245, 7, v248\n\t"
    "v_mfma_f32_16x16x32_bf16 v[40:43], v[112:115], v[120:123], v[40:43]\n\t"
    "v_add_u32 v138, s76, v248\n\t"
    "v_xor_b32 v139, 64, v138\n\t"
    "v_lshrrev_b32 v247, 2, v244\n\t"
    "v_mfma_f32_16x16x32_bf16 v[72:75], v[116:119], v[124:127], v[72:75]\n\t"
    "v_and_b32 v248, 7, v247\n\t"
    "v_lshlrev_b32 v248, 5, v248\n\t"
    "v_lshl_add_u32 v248, v247, 8, v248\n\t"
    "v_mfma_f32_16x16x32_bf16 v[12:15], v[108:111], v[124:127], v[12:15]\n\t"
    "v_and_b32 v249, 3, v244\n\t"
    "v_lshl_add_u32 v176, v249, 3, v248\n\t"
    "v_xor_b32 v177, 32, v176\n\t"
    "v_mfma_f32_16x16x32_bf16 v[44:47], v[112:115], v[128:131], v[44:47]\n\t"
    "v_xor_b32 v178, 64, v176\n\t"
    "v_xor_b32 v179, 0x60, v176\n\t"
    "v_xor_b32 v180, 0x80, v176\n\t"
    "v_mfma_f32_16x16x32_bf16 v[76:79], v[116:119], v[128:131], v[76:79]\n\t"
    "v_xor_b32 v181, 0xa0, v176\n\t"
    "v_xor_b32 v182, 0xc0, v176\n\t"
    "v_xor_b32 v183, 0xe0, v176\n\t"
    "v_mfma_f32_16x16x32_bf16 v[16:19], v[108:111], v[120:123], v[16:19]\n\t"
    "v_add_u32 v184, 0x10000, v176\n\t"
    "v_add_u32 v185, 0x10000, v177\n\t"
    "v_add_u32 v186, 0x10000, v178\n\t"
    "v_mfma_f32_16x16x32_bf16 v[48:51], v[112:115], v[120:123], v[48:51]\n\t"
    "v_add_u32 v187, 0x10000, v179\n\t"
    "v_add_u32 v188, 0x10000, v180\n\t"
    "v_add_u32 v189, 0x10000, v181\n\t"
    "v_mfma_f32_16x16x32_bf16 v[80:83], v[116:119], v[124:127], v[80:83]\n\t"
    "v_add_u32 v190, 0x10000, v182\n\t"
    "v_add_u32 v191, 0x10000, v183\n\t"
    "s_lshl_b32 s72, s64, 12\n\t"
    "v_mfma_f32_16x16x32_bf16 v[20:23], v[108:111], v[124:127], v[20:23]\n\t"
    "s_add_u32 s74, s72, 0x20000\n\t"
    "v_lshlrev_b32 v247, 7, v247\n\t"
    "v_bfe_u32 v248, v244, 3, 2\n\t"
    "v_mfma_f32_16x16x32_bf16 v[52:55], v[112:115], v[128:131], v[52:55]\n\t"
    "v_lshl_add_u32 v247, v248, 5, v247\n\t"
    "v_lshl_add_u32 v247, v249, 3, v247\n\t"
    "v_add_u32 v100, s74, v247\n\t"
    "v_mfma_f32_16x16x32_bf16 v[84:87], v[116:119], v[128:131], v[84:87]\n\t"
    "v_xor_b32 v101, 32, v100\n\t"
    "v_xor_b32 v102, 64, v100\n\t"
    "v_xor_b32 v103, 0x60, v100\n\t"
    "v_mfma_f32_16x16x32_bf16 v[24:27], v[108:111], v[120:123], v[24:27]\n\t"
    "v_xor_b32 v247, v246, v245\n\t"
    "v_lshlrev_b32 v247, 4, v247\n\t"
    "v_lshl_add_u32 v168, v245, 8, v247\n\t"
    "v_mfma_f32_16x16x32_bf16 v[56:59], v[112:115], v[120:123], v[56:59]\n\t"
    "v_xor_b32 v169, 64, v168\n\t"
    "v_xor_b32 v170, 0x80, v168\n\t"
    "v_xor_b32 v171, 0xc0, v168\n\t"
    "v_mfma_f32_16x16x32_bf16 v[88:91], v[116:119], v[124:127], v[88:91]\n\t"
    "v_add_u32 v172, 0x10000, v168\n\t"
    "v_add_u32 v173, 0x10000, v169\n\t"
    "v_add_u32 v174, 0x10000, v170\n\t"
    "v_mfma_f32_16x16x32_bf16 v[28:31], v[108:111], v[124:127], v[28:31]\n\t"
    "v_add_u32 v175, 0x10000, v171\n\t"
    "v_mov_b32 v247, 0x3f803f80\n\t"
    "v_cmp_eq_u32 vcc, 0, v249\n\t"
    "v_mfma_f32_16x16x32_bf16 v[60:63], v[112:115], v[128:131], v[60:63]\n\t"
    "s_nop 0\n\t"
    "v_cndmask_b32 v248, 0, v247, vcc\n\t"
    "v_accvgpr_write_b32 a240, v248\n\t"
    "v_accvgpr_write_b32 a241, v248\n\t"
    "v_mfma_f32_16x16x32_bf16 v[92:95], v[116:119], v[128:131], v[92:95]\n\t"
    "v_accvgpr_write_b32 a242, v248\n\t"
    "v_accvgpr_write_b32 a243, v248\n\t"
    "v_cmp_eq_u32 vcc, 1, v249\n\t"
    "v_mfma_f32_16x16x32_bf16 v[0:3], v[108:111], v[120:123], v[0:3]\n\t"
    "s_nop 0\n\t"
    "v_cndmask_b32 v248, 0, v247, vcc\n\t"
    "v_accvgpr_write_b32 a244, v248\n\t"
    "v_accvgpr_write_b32 a245, v248\n\t"
    "v_mfma_f32_16x16x32_bf16 v[32:35], v[112:115], v[120:123], v[32:35]\n\t"
    "v_accvgpr_write_b32 a246, v248\n\t"
    "v_accvgpr_write_b32 a247, v248\n\t"
    "v_cmp_eq_u32 vcc, 2, v249\n\t"
    "v_mfma_f32_16x16x32_bf16 v[64:67], v[116:119], v[124:127], v[64:67]\n\t"
    "s_nop 0\n\t"
    "v_cndmask_b32 v248, 0, v247, vcc\n\t"
    "v_accvgpr_write_b32 a248, v248\n\t"
    "v_accvgpr_write_b32 a249, v248\n\t"
    "v_mfma_f32_16x16x32_bf16 v[4:7], v[108:111], v[124:127], v[4:7]\n\t"
    "v_accvgpr_write_b32 a250, v248\n\t"
    "v_accvgpr_write_b32 a251, v248\n\t"
    "s_mov_b32 s78, 0x3e0293ee\n\t"
    "v_mfma_f32_16x16x32_bf16 v[36:39], v[112:115], v[128:131], v[36:39]\n\t"
    "s_mov_b32 s79, 0x3e0293ee\n\t"
    "s_lshr_b32 s75, s73, 3\n\t"
    "s_mul_i32 s76, s75, 13\n\t"
    "v_mfma_f32_16x16x32_bf16 v[68:71], v[116:119], v[128:131], v[68:71]\n\t"
    "s_lshr_b32 s76, s76, 6\n\t"
    "s_mul_i32 s77, s76, 5\n\t"
    "s_sub_u32 s77, s75, s77\n\t"
    "v_mfma_f32_16x16x32_bf16 v[8:11], v[108:111], v[120:123], v[8:11]\n\t"
    "s_bfe_u32 s74, s73, 0x20001\n\t"
    "s_mul_i32 s74, s74, 6\n\t"
    "s_add_u32 s70, s74, s76\n\t"
    "v_mfma_f32_16x16x32_bf16 v[40:43], v[112:115], v[120:123], v[40:43]\n\t"
    "s_and_b32 s74, s73, 1\n\t"
    "s_mul_i32 s74, s74, 5\n\t"
    "s_add_u32 s71, s74, s77\n\t"
    "v_mfma_f32_16x16x32_bf16 v[72:75], v[116:119], v[124:127], v[72:75]\n\t"
    "s_mul_i32 s33, s70, 10\n\t"
    "s_add_u32 s33, s33, s71\n\t"
    "s_mul_i32 s34, s76, 5\n\t"
    "v_mfma_f32_16x16x32_bf16 v[12:15], v[108:111], v[124:127], v[12:15]\n\t"
    "s_mul_i32 s32, s34, 0xa000\n\t"
    "s_lshl_b32 s74, s71, 7\n\t"
    "s_mul_i32 s77, s64, 0x140000\n\t"
    "v_mfma_f32_16x16x32_bf16 v[44:47], v[112:115], v[128:131], v[44:47]\n\t"
    "s_add_u32 s77, s77, s74\n\t"
    "s_sub_u32 s76, 0x140000, s74\n\t"
    "s_cmp_lt_u32 s75, 30\n\t"
    "v_mfma_f32_16x16x32_bf16 v[76:79], v[116:119], v[128:131], v[76:79]\n\t"
    "s_cselect_b32 s33, s33, -1\n\t"
    "s_cselect_b32 s76, s76, 0\n\t"
    "s_cselect_b32 s70, s70, 0\n\t"
    "v_mfma_f32_16x16x32_bf16 v[16:19], v[108:111], v[120:123], v[16:19]\n\t"
    "s_mov_b32 s54, s76\n\t"
    "s_mov_b32 s55, 0x20000\n\t"
    "s_add_u32 s52, s12, s77\n\t"
    "v_mfma_f32_16x16x32_bf16 v[48:51], v[112:115], v[120:123], v[48:51]\n\t"
    "s_addc_u32 s53, s13, 0\n\t"
    "s_and_b32 s53, s53, 0xffff\n\t"
    "s_lshl_b32 s75, s70, 2\n\t"
    "v_mfma_f32_16x16x32_bf16 v[80:83], v[116:119], v[124:127], v[80:83]\n\t"
    "s_add_u32 s75, s75, s64\n\t"
    "s_mul_i32 s75, s75, 0x5000\n\t"
    "s_add_u32 s74, s74, s75\n\t"
    "v_mfma_f32_16x16x32_bf16 v[20:23], v[108:111], v[124:127], v[20:23]\n\t"
    "s_mov_b32 s58, 0x4b80\n\t"
    "s_mov_b32 s59, 0x20000\n\t"
    "s_add_u32 s56, s14, s74\n\t"
    "v_mfma_f32_16x16x32_bf16 v[52:55], v[112:115], v[128:131], v[52:55]\n\t"
    "s_addc_u32 s57, s15, 0\n\t"
    "s_and_b32 s57, s57, 0xffff\n\t"
    "s_lshl_b32 s74, s70, 19\n\t"
    "v_mfma_f32_16x16x32_bf16 v[84:87], v[116:119], v[128:131], v[84:87]\n\t"
    "s_mov_b32 s30, 0x80000\n\t"
    "s_mov_b32 s31, 0x20000\n\t"
    "s_add_u32 s28, s16, s74\n\t"
    "v_mfma_f32_16x16x32_bf16 v[24:27], v[108:111], v[120:123], v[24:27]\n\t"
    "s_addc_u32 s29, s17, 0\n\t"
    "s_and_b32 s29, s29, 0xffff\n\t"
    "s_mov_b32 s26, 0x2e000\n\t"
    "v_mfma_f32_16x16x32_bf16 v[56:59], v[112:115], v[120:123], v[56:59]\n\t"
    "s_mov_b32 s27, 0x20000\n\t"
    "s_add_u32 s24, s16, 0xe00000\n\t"
    "s_addc_u32 s25, s17, 0\n\t"
    "v_mfma_f32_16x16x32_bf16 v[88:91], v[116:119], v[124:127], v[88:91]\n\t"
    "s_and_b32 s25, s25, 0xffff\n\t"
    "s_lshl_b32 s74, s66, 2\n\t"
    "s_add_u32 s74, s74, s64\n\t"
    "v_mfma_f32_16x16x32_bf16 v[28:31], v[108:111], v[124:127], v[28:31]\n\t"
    "s_mul_i32 s74, s74, 0x60\n\t"
    "s_add_u32 s74, s74, s65\n\t"
    "s_lshl_b32 s74, s74, 12\n\t"
    "v_mfma_f32_16x16x32_bf16 v[60:63], v[112:115], v[128:131], v[60:63]\n\t"
    "s_mov_b32 s62, 0x41000\n\t"
    "s_mov_b32 s63, 0x20000\n\t"
    "s_add_u32 s60, s16, s74\n\t"
    "v_mfma_f32_16x16x32_bf16 v[92:95], v[116:119], v[128:131], v[92:95]\n\t"
    "s_addc_u32 s61, s17, 0\n\t"
    "s_and_b32 s61, s61, 0xffff\n\t"
    "s_mov_b32 s35, 1\n\t"
    "v_mfma_f32_16x16x32_bf16 v[0:3], v[108:111], v[120:123], v[0:3]\n\t"
    "v_lshrrev_b32 v247, 3, v244\n\t"
    "v_mul_u32_u24 v247, 0x500, v247\n\t"
    "v_and_b32 v248, 7, v244\n\t"
    "v_mfma_f32_16x16x32_bf16 v[32:35], v[112:115], v[120:123], v[32:35]\n\t"
    "v_lshlrev_b32 v249, 1, v246\n\t"
    "v_xor_b32 v248, v248, v249\n\t"
    "v_lshl_add_u32 v104, v248, 4, v247\n\t"
    "v_mfma_f32_16x16x32_bf16 v[64:67], v[116:119], v[124:127], v[64:67]\n\t"
    "v_add_u32 v105, 0x2400, v104\n\t"
    "v_add_u32 v106, 0x4800, v104\n\t"
    "v_add_u32 v107, 0x6c00, v104\n\t"
    "s_add_u32 m0, s68, 0x16000\n\t"
    "s_nop 0\n\t"
    "buffer_load_dwordx4 v132, s[36:39], s69 offen lds\n\t"
    "buffer_load_dwordx4 v133, s[36:39], s69 offen offset:1024 lds\n\t"
    "s_add_u32 m0, s68, 0x24000\n\t"
    "s_nop 0\n\t"
    "buffer_load_dwordx4 v140, s[48:51], s67 offen lds\n\t"
    "buffer_load_dwordx4 v141, s[48:51], s67 offen offset:1024 lds\n\t"
    "s_add_u32 s67, s67, 0x40000\n\t"
    "s_waitcnt vmcnt(12)\n\t"
    "s_barrier\n\t"
    "ds_read_b64_tr_b16 a[64:65], v144\n\t"
    "ds_read_b64_tr_b16 a[66:67], v144 offset:1024\n\t"
    "ds_read_b64_tr_b16 a[68:69], v145\n\t"
    "ds_read_b64_tr_b16 a[70:71], v145 offset:1024\n\t"
    "ds_read_b64_tr_b16 a[72:73], v146\n\t"
    "ds_read_b64_tr_b16 a[74:75], v146 offset:1024\n\t"
    "ds_read_b64_tr_b16 a[76:77], v147\n\t"
    "ds_read_b64_tr_b16 a[78:79], v147 offset:1024\n\t"
    "ds_read_b64_tr_b16 a[80:81], v148\n\t"
    "ds_read_b64_tr_b16 a[82:83], v148 offset:1024\n\t"
    "ds_read_b64_tr_b16 a[84:85], v149\n\t"
    "ds_read_b64_tr_b16 a[86:87], v149 offset:1024\n\t"
    "ds_read_b64_tr_b16 a[88:89], v150\n\t"
    "ds_read_b64_tr_b16 a[90:91], v150 offset:1024\n\t"
    "ds_read_b64_tr_b16 a[92:93], v151\n\t"
    "ds_read_b64_tr_b16 a[94:95], v151 offset:1024\n\t"
    "s_waitcnt vmcnt(10)\n\t"
    "ds_read_b128 v[196:199], v138\n\t"
    "ds_read_b128 v[208:211], v139\n\t"
    "s_add_u32 m0, s68, 0x1a000\n\t"
    "s_nop 0\n\t"
    "buffer_load_dwordx4 v134, s[36:39], s69 offen lds\n\t"
    "buffer_load_dwordx4 v135, s[36:39], s69 offen offset:1024 lds\n\t"
    "s_waitcnt vmcnt(10)\n\t"
    "ds_read_b128 v[200:203], v138 offset:8192\n\t"
    "ds_read_b128 v[212:215], v139 offset:8192\n\t"
    "s_add_u32 m0, s68, 0x1e000\n\t"
    "s_nop 0\n\t"
    "buffer_load_dwordx4 v136, s[36:39], s69 offen lds\n\t"
    "buffer_load_dwordx4 v137, s[36:39], s69 offen offset:1024 lds\n\t"
    "s_add_u32 s69, s69, 0x80\n\t"
    "s_cmp_eq_u32 s69, 0x500\n\t"
    "s_cselect_b32 s69, 0, s69\n\t"
    "s_waitcnt vmcnt(10)\n\t"
    "ds_read_b128 v[204:207], v138 offset:16384\n\t"
    "ds_read_b128 v[216:219], v139 offset:16384\n\t"
    "s_bfe_u32 s74, s73, 0x10004\n\t"
    "s_cmp_eq_u32 s74, 1\n\t"
    "s_cbranch_scc1 Lq1%=\n\t"
    "Lq0%=:\n\t"
    "s_waitcnt vmcnt(8) lgkmcnt(0)\n\t"
    "s_barrier\n\t"
    "v_mfma_f32_16x16x32_bf16 v[0:3], a[64:67], v[196:199], 0\n\t"
    "s_add_u32 m0, s68, 0x26000\n\t"
    "ds_read_b64_tr_b16 a[96:97], v144 offset:8192\n\t"
    "v_mfma_f32_16x16x32_bf16 v[32:35], a[64:67], v[200:203], 0\n\t"
    "ds_read_b64_tr_b16 a[98:99], v144 offset:9216\n\t"
    "ds_read_b64_tr_b16 a[100:101], v145 offset:8192\n\t"
    "v_mfma_f32_16x16x32_bf16 v[64:67], a[64:67], v[204:207], 0\n\t"
    "buffer_load_dwordx4 v140, s[48:51], s67 offen lds\n\t"
    "v_mfma_f32_16x16x32_bf16 v[68:71], a[68:71], v[204:207], 0\n\t"
    "ds_read_b64_tr_b16 a[102:103], v145 offset:9216\n\t"
    "ds_read_b64_tr_b16 a[104:105], v146 offset:8192\n\t"
    "v_mfma_f32_16x16x32_bf16 v[36:39], a[68:71], v[200:203], 0\n\t"
    "buffer_load_dwordx4 v141, s[48:51], s67 offen offset:1024 lds\n\t"
    "v_mfma_f32_16x16x32_bf16 v[4:7], a[68:71], v[196:199], 0\n\t"
    "s_add_u32 s67, s67, 0x40000\n\t"
    "s_cmp_eq_u32 s67, 0x500000\n\t"
    "s_cselect_b32 s67, 0, s67\n\t"
    "v_mfma_f32_16x16x32_bf16 v[8:11], a[72:75], v[196:199], 0\n\t"
    "s_add_u32 m0, s68, 0x10000\n\t"
    "ds_read_b64_tr_b16 a[106:107], v146 offset:9216\n\t"
    "ds_read_b64_tr_b16 a[108:109], v147 offset:8192\n\t"
    "v_mfma_f32_16x16x32_bf16 v[40:43], a[72:75], v[200:203], 0\n\t"
    "buffer_load_dwordx4 v132, s[36:39], s69 offen lds\n\t"
    "v_mfma_f32_16x16x32_bf16 v[72:75], a[72:75], v[204:207], 0\n\t"
    "ds_read_b64_tr_b16 a[110:111], v147 offset:9216\n\t"
    "ds_read_b64_tr_b16 a[112:113], v148 offset:8192\n\t"
    "v_mfma_f32_16x16x32_bf16 v[76:79], a[76:79], v[204:207], 0\n\t"
    "ds_read_b64_tr_b16 a[114:115], v148 offset:9216\n\t"
    "ds_read_b64_tr_b16 a[116:117], v149 offset:8192\n\t"
    "v_mfma_f32_16x16x32_bf16 v[44:47], a[76:79], v[200:203], 0\n\t"
    "buffer_load_dwordx4 v133, s[36:39], s69 offen offset:1024 lds\n\t"
    "v_mfma_f32_16x16x32_bf16 v[12:15], a[76:79], v[196:199], 0\n\t"
    "s_add_u32 m0, s68, 0x20000\n\t"
    "ds_read_b64_tr_b16 a[118:119], v149 offset:9216\n\t"
    "ds_read_b64_tr_b16 a[120:121], v150 offset:8192\n\t"
    "v_mfma_f32_16x16x32_bf16 v[16:19], a[80:83], v[196:199], 0\n\t"
    "ds_read_b64_tr_b16 a[122:123], v150 offset:9216\n\t"
    "ds_read_b64_tr_b16 a[124:125], v151 offset:8192\n\t"
    "v_mfma_f32_16x16x32_bf16 v[48:51], a[80:83], v[200:203], 0\n\t"
    "buffer_load_dwordx4 v140, s[48:51], s67 offen lds\n\t"
    "v_mfma_f32_16x16x32_bf16 v[80:83], a[80:83], v[204:207], 0\n\t"
    "ds_read_b64_tr_b16 a[126:127], v151 offset:9216\n\t"
    "v_mfma_f32_16x16x32_bf16 v[84:87], a[84:87], v[204:207], 0\n\t"
    "buffer_load_dwordx4 v141, s[48:51], s67 offen offset:1024 lds\n\t"
    "v_mfma_f32_16x16x32_bf16 v[52:55], a[84:87], v[200:203], 0\n\t"
    "s_add_u32 s67, s67, 0x40000\n\t"
    "s_add_u32 m0, s68, 0x12000\n\t"
    "v_mfma_f32_16x16x32_bf16 v[20:23], a[84:87], v[196:199], 0\n\t"
    "s_waitcnt vmcnt(12)\n\t"
    "ds_read_b128 v[220:223], v138 offset:24576\n\t"
    "ds_read_b128 v[232:235], v139 offset:24576\n\t"
    "v_mfma_f32_16x16x32_bf16 v[24:27], a[88:91], v[196:199], 0\n\t"
    "buffer_load_dwordx4 v134, s[36:39], s69 offen lds\n\t"
    "v_mfma_f32_16x16x32_bf16 v[56:59], a[88:91], v[200:203], 0\n\t"
    "v_mfma_f32_16x16x32_bf16 v[88:91], a[88:91], v[204:207], 0\n\t"
    "v_mfma_f32_16x16x32_bf16 v[92:95], a[92:95], v[204:207], 0\n\t"
    "buffer_load_dwordx4 v135, s[36:39], s69 offen offset:1024 lds\n\t"
    "v_mfma_f32_16x16x32_bf16 v[60:63], a[92:95], v[200:203], 0\n\t"
    "v_mfma_f32_16x16x32_bf16 v[28:31], a[92:95], v[196:199], 0\n\t"
    "s_waitcnt vmcnt(12) lgkmcnt(2)\n\t"
    "s_barrier\n\t"
    "v_mfma_f32_16x16x32_bf16 v[0:3], a[96:99], v[208:211], v[0:3]\n\t"
    "s_add_u32 m0, s68, 0x14000\n\t"
    "ds_read_b64_tr_b16 a[64:65], v144 offset:16384\n\t"
    "v_mfma_f32_16x16x32_bf16 v[32:35], a[96:99], v[212:215], v[32:35]\n\t"
    "ds_read_b64_tr_b16 a[66:67], v144 offset:17408\n\t"
    "v_mfma_f32_16x16x32_bf16 v[64:67], a[96:99], v[216:219], v[64:67]\n\t"
    "buffer_load_dwordx4 v136, s[36:39], s69 offen lds\n\t"
    "v_mfma_f32_16x16x32_bf16 v[68:71], a[100:103], v[216:219], v[68:71]\n\t"
    "ds_read_b64_tr_b16 a[68:69], v145 offset:16384\n\t"
    "ds_read_b64_tr_b16 a[70:71], v145 offset:17408\n\t"
    "v_mfma_f32_16x16x32_bf16 v[36:39], a[100:103], v[212:215], v[36:39]\n\t"
    "ds_read_b64_tr_b16 a[72:73], v146 offset:16384\n\t"
    "ds_read_b64_tr_b16 a[74:75], v146 offset:17408\n\t"
    "v_mfma_f32_16x16x32_bf16 v[4:7], a[100:103], v[208:211], v[4:7]\n\t"
    "buffer_load_dwordx4 v137, s[36:39], s69 offen offset:1024 lds\n\t"
    "v_mfma_f32_16x16x32_bf16 v[8:11], a[104:107], v[208:211], v[8:11]\n\t"
    "s_add_u32 s69, s69, 0x80\n\t"
    "ds_read_b64_tr_b16 a[76:77], v147 offset:16384\n\t"
    "ds_read_b64_tr_b16 a[78:79], v147 offset:17408\n\t"
    "v_mfma_f32_16x16x32_bf16 v[40:43], a[104:107], v[212:215], v[40:43]\n\t"
    "s_cmp_eq_u32 s69, 0x500\n\t"
    "s_cselect_b32 s69, 0, s69\n\t"
    "s_add_u32 m0, s68, 0x22000\n\t"
    "v_mfma_f32_16x16x32_bf16 v[72:75], a[104:107], v[216:219], v[72:75]\n\t"
    "ds_read_b64_tr_b16 a[80:81], v148 offset:16384\n\t"
    "ds_read_b64_tr_b16 a[82:83], v148 offset:17408\n\t"
    "v_mfma_f32_16x16x32_bf16 v[76:79], a[108:111], v[216:219], v[76:79]\n\t"
    "buffer_load_dwordx4 v140, s[48:51], s67 offen lds\n\t"
    "v_mfma_f32_16x16x32_bf16 v[44:47], a[108:111], v[212:215], v[44:47]\n\t"
    "ds_read_b64_tr_b16 a[84:85], v149 offset:16384\n\t"
    "ds_read_b64_tr_b16 a[86:87], v149 offset:17408\n\t"
    "v_mfma_f32_16x16x32_bf16 v[12:15], a[108:111], v[208:211], v[12:15]\n\t"
    "ds_read_b64_tr_b16 a[88:89], v150 offset:16384\n\t"
    "ds_read_b64_tr_b16 a[90:91], v150 offset:17408\n\t"
    "v_mfma_f32_16x16x32_bf16 v[16:19], a[112:115], v[208:211], v[16:19]\n\t"
    "s_waitcnt lgkmcnt(14)\n\t"
    "ds_read_b64_tr_b16 a[92:93], v151 offset:16384\n\t"
    "ds_read_b64_tr_b16 a[94:95], v151 offset:17408\n\t"
    "v_mfma_f32_16x16x32_bf16 v[48:51], a[112:115], v[212:215], v[48:51]\n\t"
    "buffer_load_dwordx4 v141, s[48:51], s67 offen offset:1024 lds\n\t"
    "v_mfma_f32_16x16x32_bf16 v[80:83], a[112:115], v[216:219], v[80:83]\n\t"
    "s_add_u32 s67, s67, 0x40000\n\t"
    "s_cmp_eq_u32 s67, 0x500000\n\t"
    "s_cselect_b32 s67, 0, s67\n\t"
    "v_mfma_f32_16x16x32_bf16 v[84:87], a[116:119], v[216:219], v[84:87]\n\t"
    "s_add_u32 m0, s68, 0x16000\n\t"
    "v_mfma_f32_16x16x32_bf16 v[52:55], a[116:119], v[212:215], v[52:55]\n\t"
    "s_waitcnt vmcnt(14)\n\t"
    "buffer_load_dwordx4 v132, s[36:39], s69 offen lds\n\t"
    "v_mfma_f32_16x16x32_bf16 v[20:23], a[116:119], v[208:211], v[20:23]\n\t"
    "ds_read_b128 v[224:227], v138 offset:40960\n\t"
    "ds_read_b128 v[236:239], v139 offset:40960\n\t"
    "v_mfma_f32_16x16x32_bf16 v[24:27], a[120:123], v[208:211], v[24:27]\n\t"
    "s_waitcnt vmcnt(13)\n\t"
    "ds_read_b128 v[228:231], v138 offset:57344\n\t"
    "ds_read_b128 v[240:243], v139 offset:57344\n\t"
    "v_mfma_f32_16x16x32_bf16 v[56:59], a[120:123], v[212:215], v[56:59]\n\t"
    "v_mfma_f32_16x16x32_bf16 v[88:91], a[120:123], v[216:219], v[88:91]\n\t"
    "buffer_load_dwordx4 v133, s[36:39], s69 offen offset:1024 lds\n\t"
    "v_mfma_f32_16x16x32_bf16 v[92:95], a[124:127], v[216:219], v[92:95]\n\t"
    "v_mfma_f32_16x16x32_bf16 v[60:63], a[124:127], v[212:215], v[60:63]\n\t"
    "v_mfma_f32_16x16x32_bf16 v[28:31], a[124:127], v[208:211], v[28:31]\n\t"
    "s_waitcnt vmcnt(12) lgkmcnt(4)\n\t"
    "s_barrier\n\t"
    "v_mfma_f32_16x16x32_bf16 v[0:3], a[64:67], v[220:223], v[0:3]\n\t"
    "s_waitcnt lgkmcnt(3)\n\t"
    "s_add_u32 m0, s68, 0x24000\n\t"
    "ds_read_b64_tr_b16 a[96:97], v144 offset:24576\n\t"
    "v_mfma_f32_16x16x32_bf16 v[32:35], a[64:67], v[224:227], v[32:35]\n\t"
    "s_waitcnt lgkmcnt(2)\n\t"
    "ds_read_b64_tr_b16 a[98:99], v144 offset:25600\n\t"
    "ds_read_b64_tr_b16 a[100:101], v145 offset:24576\n\t"
    "v_mfma_f32_16x16x32_bf16 v[64:67], a[64:67], v[228:231], v[64:67]\n\t"
    "buffer_load_dwordx4 v140, s[48:51], s67 offen lds\n\t"
    "v_mfma_f32_16x16x32_bf16 v[68:71], a[68:71], v[228:231], v[68:71]\n\t"
    "ds_read_b64_tr_b16 a[102:103], v145 offset:25600\n\t"
    "ds_read_b64_tr_b16 a[104:105], v146 offset:24576\n\t"
    "v_mfma_f32_16x16x32_bf16 v[36:39], a[68:71], v[224:227], v[36:39]\n\t"
    "buffer_load_dwordx4 v141, s[48:51], s67 offen offset:1024 lds\n\t"
    "v_mfma_f32_16x16x32_bf16 v[4:7], a[68:71], v[220:223], v[4:7]\n\t"
    "s_add_u32 s67, s67, 0x40000\n\t"
    "s_add_u32 m0, s68, 0x1a000\n\t"
    "ds_read_b64_tr_b16 a[106:107], v146 offset:25600\n\t"
    "v_mfma_f32_16x16x32_bf16 v[8:11], a[72:75], v[220:223], v[8:11]\n\t"
    "s_waitcnt lgkmcnt(6)\n\t"
    "buffer_load_dwordx4 v134, s[36:39], s69 offen lds\n\t"
    "v_mfma_f32_16x16x32_bf16 v[40:43], a[72:75], v[224:227], v[40:43]\n\t"
    "ds_read_b64_tr_b16 a[108:109], v147 offset:24576\n\t"
    "ds_read_b64_tr_b16 a[110:111], v147 offset:25600\n\t"
    "v_mfma_f32_16x16x32_bf16 v[72:75], a[72:75], v[228:231], v[72:75]\n\t"
    "buffer_load_dwordx4 v135, s[36:39], s69 offen offset:1024 lds\n\t"
    "v_mfma_f32_16x16x32_bf16 v[76:79], a[76:79], v[228:231], v[76:79]\n\t"
    "s_add_u32 m0, s68, 0x1e000\n\t"
    "ds_read_b64_tr_b16 a[112:113], v148 offset:24576\n\t"
    "ds_read_b64_tr_b16 a[114:115], v148 offset:25600\n\t"
    "v_mfma_f32_16x16x32_bf16 v[44:47], a[76:79], v[224:227], v[44:47]\n\t"
    "buffer_load_dwordx4 v136, s[36:39], s69 offen lds\n\t"
    "v_mfma_f32_16x16x32_bf16 v[12:15], a[76:79], v[220:223], v[12:15]\n\t"
    "ds_read_b64_tr_b16 a[116:117], v149 offset:24576\n\t"
    "ds_read_b64_tr_b16 a[118:119], v149 offset:25600\n\t"
    "v_mfma_f32_16x16x32_bf16 v[16:19], a[80:83], v[220:223], v[16:19]\n\t"
    "ds_read_b64_tr_b16 a[120:121], v150 offset:24576\n\t"
    "ds_read_b64_tr_b16 a[122:123], v150 offset:25600\n\t"
    "v_mfma_f32_16x16x32_bf16 v[48:51], a[80:83], v[224:227], v[48:51]\n\t"
    "buffer_load_dwordx4 v137, s[36:39], s69 offen offset:1024 lds\n\t"
    "v_mfma_f32_16x16x32_bf16 v[80:83], a[80:83], v[228:231], v[80:83]\n\t"
    "s_add_u32 s69, s69, 0x80\n\t"
    "s_cmp_eq_u32 s69, 0x500\n\t"
    "s_cselect_b32 s69, 0, s69\n\t"
    "v_mfma_f32_16x16x32_bf16 v[84:87], a[84:87], v[228:231], v[84:87]\n\t"
    "s_add_u32 m0, s68, 0x18000\n\t"
    "ds_read_b64_tr_b16 a[124:125], v151 offset:24576\n\t"
    "ds_read_b64_tr_b16 a[126:127], v151 offset:25600\n\t"
    "v_mfma_f32_16x16x32_bf16 v[52:55], a[84:87], v[224:227], v[52:55]\n\t"
    "s_mov_b32 s74, 0x2000\n\t"
    "buffer_load_dwordx4 v192, s[40:43], 0 offen nt lds\n\t"
    "v_mfma_f32_16x16x32_bf16 v[20:23], a[84:87], v[220:223], v[20:23]\n\t"
    "s_waitcnt vmcnt(17)\n\t"
    "buffer_load_dwordx4 v193, s[40:43], 0 offen offset:1024 nt lds\n\t"
    "v_mfma_f32_16x16x32_bf16 v[24:27], a[88:91], v[220:223], v[24:27]\n\t"
    "s_add_u32 m0, s68, 0x1c000\n\t"
    "ds_read_b128 v[196:199], v138\n\t"
    "ds_read_b128 v[208:211], v139\n\t"
    "v_mfma_f32_16x16x32_bf16 v[56:59], a[88:91], v[224:227], v[56:59]\n\t"
    "buffer_load_dwordx4 v192, s[40:43], s74 offen nt lds\n\t"
    "v_mfma_f32_16x16x32_bf16 v[88:91], a[88:91], v[228:231], v[88:91]\n\t"
    "v_mfma_f32_16x16x32_bf16 v[92:95], a[92:95], v[228:231], v[92:95]\n\t"
    "buffer_load_dwordx4 v193, s[40:43], s74 offen offset:1024 nt lds\n\t"
    "v_mfma_f32_16x16x32_bf16 v[60:63], a[92:95], v[224:227], v[60:63]\n\t"
    "v_mfma_f32_16x16x32_bf16 v[28:31], a[92:95], v[220:223], v[28:31]\n\t"
    "s_waitcnt vmcnt(18) lgkmcnt(2)\n\t"
    "s_barrier\n\t"
    "v_mfma_f32_16x16x32_bf16 v[0:3], a[96:99], v[232:235], v[0:3]\n\t"
    "s_add_u32 m0, s68, 0x26000\n\t"
    "ds_read_b64_tr_b16 a[64:65], v144\n\t"
    "v_mfma_f32_16x16x32_bf16 v[32:35], a[96:99], v[236:239], v[32:35]\n\t"
    "ds_read_b64_tr_b16 a[66:67], v144 offset:1024\n\t"
    "ds_read_b64_tr_b16 a[68:69], v145\n\t"
    "v_mfma_f32_16x16x32_bf16 v[64:67], a[96:99], v[240:243], v[64:67]\n\t"
    "buffer_load_dwordx4 v140, s[48:51], s67 offen lds\n\t"
    "v_mfma_f32_16x16x32_bf16 v[68:71], a[100:103], v[240:243], v[68:71]\n\t"
    "ds_read_b64_tr_b16 a[70:71], v145 offset:1024\n\t"
    "ds_read_b64_tr_b16 a[72:73], v146\n\t"
    "v_mfma_f32_16x16x32_bf16 v[36:39], a[100:103], v[236:239], v[36:39]\n\t"
    "ds_read_b64_tr_b16 a[74:75], v146 offset:1024\n\t"
    "ds_read_b64_tr_b16 a[76:77], v147\n\t"
    "v_mfma_f32_16x16x32_bf16 v[4:7], a[100:103], v[232:235], v[4:7]\n\t"
    "ds_read_b64_tr_b16 a[78:79], v147 offset:1024\n\t"
    "ds_read_b64_tr_b16 a[80:81], v148\n\t"
    "v_mfma_f32_16x16x32_bf16 v[8:11], a[104:107], v[232:235], v[8:11]\n\t"
    "ds_read_b64_tr_b16 a[82:83], v148 offset:1024\n\t"
    "v_mfma_f32_16x16x32_bf16 v[40:43], a[104:107], v[236:239], v[40:43]\n\t"
    "buffer_load_dwordx4 v141, s[48:51], s67 offen offset:1024 lds\n\t"
    "v_mfma_f32_16x16x32_bf16 v[72:75], a[104:107], v[240:243], v[72:75]\n\t"
    "s_add_u32 s67, s67, 0x40000\n\t"
    "ds_read_b64_tr_b16 a[84:85], v149\n\t"
    "ds_read_b64_tr_b16 a[86:87], v149 offset:1024\n\t"
    "v_mfma_f32_16x16x32_bf16 v[76:79], a[108:111], v[240:243], v[76:79]\n\t"
    "s_cmp_eq_u32 s67, 0x500000\n\t"
    "ds_read_b64_tr_b16 a[88:89], v150\n\t"
    "v_mfma_f32_16x16x32_bf16 v[44:47], a[108:111], v[236:239], v[44:47]\n\t"
    "s_cselect_b32 s67, 0, s67\n\t"
    "ds_read_b64_tr_b16 a[90:91], v150 offset:1024\n\t"
    "v_mfma_f32_16x16x32_bf16 v[12:15], a[108:111], v[232:235], v[12:15]\n\t"
    "s_add_u32 m0, s68, 0x10000\n\t"
    "ds_read_b64_tr_b16 a[92:93], v151\n\t"
    "v_mfma_f32_16x16x32_bf16 v[16:19], a[112:115], v[232:235], v[16:19]\n\t"
    "s_waitcnt lgkmcnt(15)\n\t"
    "ds_read_b64_tr_b16 a[94:95], v151 offset:1024\n\t"
    "v_mfma_f32_16x16x32_bf16 v[48:51], a[112:115], v[236:239], v[48:51]\n\t"
    "buffer_load_dwordx4 v132, s[36:39], s69 offen lds\n\t"
    "v_mfma_f32_16x16x32_bf16 v[80:83], a[112:115], v[240:243], v[80:83]\n\t"
    "v_mfma_f32_16x16x32_bf16 v[84:87], a[116:119], v[240:243], v[84:87]\n\t"
    "v_mfma_f32_16x16x32_bf16 v[52:55], a[116:119], v[236:239], v[52:55]\n\t"
    "s_waitcnt vmcnt(19)\n\t"
    "ds_read_b128 v[200:203], v138 offset:8192\n\t"
    "ds_read_b128 v[212:215], v139 offset:8192\n\t"
    "v_mfma_f32_16x16x32_bf16 v[20:23], a[116:119], v[232:235], v[20:23]\n\t"
    "v_mfma_f32_16x16x32_bf16 v[24:27], a[120:123], v[232:235], v[24:27]\n\t"
    "s_waitcnt vmcnt(17)\n\t"
    "buffer_load_dwordx4 v133, s[36:39], s69 offen offset:1024 lds\n\t"
    "v_mfma_f32_16x16x32_bf16 v[56:59], a[120:123], v[236:239], v[56:59]\n\t"
    "ds_read_b128 v[204:207], v138 offset:16384\n\t"
    "ds_read_b128 v[216:219], v139 offset:16384\n\t"
    "v_mfma_f32_16x16x32_bf16 v[88:91], a[120:123], v[240:243], v[88:91]\n\t"
    "v_mfma_f32_16x16x32_bf16 v[92:95], a[124:127], v[240:243], v[92:95]\n\t"
    "v_mfma_f32_16x16x32_bf16 v[60:63], a[124:127], v[236:239], v[60:63]\n\t"
    "v_mfma_f32_16x16x32_bf16 v[28:31], a[124:127], v[232:235], v[28:31]\n\t"
    "s_waitcnt vmcnt(16) lgkmcnt(4)\n\t"
    "s_barrier\n\t"
    "v_mfma_f32_16x16x32_bf16 v[0:3], a[64:67], v[196:199], v[0:3]\n\t"
    "s_waitcnt lgkmcnt(3)\n\t"
    "s_add_u32 m0, s68, 0x20000\n\t"
    "ds_read_b64_tr_b16 a[96:97], v144 offset:8192\n\t"
    "v_mfma_f32_16x16x32_bf16 v[32:35], a[64:67], v[200:203], v[32:35]\n\t"
    "s_waitcnt lgkmcnt(2)\n\t"
    "ds_read_b64_tr_b16 a[98:99], v144 offset:9216\n\t"
    "ds_read_b64_tr_b16 a[100:101], v145 offset:8192\n\t"
    "v_mfma_f32_16x16x32_bf16 v[64:67], a[64:67], v[204:207], v[64:67]\n\t"
    "buffer_load_dwordx4 v140, s[48:51], s67 offen lds\n\t"
    "v_mfma_f32_16x16x32_bf16 v[68:71], a[68:71], v[204:207], v[68:71]\n\t"
    "ds_read_b64_tr_b16 a[102:103], v145 offset:9216\n\t"
    "ds_read_b64_tr_b16 a[104:105], v146 offset:8192\n\t"
    "v_mfma_f32_16x16x32_bf16 v[36:39], a[68:71], v[200:203], v[36:39]\n\t"
    "ds_read_b64_tr_b16 a[106:107], v146 offset:9216\n\t"
    "ds_read_b64_tr_b16 a[108:109], v147 offset:8192\n\t"
    "v_mfma_f32_16x16x32_bf16 v[4:7], a[68:71], v[196:199], v[4:7]\n\t"
    "buffer_load_dwordx4 v141, s[48:51], s67 offen offset:1024 lds\n\t"
    "v_mfma_f32_16x16x32_bf16 v[8:11], a[72:75], v[196:199], v[8:11]\n\t"
    "s_add_u32 s67, s67, 0x40000\n\t"
    "ds_read_b64_tr_b16 a[110:111], v147 offset:9216\n\t"
    "ds_read_b64_tr_b16 a[112:113], v148 offset:8192\n\t"
    "v_mfma_f32_16x16x32_bf16 v[40:43], a[72:75], v[200:203], v[40:43]\n\t"
    "s_add_u32 m0, s68, 0x12000\n\t"
    "ds_read_b64_tr_b16 a[114:115], v148 offset:9216\n\t"
    "ds_read_b64_tr_b16 a[116:117], v149 offset:8192\n\t"
    "v_mfma_f32_16x16x32_bf16 v[72:75], a[72:75], v[204:207], v[72:75]\n\t"
    "ds_read_b64_tr_b16 a[118:119], v149 offset:9216\n\t"
    "v_mfma_f32_16x16x32_bf16 v[76:79], a[76:79], v[204:207], v[76:79]\n\t"
    "buffer_load_dwordx4 v134, s[36:39], s69 offen lds\n\t"
    "v_mfma_f32_16x16x32_bf16 v[44:47], a[76:79], v[200:203], v[44:47]\n\t"
    "ds_read_b64_tr_b16 a[120:121], v150 offset:8192\n\t"
    "ds_read_b64_tr_b16 a[122:123], v150 offset:9216\n\t"
    "v_mfma_f32_16x16x32_bf16 v[12:15], a[76:79], v[196:199], v[12:15]\n\t"
    "ds_read_b64_tr_b16 a[124:125], v151 offset:8192\n\t"
    "v_mfma_f32_16x16x32_bf16 v[16:19], a[80:83], v[196:199], v[16:19]\n\t"
    "s_waitcnt lgkmcnt(15)\n\t"
    "ds_read_b64_tr_b16 a[126:127], v151 offset:9216\n\t"
    "v_mfma_f32_16x16x32_bf16 v[48:51], a[80:83], v[200:203], v[48:51]\n\t"
    "buffer_load_dwordx4 v135, s[36:39], s69 offen offset:1024 lds\n\t"
    "v_mfma_f32_16x16x32_bf16 v[80:83], a[80:83], v[204:207], v[80:83]\n\t"
    "s_add_u32 m0, s68, 0x14000\n\t"
    "v_mfma_f32_16x16x32_bf16 v[84:87], a[84:87], v[204:207], v[84:87]\n\t"
    "v_mfma_f32_16x16x32_bf16 v[52:55], a[84:87], v[200:203], v[52:55]\n\t"
    "s_waitcnt vmcnt(18)\n\t"
    "buffer_load_dwordx4 v136, s[36:39], s69 offen lds\n\t"
    "v_mfma_f32_16x16x32_bf16 v[20:23], a[84:87], v[196:199], v[20:23]\n\t"
    "ds_read_b128 v[220:223], v138 offset:24576\n\t"
    "ds_read_b128 v[232:235], v139 offset:24576\n\t"
    "v_mfma_f32_16x16x32_bf16 v[24:27], a[88:91], v[196:199], v[24:27]\n\t"
    "v_mfma_f32_16x16x32_bf16 v[56:59], a[88:91], v[200:203], v[56:59]\n\t"
    "v_mfma_f32_16x16x32_bf16 v[88:91], a[88:91], v[204:207], v[88:91]\n\t"
    "buffer_load_dwordx4 v137, s[36:39], s69 offen offset:1024 lds\n\t"
    "v_mfma_f32_16x16x32_bf16 v[92:95], a[92:95], v[204:207], v[92:95]\n\t"
    "s_add_u32 s69, s69, 0x80\n\t"
    "s_cmp_eq_u32 s69, 0x500\n\t"
    "s_cselect_b32 s69, 0, s69\n\t"
    "v_mfma_f32_16x16x32_bf16 v[60:63], a[92:95], v[200:203], v[60:63]\n\t"
    "v_mfma_f32_16x16x32_bf16 v[28:31], a[92:95], v[196:199], v[28:31]\n\t"
    "s_waitcnt vmcnt(18) lgkmcnt(2)\n\t"
    "s_barrier\n\t"
    "v_mfma_f32_16x16x32_bf16 v[0:3], a[96:99], v[208:211], v[0:3]\n\t"
    "s_add_u32 m0, s68, 0x22000\n\t"
    "ds_read_b64_tr_b16 a[64:65], v144 offset:16384\n\t"
    "v_mfma_f32_16x16x32_bf16 v[32:35], a[96:99], v[212:215], v[32:35]\n\t"
    "ds_read_b64_tr_b16 a[66:67], v144 offset:17408\n\t"
    "ds_read_b64_tr_b16 a[68:69], v145 offset:16384\n\t"
    "v_mfma_f32_16x16x32_bf16 v[64:67], a[96:99], v[216:219], v[64:67]\n\t"
    "buffer_load_dwordx4 v140, s[48:51], s67 offen lds\n\t"
    "v_mfma_f32_16x16x32_bf16 v[68:71], a[100:103], v[216:219], v[68:71]\n\t"
    "ds_read_b64_tr_b16 a[70:71], v145 offset:17408\n\t"
    "ds_read_b64_tr_b16 a[72:73], v146 offset:16384\n\t"
    "v_mfma_f32_16x16x32_bf16 v[36:39], a[100:103], v[212:215], v[36:39]\n\t"
    "ds_read_b64_tr_b16 a[74:75], v146 offset:17408\n\t"
    "ds_read_b64_tr_b16 a[76:77], v147 offset:16384\n\t"
    "v_mfma_f32_16x16x32_bf16 v[4:7], a[100:103], v[208:211], v[4:7]\n\t"
    "ds_read_b64_tr_b16 a[78:79], v147 offset:17408\n\t"
    "ds_read_b64_tr_b16 a[80:81], v148 offset:16384\n\t"
    "v_mfma_f32_16x16x32_bf16 v[8:11], a[104:107], v[208:211], v[8:11]\n\t"
    "ds_read_b64_tr_b16 a[82:83], v148 offset:17408\n\t"
    "v_mfma_f32_16x16x32_bf16 v[40:43], a[104:107], v[212:215], v[40:43]\n\t"
    "buffer_load_dwordx4 v141, s[48:51], s67 offen offset:1024 lds\n\t"
    "v_mfma_f32_16x16x32_bf16 v[72:75], a[104:107], v[216:219], v[72:75]\n\t"
    "s_add_u32 s67, s67, 0x40000\n\t"
    "ds_read_b64_tr_b16 a[84:85], v149 offset:16384\n\t"
    "ds_read_b64_tr_b16 a[86:87], v149 offset:17408\n\t"
    "v_mfma_f32_16x16x32_bf16 v[76:79], a[108:111], v[216:219], v[76:79]\n\t"
    "s_cmp_eq_u32 s67, 0x500000\n\t"
    "ds_read_b64_tr_b16 a[88:89], v150 offset:16384\n\t"
    "v_mfma_f32_16x16x32_bf16 v[44:47], a[108:111], v[212:215], v[44:47]\n\t"
    "s_cselect_b32 s67, 0, s67\n\t"
    "ds_read_b64_tr_b16 a[90:91], v150 offset:17408\n\t"
    "v_mfma_f32_16x16x32_bf16 v[12:15], a[108:111], v[208:211], v[12:15]\n\t"
    "s_add_u32 m0, s68, 0x16000\n\t"
    "ds_read_b64_tr_b16 a[92:93], v151 offset:16384\n\t"
    "v_mfma_f32_16x16x32_bf16 v[16:19], a[112:115], v[208:211], v[16:19]\n\t"
    "s_waitcnt lgkmcnt(15)\n\t"
    "ds_read_b64_tr_b16 a[94:95], v151 offset:17408\n\t"
    "v_mfma_f32_16x16x32_bf16 v[48:51], a[112:115], v[212:215], v[48:51]\n\t"
    "buffer_load_dwordx4 v132, s[36:39], s69 offen lds\n\t"
    "v_mfma_f32_16x16x32_bf16 v[80:83], a[112:115], v[216:219], v[80:83]\n\t"
    "v_mfma_f32_16x16x32_bf16 v[84:87], a[116:119], v[216:219], v[84:87]\n\t"
    "v_mfma_f32_16x16x32_bf16 v[52:55], a[116:119], v[212:215], v[52:55]\n\t"
    "s_waitcnt vmcnt(19)\n\t"
    "ds_read_b128 v[224:227], v138 offset:40960\n\t"
    "ds_read_b128 v[236:239], v139 offset:40960\n\t"
    "v_mfma_f32_16x16x32_bf16 v[20:23], a[116:119], v[208:211], v[20:23]\n\t"
    "v_mfma_f32_16x16x32_bf16 v[24:27], a[120:123], v[208:211], v[24:27]\n\t"
    "s_waitcnt vmcnt(17)\n\t"
    "buffer_load_dwordx4 v133, s[36:39], s69 offen offset:1024 lds\n\t"
    "v_mfma_f32_16x16x32_bf16 v[56:59], a[120:123], v[212:215], v[56:59]\n\t"
    "ds_read_b128 v[228:231], v138 offset:57344\n\t"
    "ds_read_b128 v[240:243], v139 offset:57344\n\t"
    "v_mfma_f32_16x16x32_bf16 v[88:91], a[120:123], v[216:219], v[88:91]\n\t"
    "v_mfma_f32_16x16x32_bf16 v[92:95], a[124:127], v[216:219], v[92:95]\n\t"
    "v_mfma_f32_16x16x32_bf16 v[60:63], a[124:127], v[212:215], v[60:63]\n\t"
    "v_mfma_f32_16x16x32_bf16 v[28:31], a[124:127], v[208:211], v[28:31]\n\t"
    "s_waitcnt vmcnt(12) lgkmcnt(4)\n\t"
    "s_barrier\n\t"
    "v_mfma_f32_16x16x32_bf16 v[0:3], a[64:67], v[220:223], v[0:3]\n\t"
    "s_waitcnt lgkmcnt(3)\n\t"
    "s_add_u32 m0, s68, 0x24000\n\t"
    "ds_read_b64_tr_b16 a[96:97], v144 offset:24576\n\t"
    "v_mfma_f32_16x16x32_bf16 v[32:35], a[64:67], v[224:227], v[32:35]\n\t"
    "s_waitcnt lgkmcnt(2)\n\t"
    "ds_read_b64_tr_b16 a[98:99], v144 offset:25600\n\t"
    "ds_read_b64_tr_b16 a[100:101], v145 offset:24576\n\t"
    "v_mfma_f32_16x16x32_bf16 v[64:67], a[64:67], v[228:231], v[64:67]\n\t"
    "buffer_load_dwordx4 v140, s[48:51], s67 offen lds\n\t"
    "v_mfma_f32_16x16x32_bf16 v[68:71], a[68:71], v[228:231], v[68:71]\n\t"
    "buffer_load_dwordx4 v141, s[48:51], s67 offen offset:1024 lds\n\t"
    "v_mfma_f32_16x16x32_bf16 v[36:39], a[68:71], v[224:227], v[36:39]\n\t"
    "s_add_u32 s67, s67, 0x40000\n\t"
    "s_add_u32 m0, s68, 0x1a000\n\t"
    "v_mfma_f32_16x16x32_bf16 v[4:7], a[68:71], v[220:223], v[4:7]\n\t"
    "buffer_load_dwordx4 v134, s[36:39], s69 offen lds\n\t"
    "v_mfma_f32_16x16x32_bf16 v[8:11], a[72:75], v[220:223], v[8:11]\n\t"
    "s_waitcnt lgkmcnt(3)\n\t"
    "ds_read_b64_tr_b16 a[102:103], v145 offset:25600\n\t"
    "ds_read_b64_tr_b16 a[104:105], v146 offset:24576\n\t"
    "v_mfma_f32_16x16x32_bf16 v[40:43], a[72:75], v[224:227], v[40:43]\n\t"
    "buffer_load_dwordx4 v135, s[36:39], s69 offen offset:1024 lds\n\t"
    "v_mfma_f32_16x16x32_bf16 v[72:75], a[72:75], v[228:231], v[72:75]\n\t"
    "s_add_u32 m0, s68, 0x1e000\n\t"
    "ds_read_b64_tr_b16 a[106:107], v146 offset:25600\n\t"
    "ds_read_b64_tr_b16 a[108:109], v147 offset:24576\n\t"
    "v_mfma_f32_16x16x32_bf16 v[76:79], a[76:79], v[228:231], v[76:79]\n\t"
    "buffer_load_dwordx4 v136, s[36:39], s69 offen lds\n\t"
    "v_mfma_f32_16x16x32_bf16 v[44:47], a[76:79], v[224:227], v[44:47]\n\t"
    "ds_read_b64_tr_b16 a[110:111], v147 offset:25600\n\t"
    "ds_read_b64_tr_b16 a[112:113], v148 offset:24576\n\t"
    "v_mfma_f32_16x16x32_bf16 v[12:15], a[76:79], v[220:223], v[12:15]\n\t"
    "buffer_load_dwordx4 v137, s[36:39], s69 offen offset:1024 lds\n\t"
    "v_mfma_f32_16x16x32_bf16 v[16:19], a[80:83], v[220:223], v[16:19]\n\t"
    "s_add_u32 s69, s69, 0x80\n\t"
    "s_cmp_eq_u32 s69, 0x500\n\t"
    "s_cselect_b32 s69, 0, s69\n\t"
    "v_mfma_f32_16x16x32_bf16 v[48:51], a[80:83], v[224:227], v[48:51]\n\t"
    "s_add_u32 m0, s68, 0x2000\n\t"
    "ds_read_b64_tr_b16 a[114:115], v148 offset:25600\n\t"
    "ds_read_b64_tr_b16 a[116:117], v149 offset:24576\n\t"
    "v_mfma_f32_16x16x32_bf16 v[80:83], a[80:83], v[228:231], v[80:83]\n\t"
    "s_mov_b32 s74, 0x4000\n\t"
    "buffer_load_dwordx4 v194, s[44:47], 0 offen nt lds\n\t"
    "v_mfma_f32_16x16x32_bf16 v[84:87], a[84:87], v[228:231], v[84:87]\n\t"
    "buffer_load_dwordx4 v195, s[44:47], 0 offen offset:1024 nt lds\n\t"
    "v_mfma_f32_16x16x32_bf16 v[52:55], a[84:87], v[224:227], v[52:55]\n\t"
    "s_add_u32 m0, s68, 0\n\t"
    "ds_read_b64_tr_b16 a[118:119], v149 offset:25600\n\t"
    "ds_read_b64_tr_b16 a[120:121], v150 offset:24576\n\t"
    "v_mfma_f32_16x16x32_bf16 v[20:23], a[84:87], v[220:223], v[20:23]\n\t"
    "s_waitcnt vmcnt(18)\n\t"
    "buffer_load_dwordx4 v192, s[40:43], s74 offen nt lds\n\t"
    "v_mfma_f32_16x16x32_bf16 v[24:27], a[88:91], v[220:223], v[24:27]\n\t"
    "ds_read_b64_tr_b16 a[122:123], v150 offset:25600\n\t"
    "ds_read_b64_tr_b16 a[124:125], v151 offset:24576\n\t"
    "v_mfma_f32_16x16x32_bf16 v[56:59], a[88:91], v[224:227], v[56:59]\n\t"
    "buffer_load_dwordx4 v193, s[40:43], s74 offen offset:1024 nt lds\n\t"
    "v_mfma_f32_16x16x32_bf16 v[88:91], a[88:91], v[228:231], v[88:91]\n\t"
    "s_add_u32 m0, s68, 0x6000\n\t"
    "ds_read_b64_tr_b16 a[126:127], v151 offset:25600\n\t"
    "ds_read_b128 v[196:199], v138\n\t"
    "v_mfma_f32_16x16x32_bf16 v[92:95], a[92:95], v[228:231], v[92:95]\n\t"
    "s_mov_b32 s74, 0x2000\n\t"
    "buffer_load_dwordx4 v194, s[44:47], s74 offen nt lds\n\t"
    "v_mfma_f32_16x16x32_bf16 v[60:63], a[92:95], v[224:227], v[60:63]\n\t"
    "ds_read_b128 v[208:211], v139\n\t"
    "buffer_load_dwordx4 v195, s[44:47], s74 offen offset:1024 nt lds\n\t"
    "v_mfma_f32_16x16x32_bf16 v[28:31], a[92:95], v[220:223], v[28:31]\n\t"
    "s_waitcnt vmcnt(20) lgkmcnt(2)\n\t"
    "s_barrier\n\t"
    "v_mfma_f32_16x16x32_bf16 v[0:3], a[96:99], v[232:235], v[0:3]\n\t"
    "s_add_u32 m0, s68, 0x26000\n\t"
    "ds_read_b64_tr_b16 a[64:65], v144\n\t"
    "v_mfma_f32_16x16x32_bf16 v[32:35], a[96:99], v[236:239], v[32:35]\n\t"
    "ds_read_b64_tr_b16 a[66:67], v144 offset:1024\n\t"
    "ds_read_b64_tr_b16 a[68:69], v145\n\t"
    "v_mfma_f32_16x16x32_bf16 v[64:67], a[96:99], v[240:243], v[64:67]\n\t"
    "buffer_load_dwordx4 v140, s[48:51], s67 offen lds\n\t"
    "v_mfma_f32_16x16x32_bf16 v[68:71], a[100:103], v[240:243], v[68:71]\n\t"
    "ds_read_b64_tr_b16 a[70:71], v145 offset:1024\n\t"
    "ds_read_b64_tr_b16 a[72:73], v146\n\t"
    "v_mfma_f32_16x16x32_bf16 v[36:39], a[100:103], v[236:239], v[36:39]\n\t"
    "ds_read_b64_tr_b16 a[74:75], v146 offset:1024\n\t"
    "ds_read_b64_tr_b16 a[76:77], v147\n\t"
    "v_mfma_f32_16x16x32_bf16 v[4:7], a[100:103], v[232:235], v[4:7]\n\t"
    "ds_read_b64_tr_b16 a[78:79], v147 offset:1024\n\t"
    "ds_read_b64_tr_b16 a[80:81], v148\n\t"
    "v_mfma_f32_16x16x32_bf16 v[8:11], a[104:107], v[232:235], v[8:11]\n\t"
    "ds_read_b64_tr_b16 a[82:83], v148 offset:1024\n\t"
    "v_mfma_f32_16x16x32_bf16 v[40:43], a[104:107], v[236:239], v[40:43]\n\t"
    "buffer_load_dwordx4 v141, s[48:51], s67 offen offset:1024 lds\n\t"
    "v_mfma_f32_16x16x32_bf16 v[72:75], a[104:107], v[240:243], v[72:75]\n\t"
    "s_add_u32 s67, s67, 0x40000\n\t"
    "ds_read_b64_tr_b16 a[84:85], v149\n\t"
    "ds_read_b64_tr_b16 a[86:87], v149 offset:1024\n\t"
    "v_mfma_f32_16x16x32_bf16 v[76:79], a[108:111], v[240:243], v[76:79]\n\t"
    "s_cmp_eq_u32 s67, 0x500000\n\t"
    "ds_read_b64_tr_b16 a[88:89], v150\n\t"
    "v_mfma_f32_16x16x32_bf16 v[44:47], a[108:111], v[236:239], v[44:47]\n\t"
    "s_cselect_b32 s67, 0, s67\n\t"
    "ds_read_b64_tr_b16 a[90:91], v150 offset:1024\n\t"
    "v_mfma_f32_16x16x32_bf16 v[12:15], a[108:111], v[232:235], v[12:15]\n\t"
    "s_add_u32 m0, s68, 0x10000\n\t"
    "ds_read_b64_tr_b16 a[92:93], v151\n\t"
    "v_mfma_f32_16x16x32_bf16 v[16:19], a[112:115], v[232:235], v[16:19]\n\t"
    "s_waitcnt lgkmcnt(15)\n\t"
    "ds_read_b64_tr_b16 a[94:95], v151 offset:1024\n\t"
    "v_mfma_f32_16x16x32_bf16 v[48:51], a[112:115], v[236:239], v[48:51]\n\t"
    "buffer_load_dwordx4 v132, s[36:39], s69 offen lds\n\t"
    "v_mfma_f32_16x16x32_bf16 v[80:83], a[112:115], v[240:243], v[80:83]\n\t"
    "v_mfma_f32_16x16x32_bf16 v[84:87], a[116:119], v[240:243], v[84:87]\n\t"
    "v_mfma_f32_16x16x32_bf16 v[52:55], a[116:119], v[236:239], v[52:55]\n\t"
    "s_waitcnt vmcnt(21)\n\t"
    "ds_read_b128 v[200:203], v138 offset:8192\n\t"
    "ds_read_b128 v[212:215], v139 offset:8192\n\t"
    "v_mfma_f32_16x16x32_bf16 v[20:23], a[116:119], v[232:235], v[20:23]\n\t"
    "v_mfma_f32_16x16x32_bf16 v[24:27], a[120:123], v[232:235], v[24:27]\n\t"
    "s_waitcnt vmcnt(19)\n\t"
    "buffer_load_dwordx4 v133, s[36:39], s69 offen offset:1024 lds\n\t"
    "v_mfma_f32_16x16x32_bf16 v[56:59], a[120:123], v[236:239], v[56:59]\n\t"
    "ds_read_b128 v[204:207], v138 offset:16384\n\t"
    "ds_read_b128 v[216:219], v139 offset:16384\n\t"
    "v_mfma_f32_16x16x32_bf16 v[88:91], a[120:123], v[240:243], v[88:91]\n\t"
    "v_mfma_f32_16x16x32_bf16 v[92:95], a[124:127], v[240:243], v[92:95]\n\t"
    "v_mfma_f32_16x16x32_bf16 v[60:63], a[124:127], v[236:239], v[60:63]\n\t"
    "v_mfma_f32_16x16x32_bf16 v[28:31], a[124:127], v[232:235], v[28:31]\n\t"
    "s_waitcnt vmcnt(18) lgkmcnt(4)\n\t"
    "s_barrier\n\t"
    "v_mfma_f32_16x16x32_bf16 v[0:3], a[64:67], v[196:199], v[0:3]\n\t"
    "s_waitcnt lgkmcnt(3)\n\t"
    "s_add_u32 m0, s68, 0x20000\n\t"
    "ds_read_b64_tr_b16 a[96:97], v144 offset:8192\n\t"
    "v_mfma_f32_16x16x32_bf16 v[32:35], a[64:67], v[200:203], v[32:35]\n\t"
    "s_waitcnt lgkmcnt(2)\n\t"
    "ds_read_b64_tr_b16 a[98:99], v144 offset:9216\n\t"
    "ds_read_b64_tr_b16 a[100:101], v145 offset:8192\n\t"
    "v_mfma_f32_16x16x32_bf16 v[64:67], a[64:67], v[204:207], v[64:67]\n\t"
    "buffer_load_dwordx4 v140, s[48:51], s67 offen lds\n\t"
    "v_mfma_f32_16x16x32_bf16 v[68:71], a[68:71], v[204:207], v[68:71]\n\t"
    "ds_read_b64_tr_b16 a[102:103], v145 offset:9216\n\t"
    "ds_read_b64_tr_b16 a[104:105], v146 offset:8192\n\t"
    "v_mfma_f32_16x16x32_bf16 v[36:39], a[68:71], v[200:203], v[36:39]\n\t"
    "ds_read_b64_tr_b16 a[106:107], v146 offset:9216\n\t"
    "ds_read_b64_tr_b16 a[108:109], v147 offset:8192\n\t"
    "v_mfma_f32_16x16x32_bf16 v[4:7], a[68:71], v[196:199], v[4:7]\n\t"
    "buffer_load_dwordx4 v141, s[48:51], s67 offen offset:1024 lds\n\t"
    "v_mfma_f32_16x16x32_bf16 v[8:11], a[72:75], v[196:199], v[8:11]\n\t"
    "s_add_u32 s67, s67, 0x40000\n\t"
    "ds_read_b64_tr_b16 a[110:111], v147 offset:9216\n\t"
    "ds_read_b64_tr_b16 a[112:113], v148 offset:8192\n\t"
    "v_mfma_f32_16x16x32_bf16 v[40:43], a[72:75], v[200:203], v[40:43]\n\t"
    "s_add_u32 m0, s68, 0x12000\n\t"
    "ds_read_b64_tr_b16 a[114:115], v148 offset:9216\n\t"
    "ds_read_b64_tr_b16 a[116:117], v149 offset:8192\n\t"
    "v_mfma_f32_16x16x32_bf16 v[72:75], a[72:75], v[204:207], v[72:75]\n\t"
    "ds_read_b64_tr_b16 a[118:119], v149 offset:9216\n\t"
    "v_mfma_f32_16x16x32_bf16 v[76:79], a[76:79], v[204:207], v[76:79]\n\t"
    "buffer_load_dwordx4 v134, s[36:39], s69 offen lds\n\t"
    "v_mfma_f32_16x16x32_bf16 v[44:47], a[76:79], v[200:203], v[44:47]\n\t"
    "ds_read_b64_tr_b16 a[120:121], v150 offset:8192\n\t"
    "ds_read_b64_tr_b16 a[122:123], v150 offset:9216\n\t"
    "v_mfma_f32_16x16x32_bf16 v[12:15], a[76:79], v[196:199], v[12:15]\n\t"
    "ds_read_b64_tr_b16 a[124:125], v151 offset:8192\n\t"
    "v_mfma_f32_16x16x32_bf16 v[16:19], a[80:83], v[196:199], v[16:19]\n\t"
    "s_waitcnt lgkmcnt(15)\n\t"
    "ds_read_b64_tr_b16 a[126:127], v151 offset:9216\n\t"
    "v_mfma_f32_16x16x32_bf16 v[48:51], a[80:83], v[200:203], v[48:51]\n\t"
    "buffer_load_dwordx4 v135, s[36:39], s69 offen offset:1024 lds\n\t"
    "v_mfma_f32_16x16x32_bf16 v[80:83], a[80:83], v[204:207], v[80:83]\n\t"
    "s_add_u32 m0, s68, 0x14000\n\t"
    "v_mfma_f32_16x16x32_bf16 v[84:87], a[84:87], v[204:207], v[84:87]\n\t"
    "v_mfma_f32_16x16x32_bf16 v[52:55], a[84:87], v[200:203], v[52:55]\n\t"
    "s_waitcnt vmcnt(20)\n\t"
    "buffer_load_dwordx4 v136, s[36:39], s69 offen lds\n\t"
    "v_mfma_f32_16x16x32_bf16 v[20:23], a[84:87], v[196:199], v[20:23]\n\t"
    "ds_read_b128 v[220:223], v138 offset:24576\n\t"
    "ds_read_b128 v[232:235], v139 offset:24576\n\t"
    "v_mfma_f32_16x16x32_bf16 v[24:27], a[88:91], v[196:199], v[24:27]\n\t"
    "v_mfma_f32_16x16x32_bf16 v[56:59], a[88:91], v[200:203], v[56:59]\n\t"
    "v_mfma_f32_16x16x32_bf16 v[88:91], a[88:91], v[204:207], v[88:91]\n\t"
    "buffer_load_dwordx4 v137, s[36:39], s69 offen offset:1024 lds\n\t"
    "v_mfma_f32_16x16x32_bf16 v[92:95], a[92:95], v[204:207], v[92:95]\n\t"
    "s_add_u32 s69, s69, 0x80\n\t"
    "s_cmp_eq_u32 s69, 0x500\n\t"
    "s_cselect_b32 s69, 0, s69\n\t"
    "v_mfma_f32_16x16x32_bf16 v[60:63], a[92:95], v[200:203], v[60:63]\n\t"
    "v_mfma_f32_16x16x32_bf16 v[28:31], a[92:95], v[196:199], v[28:31]\n\t"
    "s_cmp_lg_u32 s64, 0\n\t"
    "s_cbranch_scc1 Luq000%=\n\t"
    "s_call_b64 s[22:23], Lc10%=\n\t"
    "Luq000%=:\n\t"
    "s_cmp_lg_u32 s64, 1\n\t"
    "s_cbranch_scc1 Luq001%=\n\t"
    "s_call_b64 s[22:23], Lc11%=\n\t"
    "Luq001%=:\n\t"
    "s_cmp_lg_u32 s64, 2\n\t"
    "s_cbranch_scc1 Luq002%=\n\t"
    "s_call_b64 s[22:23], Lc4%=\n\t"
    "Luq002%=:\n\t"
    "s_cmp_lg_u32 s64, 3\n\t"
    "s_cbranch_scc1 Luq003%=\n\t"
    "s_call_b64 s[22:23], Lc5%=\n\t"
    "Luq003%=:\n\t"
    "s_waitcnt vmcnt(20) lgkmcnt(2)\n\t"
    "s_barrier\n\t"
    "v_mfma_f32_16x16x32_bf16 v[0:3], a[96:99], v[208:211], v[0:3]\n\t"
    "s_add_u32 m0, s68, 0x22000\n\t"
    "ds_read_b64_tr_b16 a[64:65], v144 offset:16384\n\t"
    "v_mfma_f32_16x16x32_bf16 v[32:35], a[96:99], v[212:215], v[32:35]\n\t"
    "ds_read_b64_tr_b16 a[66:67], v144 offset:17408\n\t"
    "ds_read_b64_tr_b16 a[68:69], v145 offset:16384\n\t"
    "v_mfma_f32_16x16x32_bf16 v[64:67], a[96:99], v[216:219], v[64:67]\n\t"
    "buffer_load_dwordx4 v140, s[48:51], s67 offen lds\n\t"
    "v_mfma_f32_16x16x32_bf16 v[68:71], a[100:103], v[216:219], v[68:71]\n\t"
    "ds_read_b64_tr_b16 a[70:71], v145 offset:17408\n\t"
    "ds_read_b64_tr_b16 a[72:73], v146 offset:16384\n\t"
    "v_mfma_f32_16x16x32_bf16 v[36:39], a[100:103], v[212:215], v[36:39]\n\t"
    "ds_read_b64_tr_b16 a[74:75], v146 offset:17408\n\t"
    "ds_read_b64_tr_b16 a[76:77], v147 offset:16384\n\t"
    "v_mfma_f32_16x16x32_bf16 v[4:7], a[100:103], v[208:211], v[4:7]\n\t"
    "ds_read_b64_tr_b16 a[78:79], v147 offset:17408\n\t"
    "ds_read_b64_tr_b16 a[80:81], v148 offset:16384\n\t"
    "v_mfma_f32_16x16x32_bf16 v[8:11], a[104:107], v[208:211], v[8:11]\n\t"
    "ds_read_b64_tr_b16 a[82:83], v148 offset:17408\n\t"
    "v_mfma_f32_16x16x32_bf16 v[40:43], a[104:107], v[212:215], v[40:43]\n\t"
    "buffer_load_dwordx4 v141, s[48:51], s67 offen offset:1024 lds\n\t"
    "v_mfma_f32_16x16x32_bf16 v[72:75], a[104:107], v[216:219], v[72:75]\n\t"
    "s_add_u32 s67, s67, 0x40000\n\t"
    "ds_read_b64_tr_b16 a[84:85], v149 offset:16384\n\t"
    "ds_read_b64_tr_b16 a[86:87], v149 offset:17408\n\t"
    "v_mfma_f32_16x16x32_bf16 v[76:79], a[108:111], v[216:219], v[76:79]\n\t"
    "s_cmp_eq_u32 s67, 0x500000\n\t"
    "ds_read_b64_tr_b16 a[88:89], v150 offset:16384\n\t"
    "v_mfma_f32_16x16x32_bf16 v[44:47], a[108:111], v[212:215], v[44:47]\n\t"
    "s_cselect_b32 s67, 0, s67\n\t"
    "ds_read_b64_tr_b16 a[90:91], v150 offset:17408\n\t"
    "v_mfma_f32_16x16x32_bf16 v[12:15], a[108:111], v[208:211], v[12:15]\n\t"
    "s_add_u32 m0, s68, 0x16000\n\t"
    "ds_read_b64_tr_b16 a[92:93], v151 offset:16384\n\t"
    "v_mfma_f32_16x16x32_bf16 v[16:19], a[112:115], v[208:211], v[16:19]\n\t"
    "s_waitcnt lgkmcnt(15)\n\t"
    "ds_read_b64_tr_b16 a[94:95], v151 offset:17408\n\t"
    "v_mfma_f32_16x16x32_bf16 v[48:51], a[112:115], v[212:215], v[48:51]\n\t"
    "buffer_load_dwordx4 v132, s[36:39], s69 offen lds\n\t"
    "v_mfma_f32_16x16x32_bf16 v[80:83], a[112:115], v[216:219], v[80:83]\n\t"
    "v_mfma_f32_16x16x32_bf16 v[84:87], a[116:119], v[216:219], v[84:87]\n\t"
    "v_mfma_f32_16x16x32_bf16 v[52:55], a[116:119], v[212:215], v[52:55]\n\t"
    "s_waitcnt vmcnt(21)\n\t"
    "ds_read_b128 v[224:227], v138 offset:40960\n\t"
    "ds_read_b128 v[236:239], v139 offset:40960\n\t"
    "v_mfma_f32_16x16x32_bf16 v[20:23], a[116:119], v[208:211], v[20:23]\n\t"
    "v_mfma_f32_16x16x32_bf16 v[24:27], a[120:123], v[208:211], v[24:27]\n\t"
    "s_waitcnt vmcnt(19)\n\t"
    "buffer_load_dwordx4 v133, s[36:39], s69 offen offset:1024 lds\n\t"
    "v_mfma_f32_16x16x32_bf16 v[56:59], a[120:123], v[212:215], v[56:59]\n\t"
    "ds_read_b128 v[228:231], v138 offset:57344\n\t"
    "ds_read_b128 v[240:243], v139 offset:57344\n\t"
    "v_mfma_f32_16x16x32_bf16 v[88:91], a[120:123], v[216:219], v[88:91]\n\t"
    "v_mfma_f32_16x16x32_bf16 v[92:95], a[124:127], v[216:219], v[92:95]\n\t"
    "v_mfma_f32_16x16x32_bf16 v[60:63], a[124:127], v[212:215], v[60:63]\n\t"
    "v_mfma_f32_16x16x32_bf16 v[28:31], a[124:127], v[208:211], v[28:31]\n\t"
    "s_waitcnt vmcnt(12) lgkmcnt(4)\n\t"
    "s_barrier\n\t"
    "v_mfma_f32_16x16x32_bf16 v[0:3], a[64:67], v[220:223], v[0:3]\n\t"
    "s_waitcnt lgkmcnt(3)\n\t"
    "s_add_u32 m0, s68, 0x24000\n\t"
    "ds_read_b64_tr_b16 a[96:97], v144 offset:24576\n\t"
    "v_mfma_f32_16x16x32_bf16 v[32:35], a[64:67], v[224:227], v[32:35]\n\t"
    "s_waitcnt lgkmcnt(2)\n\t"
    "ds_read_b64_tr_b16 a[98:99], v144 offset:25600\n\t"
    "ds_read_b64_tr_b16 a[100:101], v145 offset:24576\n\t"
    "v_mfma_f32_16x16x32_bf16 v[64:67], a[64:67], v[228:231], v[64:67]\n\t"
    "buffer_load_dwordx4 v140, s[48:51], s67 offen lds\n\t"
    "v_mfma_f32_16x16x32_bf16 v[68:71], a[68:71], v[228:231], v[68:71]\n\t"
    "buffer_load_dwordx4 v141, s[48:51], s67 offen offset:1024 lds\n\t"
    "v_mfma_f32_16x16x32_bf16 v[36:39], a[68:71], v[224:227], v[36:39]\n\t"
    "s_add_u32 s67, s67, 0x40000\n\t"
    "s_add_u32 m0, s68, 0x1a000\n\t"
    "v_mfma_f32_16x16x32_bf16 v[4:7], a[68:71], v[220:223], v[4:7]\n\t"
    "buffer_load_dwordx4 v134, s[36:39], s69 offen lds\n\t"
    "v_mfma_f32_16x16x32_bf16 v[8:11], a[72:75], v[220:223], v[8:11]\n\t"
    "s_waitcnt lgkmcnt(3)\n\t"
    "ds_read_b64_tr_b16 a[102:103], v145 offset:25600\n\t"
    "ds_read_b64_tr_b16 a[104:105], v146 offset:24576\n\t"
    "v_mfma_f32_16x16x32_bf16 v[40:43], a[72:75], v[224:227], v[40:43]\n\t"
    "buffer_load_dwordx4 v135, s[36:39], s69 offen offset:1024 lds\n\t"
    "v_mfma_f32_16x16x32_bf16 v[72:75], a[72:75], v[228:231], v[72:75]\n\t"
    "s_add_u32 m0, s68, 0x1e000\n\t"
    "ds_read_b64_tr_b16 a[106:107], v146 offset:25600\n\t"
    "ds_read_b64_tr_b16 a[108:109], v147 offset:24576\n\t"
    "v_mfma_f32_16x16x32_bf16 v[76:79], a[76:79], v[228:231], v[76:79]\n\t"
    "buffer_load_dwordx4 v136, s[36:39], s69 offen lds\n\t"
    "v_mfma_f32_16x16x32_bf16 v[44:47], a[76:79], v[224:227], v[44:47]\n\t"
    "s_mov_b32 s74, 0x6000\n\t"
    "ds_read_b64_tr_b16 a[110:111], v147 offset:25600\n\t"
    "ds_read_b64_tr_b16 a[112:113], v148 offset:24576\n\t"
    "v_mfma_f32_16x16x32_bf16 v[12:15], a[76:79], v[220:223], v[12:15]\n\t"
    "buffer_load_dwordx4 v137, s[36:39], s69 offen offset:1024 lds\n\t"
    "v_mfma_f32_16x16x32_bf16 v[16:19], a[80:83], v[220:223], v[16:19]\n\t"
    "s_add_u32 s69, s69, 0x80\n\t"
    "s_cmp_eq_u32 s69, 0x500\n\t"
    "s_cselect_b32 s69, 0, s69\n\t"
    "v_mfma_f32_16x16x32_bf16 v[48:51], a[80:83], v[224:227], v[48:51]\n\t"
    "s_add_u32 m0, s68, 0x4000\n\t"
    "ds_read_b64_tr_b16 a[114:115], v148 offset:25600\n\t"
    "ds_read_b64_tr_b16 a[116:117], v149 offset:24576\n\t"
    "v_mfma_f32_16x16x32_bf16 v[80:83], a[80:83], v[228:231], v[80:83]\n\t"
    "buffer_load_dwordx4 v192, s[40:43], s74 offen nt lds\n\t"
    "v_mfma_f32_16x16x32_bf16 v[84:87], a[84:87], v[228:231], v[84:87]\n\t"
    "buffer_load_dwordx4 v193, s[40:43], s74 offen offset:1024 nt lds\n\t"
    "v_mfma_f32_16x16x32_bf16 v[52:55], a[84:87], v[224:227], v[52:55]\n\t"
    "s_add_u32 m0, s68, 0xa000\n\t"
    "ds_read_b64_tr_b16 a[118:119], v149 offset:25600\n\t"
    "ds_read_b64_tr_b16 a[120:121], v150 offset:24576\n\t"
    "v_mfma_f32_16x16x32_bf16 v[20:23], a[84:87], v[220:223], v[20:23]\n\t"
    "s_mov_b32 s74, 0x4000\n\t"
    "buffer_load_dwordx4 v194, s[44:47], s74 offen nt lds\n\t"
    "v_mfma_f32_16x16x32_bf16 v[24:27], a[88:91], v[220:223], v[24:27]\n\t"
    "s_waitcnt vmcnt(19)\n\t"
    "ds_read_b64_tr_b16 a[122:123], v150 offset:25600\n\t"
    "ds_read_b64_tr_b16 a[124:125], v151 offset:24576\n\t"
    "v_mfma_f32_16x16x32_bf16 v[56:59], a[88:91], v[224:227], v[56:59]\n\t"
    "buffer_load_dwordx4 v195, s[44:47], s74 offen offset:1024 nt lds\n\t"
    "v_mfma_f32_16x16x32_bf16 v[88:91], a[88:91], v[228:231], v[88:91]\n\t"
    "s_add_u32 m0, s68, 0x8000\n\t"
    "ds_read_b64_tr_b16 a[126:127], v151 offset:25600\n\t"
    "ds_read_b128 v[196:199], v138\n\t"
    "v_mfma_f32_16x16x32_bf16 v[92:95], a[92:95], v[228:231], v[92:95]\n\t"
    "s_mov_b32 s74, 0x8000\n\t"
    "buffer_load_dwordx4 v192, s[40:43], s74 offen nt lds\n\t"
    "v_mfma_f32_16x16x32_bf16 v[60:63], a[92:95], v[224:227], v[60:63]\n\t"
    "ds_read_b128 v[208:211], v139\n\t"
    "buffer_load_dwordx4 v193, s[40:43], s74 offen offset:1024 nt lds\n\t"
    "v_mfma_f32_16x16x32_bf16 v[28:31], a[92:95], v[220:223], v[28:31]\n\t"
    "s_waitcnt vmcnt(20) lgkmcnt(2)\n\t"
    "s_barrier\n\t"
    "v_mfma_f32_16x16x32_bf16 v[0:3], a[96:99], v[232:235], v[0:3]\n\t"
    "s_add_u32 m0, s68, 0x26000\n\t"
    "ds_read_b64_tr_b16 a[64:65], v144\n\t"
    "v_mfma_f32_16x16x32_bf16 v[32:35], a[96:99], v[236:239], v[32:35]\n\t"
    "ds_read_b64_tr_b16 a[66:67], v144 offset:1024\n\t"
    "ds_read_b64_tr_b16 a[68:69], v145\n\t"
    "v_mfma_f32_16x16x32_bf16 v[64:67], a[96:99], v[240:243], v[64:67]\n\t"
    "buffer_load_dwordx4 v140, s[48:51], s67 offen lds\n\t"
    "v_mfma_f32_16x16x32_bf16 v[68:71], a[100:103], v[240:243], v[68:71]\n\t"
    "ds_read_b64_tr_b16 a[70:71], v145 offset:1024\n\t"
    "ds_read_b64_tr_b16 a[72:73], v146\n\t"
    "v_mfma_f32_16x16x32_bf16 v[36:39], a[100:103], v[236:239], v[36:39]\n\t"
    "ds_read_b64_tr_b16 a[74:75], v146 offset:1024\n\t"
    "ds_read_b64_tr_b16 a[76:77], v147\n\t"
    "v_mfma_f32_16x16x32_bf16 v[4:7], a[100:103], v[232:235], v[4:7]\n\t"
    "ds_read_b64_tr_b16 a[78:79], v147 offset:1024\n\t"
    "ds_read_b64_tr_b16 a[80:81], v148\n\t"
    "v_mfma_f32_16x16x32_bf16 v[8:11], a[104:107], v[232:235], v[8:11]\n\t"
    "ds_read_b64_tr_b16 a[82:83], v148 offset:1024\n\t"
    "v_mfma_f32_16x16x32_bf16 v[40:43], a[104:107], v[236:239], v[40:43]\n\t"
    "buffer_load_dwordx4 v141, s[48:51], s67 offen offset:1024 lds\n\t"
    "v_mfma_f32_16x16x32_bf16 v[72:75], a[104:107], v[240:243], v[72:75]\n\t"
    "s_add_u32 s67, s67, 0x40000\n\t"
    "ds_read_b64_tr_b16 a[84:85], v149\n\t"
    "ds_read_b64_tr_b16 a[86:87], v149 offset:1024\n\t"
    "v_mfma_f32_16x16x32_bf16 v[76:79], a[108:111], v[240:243], v[76:79]\n\t"
    "s_cmp_eq_u32 s67, 0x500000\n\t"
    "ds_read_b64_tr_b16 a[88:89], v150\n\t"
    "v_mfma_f32_16x16x32_bf16 v[44:47], a[108:111], v[236:239], v[44:47]\n\t"
    "s_cselect_b32 s67, 0, s67\n\t"
    "ds_read_b64_tr_b16 a[90:91], v150 offset:1024\n\t"
    "v_mfma_f32_16x16x32_bf16 v[12:15], a[108:111], v[232:235], v[12:15]\n\t"
    "s_add_u32 m0, s68, 0x10000\n\t"
    "ds_read_b64_tr_b16 a[92:93], v151\n\t"
    "v_mfma_f32_16x16x32_bf16 v[16:19], a[112:115], v[232:235], v[16:19]\n\t"
    "s_waitcnt lgkmcnt(15)\n\t"
    "ds_read_b64_tr_b16 a[94:95], v151 offset:1024\n\t"
    "v_mfma_f32_16x16x32_bf16 v[48:51], a[112:115], v[236:239], v[48:51]\n\t"
    "buffer_load_dwordx4 v132, s[36:39], s69 offen lds\n\t"
    "v_mfma_f32_16x16x32_bf16 v[80:83], a[112:115], v[240:243], v[80:83]\n\t"
    "v_mfma_f32_16x16x32_bf16 v[84:87], a[116:119], v[240:243], v[84:87]\n\t"
    "v_mfma_f32_16x16x32_bf16 v[52:55], a[116:119], v[236:239], v[52:55]\n\t"
    "s_waitcnt vmcnt(21)\n\t"
    "ds_read_b128 v[200:203], v138 offset:8192\n\t"
    "ds_read_b128 v[212:215], v139 offset:8192\n\t"
    "v_mfma_f32_16x16x32_bf16 v[20:23], a[116:119], v[232:235], v[20:23]\n\t"
    "v_mfma_f32_16x16x32_bf16 v[24:27], a[120:123], v[232:235], v[24:27]\n\t"
    "s_waitcnt vmcnt(19)\n\t"
    "buffer_load_dwordx4 v133, s[36:39], s69 offen offset:1024 lds\n\t"
    "v_mfma_f32_16x16x32_bf16 v[56:59], a[120:123], v[236:239], v[56:59]\n\t"
    "ds_read_b128 v[204:207], v138 offset:16384\n\t"
    "ds_read_b128 v[216:219], v139 offset:16384\n\t"
    "v_mfma_f32_16x16x32_bf16 v[88:91], a[120:123], v[240:243], v[88:91]\n\t"
    "v_mfma_f32_16x16x32_bf16 v[92:95], a[124:127], v[240:243], v[92:95]\n\t"
    "v_mfma_f32_16x16x32_bf16 v[60:63], a[124:127], v[236:239], v[60:63]\n\t"
    "v_mfma_f32_16x16x32_bf16 v[28:31], a[124:127], v[232:235], v[28:31]\n\t"
    "s_waitcnt vmcnt(18) lgkmcnt(4)\n\t"
    "s_barrier\n\t"
    "v_mfma_f32_16x16x32_bf16 v[0:3], a[64:67], v[196:199], v[0:3]\n\t"
    "s_waitcnt lgkmcnt(3)\n\t"
    "s_add_u32 m0, s68, 0x20000\n\t"
    "ds_read_b64_tr_b16 a[96:97], v144 offset:8192\n\t"
    "v_mfma_f32_16x16x32_bf16 v[32:35], a[64:67], v[200:203], v[32:35]\n\t"
    "s_waitcnt lgkmcnt(2)\n\t"
    "ds_read_b64_tr_b16 a[98:99], v144 offset:9216\n\t"
    "ds_read_b64_tr_b16 a[100:101], v145 offset:8192\n\t"
    "v_mfma_f32_16x16x32_bf16 v[64:67], a[64:67], v[204:207], v[64:67]\n\t"
    "buffer_load_dwordx4 v140, s[48:51], s67 offen lds\n\t"
    "v_mfma_f32_16x16x32_bf16 v[68:71], a[68:71], v[204:207], v[68:71]\n\t"
    "ds_read_b64_tr_b16 a[102:103], v145 offset:9216\n\t"
    "ds_read_b64_tr_b16 a[104:105], v146 offset:8192\n\t"
    "v_mfma_f32_16x16x32_bf16 v[36:39], a[68:71], v[200:203], v[36:39]\n\t"
    "ds_read_b64_tr_b16 a[106:107], v146 offset:9216\n\t"
    "ds_read_b64_tr_b16 a[108:109], v147 offset:8192\n\t"
    "v_mfma_f32_16x16x32_bf16 v[4:7], a[68:71], v[196:199], v[4:7]\n\t"
    "buffer_load_dwordx4 v141, s[48:51], s67 offen offset:1024 lds\n\t"
    "v_mfma_f32_16x16x32_bf16 v[8:11], a[72:75], v[196:199], v[8:11]\n\t"
    "s_add_u32 s67, s67, 0x40000\n\t"
    "ds_read_b64_tr_b16 a[110:111], v147 offset:9216\n\t"
    "ds_read_b64_tr_b16 a[112:113], v148 offset:8192\n\t"
    "v_mfma_f32_16x16x32_bf16 v[40:43], a[72:75], v[200:203], v[40:43]\n\t"
    "s_add_u32 m0, s68, 0x12000\n\t"
    "ds_read_b64_tr_b16 a[114:115], v148 offset:9216\n\t"
    "ds_read_b64_tr_b16 a[116:117], v149 offset:8192\n\t"
    "v_mfma_f32_16x16x32_bf16 v[72:75], a[72:75], v[204:207], v[72:75]\n\t"
    "ds_read_b64_tr_b16 a[118:119], v149 offset:9216\n\t"
    "v_mfma_f32_16x16x32_bf16 v[76:79], a[76:79], v[204:207], v[76:79]\n\t"
    "buffer_load_dwordx4 v134, s[36:39], s69 offen lds\n\t"
    "v_mfma_f32_16x16x32_bf16 v[44:47], a[76:79], v[200:203], v[44:47]\n\t"
    "ds_read_b64_tr_b16 a[120:121], v150 offset:8192\n\t"
    "ds_read_b64_tr_b16 a[122:123], v150 offset:9216\n\t"
    "v_mfma_f32_16x16x32_bf16 v[12:15], a[76:79], v[196:199], v[12:15]\n\t"
    "ds_read_b64_tr_b16 a[124:125], v151 offset:8192\n\t"
    "v_mfma_f32_16x16x32_bf16 v[16:19], a[80:83], v[196:199], v[16:19]\n\t"
    "s_waitcnt lgkmcnt(15)\n\t"
    "ds_read_b64_tr_b16 a[126:127], v151 offset:9216\n\t"
    "v_mfma_f32_16x16x32_bf16 v[48:51], a[80:83], v[200:203], v[48:51]\n\t"
    "buffer_load_dwordx4 v135, s[36:39], s69 offen offset:1024 lds\n\t"
    "v_mfma_f32_16x16x32_bf16 v[80:83], a[80:83], v[204:207], v[80:83]\n\t"
    "s_add_u32 m0, s68, 0x14000\n\t"
    "v_mfma_f32_16x16x32_bf16 v[84:87], a[84:87], v[204:207], v[84:87]\n\t"
    "v_mfma_f32_16x16x32_bf16 v[52:55], a[84:87], v[200:203], v[52:55]\n\t"
    "s_waitcnt vmcnt(20)\n\t"
    "buffer_load_dwordx4 v136, s[36:39], s69 offen lds\n\t"
    "v_mfma_f32_16x16x32_bf16 v[20:23], a[84:87], v[196:199], v[20:23]\n\t"
    "ds_read_b128 v[220:223], v138 offset:24576\n\t"
    "ds_read_b128 v[232:235], v139 offset:24576\n\t"
    "v_mfma_f32_16x16x32_bf16 v[24:27], a[88:91], v[196:199], v[24:27]\n\t"
    "v_mfma_f32_16x16x32_bf16 v[56:59], a[88:91], v[200:203], v[56:59]\n\t"
    "v_mfma_f32_16x16x32_bf16 v[88:91], a[88:91], v[204:207], v[88:91]\n\t"
    "buffer_load_dwordx4 v137, s[36:39], s69 offen offset:1024 lds\n\t"
    "v_mfma_f32_16x16x32_bf16 v[92:95], a[92:95], v[204:207], v[92:95]\n\t"
    "s_add_u32 s69, s69, 0x80\n\t"
    "s_cmp_eq_u32 s69, 0x500\n\t"
    "s_cselect_b32 s69, 0, s69\n\t"
    "v_mfma_f32_16x16x32_bf16 v[60:63], a[92:95], v[200:203], v[60:63]\n\t"
    "v_mfma_f32_16x16x32_bf16 v[28:31], a[92:95], v[196:199], v[28:31]\n\t"
    "s_cmp_lg_u32 s64, 0\n\t"
    "s_cbranch_scc1 Luq010%=\n\t"
    "s_call_b64 s[22:23], Lc6%=\n\t"
    "Luq010%=:\n\t"
    "s_cmp_lg_u32 s64, 1\n\t"
    "s_cbranch_scc1 Luq011%=\n\t"
    "s_call_b64 s[22:23], Lc7%=\n\t"
    "Luq011%=:\n\t"
    "s_cmp_lg_u32 s64, 2\n\t"
    "s_cbranch_scc1 Luq012%=\n\t"
    "s_call_b64 s[22:23], Lc8%=\n\t"
    "Luq012%=:\n\t"
    "s_cmp_lg_u32 s64, 3\n\t"
    "s_cbranch_scc1 Luq013%=\n\t"
    "s_call_b64 s[22:23], Lc9%=\n\t"
    "Luq013%=:\n\t"
    "s_waitcnt vmcnt(20) lgkmcnt(2)\n\t"
    "s_barrier\n\t"
    "v_mfma_f32_16x16x32_bf16 v[0:3], a[96:99], v[208:211], v[0:3]\n\t"
    "s_add_u32 m0, s68, 0x22000\n\t"
    "ds_read_b64_tr_b16 a[64:65], v144 offset:16384\n\t"
    "v_mfma_f32_16x16x32_bf16 v[32:35], a[96:99], v[212:215], v[32:35]\n\t"
    "ds_read_b64_tr_b16 a[66:67], v144 offset:17408\n\t"
    "ds_read_b64_tr_b16 a[68:69], v145 offset:16384\n\t"
    "v_mfma_f32_16x16x32_bf16 v[64:67], a[96:99], v[216:219], v[64:67]\n\t"
    "buffer_load_dwordx4 v140, s[48:51], s67 offen lds\n\t"
    "v_mfma_f32_16x16x32_bf16 v[68:71], a[100:103], v[216:219], v[68:71]\n\t"
    "ds_read_b64_tr_b16 a[70:71], v145 offset:17408\n\t"
    "ds_read_b64_tr_b16 a[72:73], v146 offset:16384\n\t"
    "v_mfma_f32_16x16x32_bf16 v[36:39], a[100:103], v[212:215], v[36:39]\n\t"
    "ds_read_b64_tr_b16 a[74:75], v146 offset:17408\n\t"
    "ds_read_b64_tr_b16 a[76:77], v147 offset:16384\n\t"
    "v_mfma_f32_16x16x32_bf16 v[4:7], a[100:103], v[208:211], v[4:7]\n\t"
    "ds_read_b64_tr_b16 a[78:79], v147 offset:17408\n\t"
    "ds_read_b64_tr_b16 a[80:81], v148 offset:16384\n\t"
    "v_mfma_f32_16x16x32_bf16 v[8:11], a[104:107], v[208:211], v[8:11]\n\t"
    "ds_read_b64_tr_b16 a[82:83], v148 offset:17408\n\t"
    "v_mfma_f32_16x16x32_bf16 v[40:43], a[104:107], v[212:215], v[40:43]\n\t"
    "buffer_load_dwordx4 v141, s[48:51], s67 offen offset:1024 lds\n\t"
    "v_mfma_f32_16x16x32_bf16 v[72:75], a[104:107], v[216:219], v[72:75]\n\t"
    "s_add_u32 s67, s67, 0x40000\n\t"
    "ds_read_b64_tr_b16 a[84:85], v149 offset:16384\n\t"
    "ds_read_b64_tr_b16 a[86:87], v149 offset:17408\n\t"
    "v_mfma_f32_16x16x32_bf16 v[76:79], a[108:111], v[216:219], v[76:79]\n\t"
    "s_cmp_eq_u32 s67, 0x500000\n\t"
    "ds_read_b64_tr_b16 a[88:89], v150 offset:16384\n\t"
    "v_mfma_f32_16x16x32_bf16 v[44:47], a[108:111], v[212:215], v[44:47]\n\t"
    "s_cselect_b32 s67, 0, s67\n\t"
    "ds_read_b64_tr_b16 a[90:91], v150 offset:17408\n\t"
    "v_mfma_f32_16x16x32_bf16 v[12:15], a[108:111], v[208:211], v[12:15]\n\t"
    "s_add_u32 m0, s68, 0x16000\n\t"
    "ds_read_b64_tr_b16 a[92:93], v151 offset:16384\n\t"
    "v_mfma_f32_16x16x32_bf16 v[16:19], a[112:115], v[208:211], v[16:19]\n\t"
    "s_waitcnt lgkmcnt(15)\n\t"
    "ds_read_b64_tr_b16 a[94:95], v151 offset:17408\n\t"
    "v_mfma_f32_16x16x32_bf16 v[48:51], a[112:115], v[212:215], v[48:51]\n\t"
    "buffer_load_dwordx4 v132, s[36:39], s69 offen lds\n\t"
    "v_mfma_f32_16x16x32_bf16 v[80:83], a[112:115], v[216:219], v[80:83]\n\t"
    "v_mfma_f32_16x16x32_bf16 v[84:87], a[116:119], v[216:219], v[84:87]\n\t"
    "v_mfma_f32_16x16x32_bf16 v[52:55], a[116:119], v[212:215], v[52:55]\n\t"
    "s_waitcnt vmcnt(21)\n\t"
    "ds_read_b128 v[224:227], v138 offset:40960\n\t"
    "ds_read_b128 v[236:239], v139 offset:40960\n\t"
    "v_mfma_f32_16x16x32_bf16 v[20:23], a[116:119], v[208:211], v[20:23]\n\t"
    "v_mfma_f32_16x16x32_bf16 v[24:27], a[120:123], v[208:211], v[24:27]\n\t"
    "s_waitcnt vmcnt(19)\n\t"
    "buffer_load_dwordx4 v133, s[36:39], s69 offen offset:1024 lds\n\t"
    "v_mfma_f32_16x16x32_bf16 v[56:59], a[120:123], v[212:215], v[56:59]\n\t"
    "ds_read_b128 v[228:231], v138 offset:57344\n\t"
    "ds_read_b128 v[240:243], v139 offset:57344\n\t"
    "v_mfma_f32_16x16x32_bf16 v[88:91], a[120:123], v[216:219], v[88:91]\n\t"
    "v_mfma_f32_16x16x32_bf16 v[92:95], a[124:127], v[216:219], v[92:95]\n\t"
    "v_mfma_f32_16x16x32_bf16 v[60:63], a[124:127], v[212:215], v[60:63]\n\t"
    "v_mfma_f32_16x16x32_bf16 v[28:31], a[124:127], v[208:211], v[28:31]\n\t"
    "s_waitcnt vmcnt(12) lgkmcnt(4)\n\t"
    "s_barrier\n\t"
    "v_mfma_f32_16x16x32_bf16 v[0:3], a[64:67], v[220:223], v[0:3]\n\t"
    "s_waitcnt lgkmcnt(3)\n\t"
    "s_add_u32 m0, s68, 0x24000\n\t"
    "ds_read_b64_tr_b16 a[96:97], v144 offset:24576\n\t"
    "v_mfma_f32_16x16x32_bf16 v[32:35], a[64:67], v[224:227], v[32:35]\n\t"
    "s_waitcnt lgkmcnt(2)\n\t"
    "ds_read_b64_tr_b16 a[98:99], v144 offset:25600\n\t"
    "ds_read_b64_tr_b16 a[100:101], v145 offset:24576\n\t"
    "v_mfma_f32_16x16x32_bf16 v[64:67], a[64:67], v[228:231], v[64:67]\n\t"
    "buffer_load_dwordx4 v140, s[48:51], s67 offen lds\n\t"
    "v_mfma_f32_16x16x32_bf16 v[68:71], a[68:71], v[228:231], v[68:71]\n\t"
    "ds_read_b64_tr_b16 a[102:103], v145 offset:25600\n\t"
    "ds_read_b64_tr_b16 a[104:105], v146 offset:24576\n\t"
    "v_mfma_f32_16x16x32_bf16 v[36:39], a[68:71], v[224:227], v[36:39]\n\t"
    "buffer_load_dwordx4 v141, s[48:51], s67 offen offset:1024 lds\n\t"
    "v_mfma_f32_16x16x32_bf16 v[4:7], a[68:71], v[220:223], v[4:7]\n\t"
    "s_add_u32 s67, s67, 0x40000\n\t"
    "s_add_u32 m0, s68, 0x1a000\n\t"
    "ds_read_b64_tr_b16 a[106:107], v146 offset:25600\n\t"
    "v_mfma_f32_16x16x32_bf16 v[8:11], a[72:75], v[220:223], v[8:11]\n\t"
    "s_waitcnt lgkmcnt(6)\n\t"
    "buffer_load_dwordx4 v134, s[36:39], s69 offen lds\n\t"
    "v_mfma_f32_16x16x32_bf16 v[40:43], a[72:75], v[224:227], v[40:43]\n\t"
    "ds_read_b64_tr_b16 a[108:109], v147 offset:24576\n\t"
    "ds_read_b64_tr_b16 a[110:111], v147 offset:25600\n\t"
    "v_mfma_f32_16x16x32_bf16 v[72:75], a[72:75], v[228:231], v[72:75]\n\t"
    "buffer_load_dwordx4 v135, s[36:39], s69 offen offset:1024 lds\n\t"
    "v_mfma_f32_16x16x32_bf16 v[76:79], a[76:79], v[228:231], v[76:79]\n\t"
    "s_add_u32 m0, s68, 0x1e000\n\t"
    "ds_read_b64_tr_b16 a[112:113], v148 offset:24576\n\t"
    "ds_read_b64_tr_b16 a[114:115], v148 offset:25600\n\t"
    "v_mfma_f32_16x16x32_bf16 v[44:47], a[76:79], v[224:227], v[44:47]\n\t"
    "buffer_load_dwordx4 v136, s[36:39], s69 offen lds\n\t"
    "v_mfma_f32_16x16x32_bf16 v[12:15], a[76:79], v[220:223], v[12:15]\n\t"
    "s_mov_b32 s74, 0x6000\n\t"
    "ds_read_b64_tr_b16 a[116:117], v149 offset:24576\n\t"
    "ds_read_b64_tr_b16 a[118:119], v149 offset:25600\n\t"
    "v_mfma_f32_16x16x32_bf16 v[16:19], a[80:83], v[220:223], v[16:19]\n\t"
    "ds_read_b64_tr_b16 a[120:121], v150 offset:24576\n\t"
    "ds_read_b64_tr_b16 a[122:123], v150 offset:25600\n\t"
    "v_mfma_f32_16x16x32_bf16 v[48:51], a[80:83], v[224:227], v[48:51]\n\t"
    "buffer_load_dwordx4 v137, s[36:39], s69 offen offset:1024 lds\n\t"
    "v_mfma_f32_16x16x32_bf16 v[80:83], a[80:83], v[228:231], v[80:83]\n\t"
    "s_add_u32 m0, s68, 0xe000\n\t"
    "ds_read_b64_tr_b16 a[124:125], v151 offset:24576\n\t"
    "ds_read_b64_tr_b16 a[126:127], v151 offset:25600\n\t"
    "v_mfma_f32_16x16x32_bf16 v[84:87], a[84:87], v[228:231], v[84:87]\n\t"
    "buffer_load_dwordx4 v194, s[44:47], s74 offen nt lds\n\t"
    "v_mfma_f32_16x16x32_bf16 v[52:55], a[84:87], v[224:227], v[52:55]\n\t"
    "s_waitcnt vmcnt(17)\n\t"
    "ds_read_b128 v[196:199], v138\n\t"
    "ds_read_b128 v[208:211], v139\n\t"
    "v_mfma_f32_16x16x32_bf16 v[20:23], a[84:87], v[220:223], v[20:23]\n\t"
    "buffer_load_dwordx4 v195, s[44:47], s74 offen offset:1024 nt lds\n\t"
    "v_mfma_f32_16x16x32_bf16 v[24:27], a[88:91], v[220:223], v[24:27]\n\t"
    "s_add_u32 m0, s68, 0xc000\n\t"
    "s_mov_b32 s74, 0xa000\n\t"
    "v_mfma_f32_16x16x32_bf16 v[56:59], a[88:91], v[224:227], v[56:59]\n\t"
    "buffer_load_dwordx4 v192, s[40:43], s74 offen nt lds\n\t"
    "v_mfma_f32_16x16x32_bf16 v[88:91], a[88:91], v[228:231], v[88:91]\n\t"
    "v_mfma_f32_16x16x32_bf16 v[92:95], a[92:95], v[228:231], v[92:95]\n\t"
    "buffer_load_dwordx4 v193, s[40:43], s74 offen offset:1024 nt lds\n\t"
    "v_mfma_f32_16x16x32_bf16 v[60:63], a[92:95], v[224:227], v[60:63]\n\t"
    "v_mfma_f32_16x16x32_bf16 v[28:31], a[92:95], v[220:223], v[28:31]\n\t"
    "s_waitcnt vmcnt(18) lgkmcnt(2)\n\t"
    "s_barrier\n\t"
    "v_mfma_f32_16x16x32_bf16 v[0:3], a[96:99], v[232:235], v[0:3]\n\t"
    "s_add_u32 m0, s68, 0x26000\n\t"
    "ds_read_b64_tr_b16 a[64:65], v144\n\t"
    "v_mfma_f32_16x16x32_bf16 v[32:35], a[96:99], v[236:239], v[32:35]\n\t"
    "ds_read_b64_tr_b16 a[66:67], v144 offset:1024\n\t"
    "ds_read_b64_tr_b16 a[68:69], v145\n\t"
    "v_mfma_f32_16x16x32_bf16 v[64:67], a[96:99], v[240:243], v[64:67]\n\t"
    "buffer_load_dwordx4 v140, s[48:51], s67 offen lds\n\t"
    "v_mfma_f32_16x16x32_bf16 v[68:71], a[100:103], v[240:243], v[68:71]\n\t"
    "ds_read_b64_tr_b16 a[70:71], v145 offset:1024\n\t"
    "ds_read_b64_tr_b16 a[72:73], v146\n\t"
    "v_mfma_f32_16x16x32_bf16 v[36:39], a[100:103], v[236:239], v[36:39]\n\t"
    "ds_read_b64_tr_b16 a[74:75], v146 offset:1024\n\t"
    "ds_read_b64_tr_b16 a[76:77], v147\n\t"
    "v_mfma_f32_16x16x32_bf16 v[4:7], a[100:103], v[232:235], v[4:7]\n\t"
    "ds_read_b64_tr_b16 a[78:79], v147 offset:1024\n\t"
    "ds_read_b64_tr_b16 a[80:81], v148\n\t"
    "v_mfma_f32_16x16x32_bf16 v[8:11], a[104:107], v[232:235], v[8:11]\n\t"
    "ds_read_b64_tr_b16 a[82:83], v148 offset:1024\n\t"
    "v_mfma_f32_16x16x32_bf16 v[40:43], a[104:107], v[236:239], v[40:43]\n\t"
    "buffer_load_dwordx4 v141, s[48:51], s67 offen offset:1024 lds\n\t"
    "v_mfma_f32_16x16x32_bf16 v[72:75], a[104:107], v[240:243], v[72:75]\n\t"
    "ds_read_b64_tr_b16 a[84:85], v149\n\t"
    "ds_read_b64_tr_b16 a[86:87], v149 offset:1024\n\t"
    "v_mfma_f32_16x16x32_bf16 v[76:79], a[108:111], v[240:243], v[76:79]\n\t"
    "s_add_u32 m0, s68, 0x10000\n\t"
    "ds_read_b64_tr_b16 a[88:89], v150\n\t"
    "v_mfma_f32_16x16x32_bf16 v[44:47], a[108:111], v[236:239], v[44:47]\n\t"
    "s_waitcnt lgkmcnt(13)\n\t"
    "ds_read_b64_tr_b16 a[90:91], v150 offset:1024\n\t"
    "v_mfma_f32_16x16x32_bf16 v[12:15], a[108:111], v[232:235], v[12:15]\n\t"
    "s_mov_b32 s74, 0xc000\n\t"
    "ds_read_b64_tr_b16 a[92:93], v151\n\t"
    "v_mfma_f32_16x16x32_bf16 v[16:19], a[112:115], v[232:235], v[16:19]\n\t"
    "ds_read_b64_tr_b16 a[94:95], v151 offset:1024\n\t"
    "v_mfma_f32_16x16x32_bf16 v[48:51], a[112:115], v[236:239], v[48:51]\n\t"
    "buffer_load_dwordx4 v192, s[40:43], s74 offen nt lds\n\t"
    "v_mfma_f32_16x16x32_bf16 v[80:83], a[112:115], v[240:243], v[80:83]\n\t"
    "v_mfma_f32_16x16x32_bf16 v[84:87], a[116:119], v[240:243], v[84:87]\n\t"
    "v_mfma_f32_16x16x32_bf16 v[52:55], a[116:119], v[236:239], v[52:55]\n\t"
    "s_waitcnt vmcnt(19)\n\t"
    "ds_read_b128 v[200:203], v138 offset:8192\n\t"
    "ds_read_b128 v[212:215], v139 offset:8192\n\t"
    "v_mfma_f32_16x16x32_bf16 v[20:23], a[116:119], v[232:235], v[20:23]\n\t"
    "v_mfma_f32_16x16x32_bf16 v[24:27], a[120:123], v[232:235], v[24:27]\n\t"
    "s_waitcnt vmcnt(17)\n\t"
    "buffer_load_dwordx4 v193, s[40:43], s74 offen offset:1024 nt lds\n\t"
    "v_mfma_f32_16x16x32_bf16 v[56:59], a[120:123], v[236:239], v[56:59]\n\t"
    "ds_read_b128 v[204:207], v138 offset:16384\n\t"
    "ds_read_b128 v[216:219], v139 offset:16384\n\t"
    "v_mfma_f32_16x16x32_bf16 v[88:91], a[120:123], v[240:243], v[88:91]\n\t"
    "v_mfma_f32_16x16x32_bf16 v[92:95], a[124:127], v[240:243], v[92:95]\n\t"
    "v_mfma_f32_16x16x32_bf16 v[60:63], a[124:127], v[236:239], v[60:63]\n\t"
    "v_mfma_f32_16x16x32_bf16 v[28:31], a[124:127], v[232:235], v[28:31]\n\t"
    "s_waitcnt vmcnt(16) lgkmcnt(4)\n\t"
    "s_barrier\n\t"
    "v_mfma_f32_16x16x32_bf16 v[0:3], a[64:67], v[196:199], v[0:3]\n\t"
    "s_waitcnt lgkmcnt(3)\n\t"
    "s_add_u32 m0, s68, 0x12000\n\t"
    "ds_read_b64_tr_b16 a[96:97], v144 offset:8192\n\t"
    "v_mfma_f32_16x16x32_bf16 v[32:35], a[64:67], v[200:203], v[32:35]\n\t"
    "s_waitcnt lgkmcnt(2)\n\t"
    "s_mov_b32 s74, 0x8000\n\t"
    "v_mfma_f32_16x16x32_bf16 v[64:67], a[64:67], v[204:207], v[64:67]\n\t"
    "buffer_load_dwordx4 v194, s[44:47], s74 offen nt lds\n\t"
    "v_mfma_f32_16x16x32_bf16 v[68:71], a[68:71], v[204:207], v[68:71]\n\t"
    "ds_read_b64_tr_b16 a[98:99], v144 offset:9216\n\t"
    "ds_read_b64_tr_b16 a[100:101], v145 offset:8192\n\t"
    "v_mfma_f32_16x16x32_bf16 v[36:39], a[68:71], v[200:203], v[36:39]\n\t"
    "ds_read_b64_tr_b16 a[102:103], v145 offset:9216\n\t"
    "ds_read_b64_tr_b16 a[104:105], v146 offset:8192\n\t"
    "v_mfma_f32_16x16x32_bf16 v[4:7], a[68:71], v[196:199], v[4:7]\n\t"
    "ds_read_b64_tr_b16 a[106:107], v146 offset:9216\n\t"
    "ds_read_b64_tr_b16 a[108:109], v147 offset:8192\n\t"
    "v_mfma_f32_16x16x32_bf16 v[8:11], a[72:75], v[196:199], v[8:11]\n\t"
    "ds_read_b64_tr_b16 a[110:111], v147 offset:9216\n\t"
    "ds_read_b64_tr_b16 a[112:113], v148 offset:8192\n\t"
    "v_mfma_f32_16x16x32_bf16 v[40:43], a[72:75], v[200:203], v[40:43]\n\t"
    "buffer_load_dwordx4 v195, s[44:47], s74 offen offset:1024 nt lds\n\t"
    "v_mfma_f32_16x16x32_bf16 v[72:75], a[72:75], v[204:207], v[72:75]\n\t"
    "ds_read_b64_tr_b16 a[114:115], v148 offset:9216\n\t"
    "ds_read_b64_tr_b16 a[116:117], v149 offset:8192\n\t"
    "v_mfma_f32_16x16x32_bf16 v[76:79], a[76:79], v[204:207], v[76:79]\n\t"
    "s_add_u32 m0, s68, 0x14000\n\t"
    "ds_read_b64_tr_b16 a[118:119], v149 offset:9216\n\t"
    "ds_read_b64_tr_b16 a[120:121], v150 offset:8192\n\t"
    "v_mfma_f32_16x16x32_bf16 v[44:47], a[76:79], v[200:203], v[44:47]\n\t"
    "s_waitcnt lgkmcnt(13)\n\t"
    "ds_read_b64_tr_b16 a[122:123], v150 offset:9216\n\t"
    "v_mfma_f32_16x16x32_bf16 v[12:15], a[76:79], v[196:199], v[12:15]\n\t"
    "s_mov_b32 s74, 0xe000\n\t"
    "ds_read_b64_tr_b16 a[124:125], v151 offset:8192\n\t"
    "v_mfma_f32_16x16x32_bf16 v[16:19], a[80:83], v[196:199], v[16:19]\n\t"
    "ds_read_b64_tr_b16 a[126:127], v151 offset:9216\n\t"
    "v_mfma_f32_16x16x32_bf16 v[48:51], a[80:83], v[200:203], v[48:51]\n\t"
    "buffer_load_dwordx4 v192, s[40:43], s74 offen nt lds\n\t"
    "v_mfma_f32_16x16x32_bf16 v[80:83], a[80:83], v[204:207], v[80:83]\n\t"
    "v_mfma_f32_16x16x32_bf16 v[84:87], a[84:87], v[204:207], v[84:87]\n\t"
    "v_mfma_f32_16x16x32_bf16 v[52:55], a[84:87], v[200:203], v[52:55]\n\t"
    "s_waitcnt vmcnt(17)\n\t"
    "ds_read_b128 v[220:223], v138 offset:24576\n\t"
    "ds_read_b128 v[232:235], v139 offset:24576\n\t"
    "v_mfma_f32_16x16x32_bf16 v[20:23], a[84:87], v[196:199], v[20:23]\n\t"
    "s_memtime s[20:21]\n\t"
    "v_mfma_f32_16x16x32_bf16 v[24:27], a[88:91], v[196:199], v[24:27]\n\t"
    "s_memrealtime s[70:71]\n\t"
    "buffer_load_dwordx4 v193, s[40:43], s74 offen offset:1024 nt lds\n\t"
    "v_mfma_f32_16x16x32_bf16 v[56:59], a[88:91], v[200:203], v[56:59]\n\t"
    "v_mfma_f32_16x16x32_bf16 v[88:91], a[88:91], v[204:207], v[88:91]\n\t"
    "v_mfma_f32_16x16x32_bf16 v[92:95], a[92:95], v[204:207], v[92:95]\n\t"
    "v_mfma_f32_16x16x32_bf16 v[60:63], a[92:95], v[200:203], v[60:63]\n\t"
    "v_mfma_f32_16x16x32_bf16 v[28:31], a[92:95], v[196:199], v[28:31]\n\t"
    "s_waitcnt vmcnt(16) lgkmcnt(0)\n\t"
    "s_barrier\n\t"
    "v_mfma_f32_16x16x32_bf16 v[0:3], a[96:99], v[208:211], v[0:3]\n\t"
    "s_add_u32 m0, s68, 0x16000\n\t"
    "ds_read_b64_tr_b16 a[64:65], v144 offset:16384\n\t"
    "v_mfma_f32_16x16x32_bf16 v[32:35], a[96:99], v[212:215], v[32:35]\n\t"
    "s_mov_b32 s74, 0xa000\n\t"
    "v_mfma_f32_16x16x32_bf16 v[64:67], a[96:99], v[216:219], v[64:67]\n\t"
    "buffer_load_dwordx4 v194, s[44:47], s74 offen nt lds\n\t"
    "v_mfma_f32_16x16x32_bf16 v[68:71], a[100:103], v[216:219], v[68:71]\n\t"
    "ds_read_b64_tr_b16 a[66:67], v144 offset:17408\n\t"
    "ds_read_b64_tr_b16 a[68:69], v145 offset:16384\n\t"
    "v_mfma_f32_16x16x32_bf16 v[36:39], a[100:103], v[212:215], v[36:39]\n\t"
    "ds_read_b64_tr_b16 a[70:71], v145 offset:17408\n\t"
    "ds_read_b64_tr_b16 a[72:73], v146 offset:16384\n\t"
    "v_mfma_f32_16x16x32_bf16 v[4:7], a[100:103], v[208:211], v[4:7]\n\t"
    "ds_read_b64_tr_b16 a[74:75], v146 offset:17408\n\t"
    "ds_read_b64_tr_b16 a[76:77], v147 offset:16384\n\t"
    "v_mfma_f32_16x16x32_bf16 v[8:11], a[104:107], v[208:211], v[8:11]\n\t"
    "ds_read_b64_tr_b16 a[78:79], v147 offset:17408\n\t"
    "ds_read_b64_tr_b16 a[80:81], v148 offset:16384\n\t"
    "v_mfma_f32_16x16x32_bf16 v[40:43], a[104:107], v[212:215], v[40:43]\n\t"
    "ds_read_b64_tr_b16 a[82:83], v148 offset:17408\n\t"
    "ds_read_b64_tr_b16 a[84:85], v149 offset:16384\n\t"
    "v_mfma_f32_16x16x32_bf16 v[72:75], a[104:107], v[216:219], v[72:75]\n\t"
    "ds_read_b64_tr_b16 a[86:87], v149 offset:17408\n\t"
    "v_mfma_f32_16x16x32_bf16 v[76:79], a[108:111], v[216:219], v[76:79]\n\t"
    "ds_read_b64_tr_b16 a[88:89], v150 offset:16384\n\t"
    "v_mfma_f32_16x16x32_bf16 v[44:47], a[108:111], v[212:215], v[44:47]\n\t"
    "ds_read_b64_tr_b16 a[90:91], v150 offset:17408\n\t"
    "v_mfma_f32_16x16x32_bf16 v[12:15], a[108:111], v[208:211], v[12:15]\n\t"
    "s_sub_u32 s20, s20, s18\n\t"
    "ds_read_b64_tr_b16 a[92:93], v151 offset:16384\n\t"
    "v_mfma_f32_16x16x32_bf16 v[16:19], a[112:115], v[208:211], v[16:19]\n\t"
    "s_sub_u32 s21, s70, s98\n\t"
    "ds_read_b64_tr_b16 a[94:95], v151 offset:17408\n\t"
    "v_mfma_f32_16x16x32_bf16 v[48:51], a[112:115], v[212:215], v[48:51]\n\t"
    "s_lshl_b32 s20, s20, 4\n\t"
    "buffer_load_dwordx4 v195, s[44:47], s74 offen offset:1024 nt lds\n\t"
    "v_mfma_f32_16x16x32_bf16 v[80:83], a[112:115], v[216:219], v[80:83]\n\t"
    "s_mul_i32 s21, s21, 0x113\n\t"
    "v_mfma_f32_16x16x32_bf16 v[84:87], a[116:119], v[216:219], v[84:87]\n\t"
    "s_cmp_lt_u32 s20, s21\n\t"
    "v_mfma_f32_16x16x32_bf16 v[52:55], a[116:119], v[212:215], v[52:55]\n\t"
    "s_waitcnt vmcnt(16)\n\t"
    "ds_read_b128 v[224:227], v138 offset:40960\n\t"
    "ds_read_b128 v[236:239], v139 offset:40960\n\t"
    "v_mfma_f32_16x16x32_bf16 v[20:23], a[116:119], v[208:211], v[20:23]\n\t"
    "s_cselect_b32 s19, 44, 57\n\t"
    "v_mfma_f32_16x16x32_bf16 v[24:27], a[120:123], v[208:211], v[24:27]\n\t"
    "s_waitcnt vmcnt(14)\n\t"
    "ds_read_b128 v[228:231], v138 offset:57344\n\t"
    "ds_read_b128 v[240:243], v139 offset:57344\n\t"
    "v_mfma_f32_16x16x32_bf16 v[56:59], a[120:123], v[212:215], v[56:59]\n\t"
    "s_cselect_b32 s20, 63, 0\n\t"
    "v_mfma_f32_16x16x32_bf16 v[88:91], a[120:123], v[216:219], v[88:91]\n\t"
    "v_mfma_f32_16x16x32_bf16 v[92:95], a[124:127], v[216:219], v[92:95]\n\t"
    "v_mfma_f32_16x16x32_bf16 v[60:63], a[124:127], v[212:215], v[60:63]\n\t"
    "v_mfma_f32_16x16x32_bf16 v[28:31], a[124:127], v[208:211], v[28:31]\n\t"
    "Lqt%=:\n\t"
    "s_waitcnt vmcnt(8) lgkmcnt(4)\n\t"
    "s_barrier\n\t"
    "v_mfma_f32_16x16x32_bf16 v[0:3], a[64:67], v[220:223], v[0:3]\n\t"
    "s_add_u32 m0, s68, 0x1a000\n\t"
    "ds_read_b64_tr_b16 a[96:97], v144 offset:24576\n\t"
    "v_mfma_f32_16x16x32_bf16 v[4:7], a[68:71], v[220:223], v[4:7]\n\t"
    "s_waitcnt lgkmcnt(3)\n\t"
    "s_mov_b32 s74, 0xc000\n\t"
    "ds_read_b64_tr_b16 a[98:99], v144 offset:25600\n\t"
    "v_mfma_f32_16x16x32_bf16 v[8:11], a[72:75], v[220:223], v[8:11]\n\t"
    "buffer_load_dwordx4 v194, s[44:47], s74 offen nt lds\n\t"
    "v_mfma_f32_16x16x32_bf16 v[12:15], a[76:79], v[220:223], v[12:15]\n\t"
    "ds_read_b64_tr_b16 a[100:101], v145 offset:24576\n\t"
    "v_mov_b64 v[96:97], 0\n\t"
    "v_mfma_f32_16x16x32_bf16 v[16:19], a[80:83], v[220:223], v[16:19]\n\t"
    "ds_read_b64_tr_b16 a[102:103], v145 offset:25600\n\t"
    "v_mov_b64 v[98:99], 0\n\t"
    "v_mfma_f32_16x16x32_bf16 v[20:23], a[84:87], v[220:223], v[20:23]\n\t"
    "s_waitcnt lgkmcnt(4)\n\t"
    "buffer_load_dwordx4 v195, s[44:47], s74 offen offset:1024 nt lds\n\t"
    "v_mfma_f32_16x16x32_bf16 v[24:27], a[88:91], v[220:223], v[24:27]\n\t"
    "ds_read_b64_tr_b16 a[104:105], v146 offset:24576\n\t"
    "ds_read_b64_tr_b16 a[106:107], v146 offset:25600\n\t"
    "v_mfma_f32_16x16x32_bf16 v[28:31], a[92:95], v[220:223], v[28:31]\n\t"
    "ds_read_b64_tr_b16 a[108:109], v147 offset:24576\n\t"
    "ds_read_b64_tr_b16 a[110:111], v147 offset:25600\n\t"
    "v_mfma_f32_16x16x32_bf16 v[32:35], a[64:67], v[224:227], v[32:35]\n\t"
    "s_mul_i32 s76, s66, 30\n\t"
    "ds_read_b64_tr_b16 a[112:113], v148 offset:24576\n\t"
    "ds_read_b64_tr_b16 a[114:115], v148 offset:25600\n\t"
    "v_mfma_f32_16x16x32_bf16 v[36:39], a[68:71], v[224:227], v[36:39]\n\t"
    "ds_read_b64_tr_b16 a[116:117], v149 offset:24576\n\t"
    "ds_read_b64_tr_b16 a[118:119], v149 offset:25600\n\t"
    "v_mfma_f32_16x16x32_bf16 v[40:43], a[72:75], v[224:227], v[40:43]\n\t"
    "ds_read_b64_tr_b16 a[120:121], v150 offset:24576\n\t"
    "v_add_u32 v247, s76, v244\n\t"
    "v_mfma_f32_16x16x32_bf16 v[44:47], a[76:79], v[224:227], v[44:47]\n\t"
    "ds_read_b64_tr_b16 a[122:123], v150 offset:25600\n\t"
    "ds_read_b64_tr_b16 a[124:125], v151 offset:24576\n\t"
    "v_mfma_f32_16x16x32_bf16 v[48:51], a[80:83], v[224:227], v[48:51]\n\t"
    "ds_read_b64_tr_b16 a[126:127], v151 offset:25600\n\t"
    "v_lshlrev_b32 v247, 9, v247\n\t"
    "v_mfma_f32_16x16x32_bf16 v[52:55], a[84:87], v[224:227], v[52:55]\n\t"
    "ds_read_b128 a[0:3], v172 offset:32768\n\t"
    "v_cmp_gt_u32 vcc, 30, v244\n\t"
    "v_mfma_f32_16x16x32_bf16 v[56:59], a[88:91], v[224:227], v[56:59]\n\t"
    "s_lshl_b32 s76, s33, 9\n\t"
    "ds_read_b128 a[4:7], v173 offset:32768\n\t"
    "v_cndmask_b32 v247, 2.0, v247, vcc\n\t"
    "v_mfma_f32_16x16x32_bf16 v[60:63], a[92:95], v[224:227], v[60:63]\n\t"
    "s_waitcnt lgkmcnt(15)\n\t"
    "ds_read_b128 a[8:11], v174 offset:32768\n\t"
    "v_mfma_f32_16x16x32_bf16 v[0:3], a[96:99], v[232:235], v[0:3]\n\t"
    "s_waitcnt lgkmcnt(15)\n\t"
    "ds_read_b128 a[12:15], v175 offset:32768\n\t"
    "v_mov_b32 v252, s76\n\t"
    "v_mfma_f32_16x16x32_bf16 v[4:7], a[100:103], v[232:235], v[4:7]\n\t"
    "s_waitcnt lgkmcnt(14)\n\t"
    "ds_read_b128 a[16:19], v172 offset:36864\n\t"
    "v_mfma_f32_16x16x32_bf16 v[8:11], a[104:107], v[232:235], v[8:11]\n\t"
    "s_waitcnt lgkmcnt(13)\n\t"
    "ds_read_b128 a[20:23], v173 offset:36864\n\t"
    "v_mfma_f32_16x16x32_bf16 v[12:15], a[108:111], v[232:235], v[12:15]\n\t"
    "s_waitcnt lgkmcnt(12)\n\t"
    "ds_read_b128 a[24:27], v174 offset:36864\n\t"
    "v_mfma_f32_16x16x32_bf16 v[16:19], a[112:115], v[232:235], v[16:19]\n\t"
    "s_waitcnt lgkmcnt(11)\n\t"
    "buffer_load_dwordx2 v[248:249], v247, s[24:27], 0 offen offset:256 sc1\n\t"
    "v_mfma_f32_16x16x32_bf16 v[20:23], a[116:119], v[232:235], v[20:23]\n\t"
    "s_waitcnt lgkmcnt(9)\n\t"
    "ds_read_b128 a[28:31], v175 offset:36864\n\t"
    "v_mfma_f32_16x16x32_bf16 v[24:27], a[120:123], v[232:235], v[24:27]\n\t"
    "s_waitcnt lgkmcnt(8)\n\t"
    "buffer_load_dwordx2 v[250:251], v252, s[24:27], 0 offen offset:256 sc1\n\t"
    "v_mfma_f32_16x16x32_bf16 v[28:31], a[124:127], v[232:235], v[28:31]\n\t"
    "s_mov_b32 vcc_lo, 0xffff0000\n\t"
    "s_mov_b32 vcc_hi, 0xffff0000\n\t"
    "v_pk_mul_f32 v[0:1], v[0:1], s[78:79]\n\t"
    "v_pk_mul_f32 v[2:3], v[2:3], s[78:79]\n\t"
    "v_pk_mul_f32 v[4:5], v[4:5], s[78:79]\n\t"
    "v_pk_mul_f32 v[6:7], v[6:7], s[78:79]\n\t"
    "v_pk_mul_f32 v[8:9], v[8:9], s[78:79]\n\t"
    "v_pk_mul_f32 v[10:11], v[10:11], s[78:79]\n\t"
    "v_pk_mul_f32 v[12:13], v[12:13], s[78:79]\n\t"
    "v_pk_mul_f32 v[14:15], v[14:15], s[78:79]\n\t"
    "v_pk_mul_f32 v[16:17], v[16:17], s[78:79]\n\t"
    "v_pk_mul_f32 v[18:19], v[18:19], s[78:79]\n\t"
    "v_pk_mul_f32 v[20:21], v[20:21], s[78:79]\n\t"
    "v_pk_mul_f32 v[22:23], v[22:23], s[78:79]\n\t"
    "v_pk_mul_f32 v[24:25], v[24:25], s[78:79]\n\t"
    "v_pk_mul_f32 v[26:27], v[26:27], s[78:79]\n\t"
    "v_pk_mul_f32 v[28:29], v[28:29], s[78:79]\n\t"
    "v_pk_mul_f32 v[30:31], v[30:31], s[78:79]\n\t"
    "v_mfma_f32_16x16x32_bf16 v[64:67], a[64:67], v[228:231], v[64:67]\n\t"
    "v_cvt_pk_bf16_f32 v0, v0, v1\n\t"
    "v_cvt_pk_bf16_f32 v1, v2, v3\n\t"
    "v_mfma_f32_16x16x32_bf16 v[68:71], a[68:71], v[228:231], v[68:71]\n\t"
    "v_cvt_pk_bf16_f32 v4, v4, v5\n\t"
    "v_cvt_pk_bf16_f32 v5, v6, v7\n\t"
    "v_mfma_f32_16x16x32_bf16 v[72:75], a[72:75], v[228:231], v[72:75]\n\t"
    "v_cndmask_b32 v196, v0, v4, vcc\n\t"
    "v_cndmask_b32 v197, v1, v5, vcc\n\t"
    "v_mfma_f32_16x16x32_bf16 v[76:79], a[76:79], v[228:231], v[76:79]\n\t"
    "v_cndmask_b32 v198, v4, v0, vcc\n\t"
    "v_cndmask_b32 v199, v5, v1, vcc\n\t"
    "v_mfma_f32_16x16x32_bf16 v[80:83], a[80:83], v[228:231], v[80:83]\n\t"
    "v_cvt_pk_bf16_f32 v8, v8, v9\n\t"
    "v_cvt_pk_bf16_f32 v9, v10, v11\n\t"
    "v_mfma_f32_16x16x32_bf16 v[84:87], a[84:87], v[228:231], v[84:87]\n\t"
    "v_cvt_pk_bf16_f32 v12, v12, v13\n\t"
    "v_cvt_pk_bf16_f32 v13, v14, v15\n\t"
    "v_mfma_f32_16x16x32_bf16 v[88:91], a[88:91], v[228:231], v[88:91]\n\t"
    "v_cndmask_b32 v200, v8, v12, vcc\n\t"
    "v_cndmask_b32 v201, v9, v13, vcc\n\t"
    "v_mfma_f32_16x16x32_bf16 v[92:95], a[92:95], v[228:231], v[92:95]\n\t"
    "v_cndmask_b32 v202, v12, v8, vcc\n\t"
    "v_cndmask_b32 v203, v13, v9, vcc\n\t"
    "v_mfma_f32_16x16x32_bf16 v[32:35], a[96:99], v[236:239], v[32:35]\n\t"
    "s_waitcnt lgkmcnt(0)\n\t"
    "s_barrier\n\t"
    "s_add_u32 m0, s68, 0x18000\n\t"
    "s_mov_b32 s74, 0x10000\n\t"
    "buffer_load_dwordx4 v192, s[40:43], s74 offen nt lds\n\t"
    "v_cvt_pk_bf16_f32 v16, v16, v17\n\t"
    "v_cvt_pk_bf16_f32 v17, v18, v19\n\t"
    "v_mfma_f32_16x16x32_bf16 v[36:39], a[100:103], v[236:239], v[36:39]\n\t"
    "v_cvt_pk_bf16_f32 v20, v20, v21\n\t"
    "v_cvt_pk_bf16_f32 v21, v22, v23\n\t"
    "v_mfma_f32_16x16x32_bf16 v[40:43], a[104:107], v[236:239], v[40:43]\n\t"
    "buffer_load_dwordx4 v193, s[40:43], s74 offen offset:1024 nt lds\n\t"
    "v_cndmask_b32 v204, v16, v20, vcc\n\t"
    "v_cndmask_b32 v205, v17, v21, vcc\n\t"
    "v_mfma_f32_16x16x32_bf16 v[44:47], a[108:111], v[236:239], v[44:47]\n\t"
    "v_cndmask_b32 v206, v20, v16, vcc\n\t"
    "v_cndmask_b32 v207, v21, v17, vcc\n\t"
    "v_mfma_f32_16x16x32_bf16 v[48:51], a[112:115], v[236:239], v[48:51]\n\t"
    "v_cvt_pk_bf16_f32 v24, v24, v25\n\t"
    "v_cvt_pk_bf16_f32 v25, v26, v27\n\t"
    "v_mfma_f32_16x16x32_bf16 v[52:55], a[116:119], v[236:239], v[52:55]\n\t"
    "v_cvt_pk_bf16_f32 v28, v28, v29\n\t"
    "v_cvt_pk_bf16_f32 v29, v30, v31\n\t"
    "v_mfma_f32_16x16x32_bf16 v[56:59], a[120:123], v[236:239], v[56:59]\n\t"
    "v_cndmask_b32 v208, v24, v28, vcc\n\t"
    "v_cndmask_b32 v209, v25, v29, vcc\n\t"
    "v_mfma_f32_16x16x32_bf16 v[60:63], a[124:127], v[236:239], v[60:63]\n\t"
    "v_cndmask_b32 v210, v28, v24, vcc\n\t"
    "v_cndmask_b32 v211, v29, v25, vcc\n\t"
    "v_pk_mul_f32 v[32:33], v[32:33], s[78:79]\n\t"
    "v_pk_mul_f32 v[34:35], v[34:35], s[78:79]\n\t"
    "v_pk_mul_f32 v[36:37], v[36:37], s[78:79]\n\t"
    "v_pk_mul_f32 v[38:39], v[38:39], s[78:79]\n\t"
    "v_pk_mul_f32 v[40:41], v[40:41], s[78:79]\n\t"
    "v_pk_mul_f32 v[42:43], v[42:43], s[78:79]\n\t"
    "v_pk_mul_f32 v[44:45], v[44:45], s[78:79]\n\t"
    "v_pk_mul_f32 v[46:47], v[46:47], s[78:79]\n\t"
    "v_pk_mul_f32 v[48:49], v[48:49], s[78:79]\n\t"
    "v_pk_mul_f32 v[50:51], v[50:51], s[78:79]\n\t"
    "v_pk_mul_f32 v[52:53], v[52:53], s[78:79]\n\t"
    "v_pk_mul_f32 v[54:55], v[54:55], s[78:79]\n\t"
    "v_pk_mul_f32 v[56:57], v[56:57], s[78:79]\n\t"
    "v_pk_mul_f32 v[58:59], v[58:59], s[78:79]\n\t"
    "v_pk_mul_f32 v[60:61], v[60:61], s[78:79]\n\t"
    "v_pk_mul_f32 v[62:63], v[62:63], s[78:79]\n\t"
    "v_mfma_f32_16x16x32_bf16 v[64:67], a[96:99], v[240:243], v[64:67]\n\t"
    "v_cvt_pk_bf16_f32 v32, v32, v33\n\t"
    "v_cvt_pk_bf16_f32 v33, v34, v35\n\t"
    "v_mfma_f32_16x16x32_bf16 v[68:71], a[100:103], v[240:243], v[68:71]\n\t"
    "v_cvt_pk_bf16_f32 v36, v36, v37\n\t"
    "v_cvt_pk_bf16_f32 v37, v38, v39\n\t"
    "v_mfma_f32_16x16x32_bf16 v[72:75], a[104:107], v[240:243], v[72:75]\n\t"
    "v_cndmask_b32 v212, v32, v36, vcc\n\t"
    "v_cndmask_b32 v213, v33, v37, vcc\n\t"
    "v_mfma_f32_16x16x32_bf16 v[76:79], a[108:111], v[240:243], v[76:79]\n\t"
    "v_cndmask_b32 v214, v36, v32, vcc\n\t"
    "v_cndmask_b32 v215, v37, v33, vcc\n\t"
    "v_mfma_f32_16x16x32_bf16 v[80:83], a[112:115], v[240:243], v[80:83]\n\t"
    "v_cvt_pk_bf16_f32 v40, v40, v41\n\t"
    "v_cvt_pk_bf16_f32 v41, v42, v43\n\t"
    "v_mfma_f32_16x16x32_bf16 v[84:87], a[116:119], v[240:243], v[84:87]\n\t"
    "v_cvt_pk_bf16_f32 v44, v44, v45\n\t"
    "v_cvt_pk_bf16_f32 v45, v46, v47\n\t"
    "v_mfma_f32_16x16x32_bf16 v[88:91], a[120:123], v[240:243], v[88:91]\n\t"
    "v_cndmask_b32 v216, v40, v44, vcc\n\t"
    "v_cndmask_b32 v217, v41, v45, vcc\n\t"
    "v_mfma_f32_16x16x32_bf16 v[92:95], a[124:127], v[240:243], v[92:95]\n\t"
    "v_cndmask_b32 v218, v44, v40, vcc\n\t"
    "v_cndmask_b32 v219, v45, v41, vcc\n\t"
    "v_mfma_f32_16x16x32_bf16 v[108:111], a[0:3], v[196:199], 0\n\t"
    "v_cvt_pk_bf16_f32 v48, v48, v49\n\t"
    "v_cvt_pk_bf16_f32 v49, v50, v51\n\t"
    "v_mfma_f32_16x16x32_bf16 v[108:111], a[4:7], v[200:203], v[108:111]\n\t"
    "v_cvt_pk_bf16_f32 v52, v52, v53\n\t"
    "v_cvt_pk_bf16_f32 v53, v54, v55\n\t"
    "v_mfma_f32_16x16x32_bf16 v[108:111], a[8:11], v[204:207], v[108:111]\n\t"
    "v_cndmask_b32 v220, v48, v52, vcc\n\t"
    "v_cndmask_b32 v221, v49, v53, vcc\n\t"
    "v_mfma_f32_16x16x32_bf16 v[108:111], a[12:15], v[208:211], v[108:111]\n\t"
    "v_cndmask_b32 v222, v52, v48, vcc\n\t"
    "v_cndmask_b32 v223, v53, v49, vcc\n\t"
    "v_mfma_f32_16x16x32_bf16 v[120:123], a[16:19], v[196:199], 0\n\t"
    "v_cvt_pk_bf16_f32 v56, v56, v57\n\t"
    "v_cvt_pk_bf16_f32 v57, v58, v59\n\t"
    "v_mfma_f32_16x16x32_bf16 v[120:123], a[20:23], v[200:203], v[120:123]\n\t"
    "v_cvt_pk_bf16_f32 v60, v60, v61\n\t"
    "v_cvt_pk_bf16_f32 v61, v62, v63\n\t"
    "v_mfma_f32_16x16x32_bf16 v[120:123], a[24:27], v[204:207], v[120:123]\n\t"
    "v_cndmask_b32 v224, v56, v60, vcc\n\t"
    "v_cndmask_b32 v225, v57, v61, vcc\n\t"
    "v_mfma_f32_16x16x32_bf16 v[120:123], a[28:31], v[208:211], v[120:123]\n\t"
    "v_cndmask_b32 v226, v60, v56, vcc\n\t"
    "v_cndmask_b32 v227, v61, v57, vcc\n\t"
    "v_pk_mul_f32 v[64:65], v[64:65], s[78:79]\n\t"
    "v_pk_mul_f32 v[66:67], v[66:67], s[78:79]\n\t"
    "v_pk_mul_f32 v[68:69], v[68:69], s[78:79]\n\t"
    "v_pk_mul_f32 v[70:71], v[70:71], s[78:79]\n\t"
    "v_pk_mul_f32 v[72:73], v[72:73], s[78:79]\n\t"
    "v_pk_mul_f32 v[74:75], v[74:75], s[78:79]\n\t"
    "v_pk_mul_f32 v[76:77], v[76:77], s[78:79]\n\t"
    "v_pk_mul_f32 v[78:79], v[78:79], s[78:79]\n\t"
    "v_pk_mul_f32 v[80:81], v[80:81], s[78:79]\n\t"
    "v_pk_mul_f32 v[82:83], v[82:83], s[78:79]\n\t"
    "v_pk_mul_f32 v[84:85], v[84:85], s[78:79]\n\t"
    "v_pk_mul_f32 v[86:87], v[86:87], s[78:79]\n\t"
    "v_pk_mul_f32 v[88:89], v[88:89], s[78:79]\n\t"
    "v_pk_mul_f32 v[90:91], v[90:91], s[78:79]\n\t"
    "v_pk_mul_f32 v[92:93], v[92:93], s[78:79]\n\t"
    "v_pk_mul_f32 v[94:95], v[94:95], s[78:79]\n\t"
    "v_mfma_f32_16x16x32_bf16 v[112:115], a[0:3], v[212:215], 0\n\t"
    "v_cvt_pk_bf16_f32 v64, v64, v65\n\t"
    "v_cvt_pk_bf16_f32 v65, v66, v67\n\t"
    "v_mfma_f32_16x16x32_bf16 v[112:115], a[4:7], v[216:219], v[112:115]\n\t"
    "v_cvt_pk_bf16_f32 v68, v68, v69\n\t"
    "v_cvt_pk_bf16_f32 v69, v70, v71\n\t"
    "v_mfma_f32_16x16x32_bf16 v[112:115], a[8:11], v[220:223], v[112:115]\n\t"
    "v_cndmask_b32 v228, v64, v68, vcc\n\t"
    "v_cndmask_b32 v229, v65, v69, vcc\n\t"
    "v_mfma_f32_16x16x32_bf16 v[112:115], a[12:15], v[224:227], v[112:115]\n\t"
    "v_cndmask_b32 v230, v68, v64, vcc\n\t"
    "v_cndmask_b32 v231, v69, v65, vcc\n\t"
    "v_mfma_f32_16x16x32_bf16 v[124:127], a[16:19], v[212:215], 0\n\t"
    "v_cvt_pk_bf16_f32 v72, v72, v73\n\t"
    "v_cvt_pk_bf16_f32 v73, v74, v75\n\t"
    "v_mfma_f32_16x16x32_bf16 v[124:127], a[20:23], v[216:219], v[124:127]\n\t"
    "v_cvt_pk_bf16_f32 v76, v76, v77\n\t"
    "v_cvt_pk_bf16_f32 v77, v78, v79\n\t"
    "v_mfma_f32_16x16x32_bf16 v[124:127], a[24:27], v[220:223], v[124:127]\n\t"
    "v_cndmask_b32 v232, v72, v76, vcc\n\t"
    "v_cndmask_b32 v233, v73, v77, vcc\n\t"
    "v_mfma_f32_16x16x32_bf16 v[124:127], a[28:31], v[224:227], v[124:127]\n\t"
    "v_cndmask_b32 v234, v76, v72, vcc\n\t"
    "v_cndmask_b32 v235, v77, v73, vcc\n\t"
    "v_mfma_f32_32x32x16_bf16 v[0:15], v[96:99], v[96:99], 0\n\t"
    "v_cvt_pk_bf16_f32 v80, v80, v81\n\t"
    "v_cvt_pk_bf16_f32 v81, v82, v83\n\t"
    "v_cvt_pk_bf16_f32 v84, v84, v85\n\t"
    "v_cvt_pk_bf16_f32 v85, v86, v87\n\t"
    "v_cndmask_b32 v236, v80, v84, vcc\n\t"
    "v_cndmask_b32 v237, v81, v85, vcc\n\t"
    "v_cndmask_b32 v238, v84, v80, vcc\n\t"
    "v_cndmask_b32 v239, v85, v81, vcc\n\t"
    "v_mfma_f32_32x32x16_bf16 v[16:31], v[96:99], v[96:99], 0\n\t"
    "v_cvt_pk_bf16_f32 v88, v88, v89\n\t"
    "v_cvt_pk_bf16_f32 v89, v90, v91\n\t"
    "v_cvt_pk_bf16_f32 v92, v92, v93\n\t"
    "v_cvt_pk_bf16_f32 v93, v94, v95\n\t"
    "v_cndmask_b32 v240, v88, v92, vcc\n\t"
    "v_cndmask_b32 v241, v89, v93, vcc\n\t"
    "v_cndmask_b32 v242, v92, v88, vcc\n\t"
    "v_cndmask_b32 v243, v93, v89, vcc\n\t"
    "v_mfma_f32_16x16x32_bf16 v[116:119], a[0:3], v[228:231], 0\n\t"
    "v_max3_f32 v144, v108, v109, v110\n\t"
    "v_max3_f32 v145, v112, v113, v114\n\t"
    "v_mfma_f32_16x16x32_bf16 v[116:119], a[4:7], v[232:235], v[116:119]\n\t"
    "v_max3_f32 v144, v144, v111, v120\n\t"
    "v_max3_f32 v145, v145, v115, v124\n\t"
    "v_mfma_f32_16x16x32_bf16 v[116:119], a[8:11], v[236:239], v[116:119]\n\t"
    "v_max3_f32 v144, v144, v121, v122\n\t"
    "v_max3_f32 v145, v145, v125, v126\n\t"
    "v_mfma_f32_16x16x32_bf16 v[116:119], a[12:15], v[240:243], v[116:119]\n\t"
    "v_max_f32 v144, v144, v123\n\t"
    "v_max_f32 v145, v145, v127\n\t"
    "v_mfma_f32_16x16x32_bf16 v[128:131], a[16:19], v[228:231], 0\n\t"
    "v_mov_b32 v148, v144\n\t"
    "v_mov_b32 v149, v145\n\t"
    "v_mfma_f32_16x16x32_bf16 v[128:131], a[20:23], v[232:235], v[128:131]\n\t"
    "ds_read_b128 a[0:3], v172 offset:49152\n\t"
    "v_permlane32_swap_b32 v144, v148\n\t"
    "v_mfma_f32_16x16x32_bf16 v[128:131], a[24:27], v[236:239], v[128:131]\n\t"
    "ds_read_b128 a[4:7], v173 offset:49152\n\t"
    "v_permlane32_swap_b32 v145, v149\n\t"
    "v_mfma_f32_16x16x32_bf16 v[128:131], a[28:31], v[240:243], v[128:131]\n\t"
    "ds_read_b128 a[8:11], v174 offset:49152\n\t"
    "ds_read_b128 a[12:15], v175 offset:49152\n\t"
    "ds_read_b128 a[16:19], v172 offset:53248\n\t"
    "ds_read_b128 a[20:23], v173 offset:53248\n\t"
    "ds_read_b128 a[24:27], v174 offset:53248\n\t"
    "ds_read_b128 a[28:31], v175 offset:53248\n\t"
    "v_max_f32 v144, v144, v148\n\t"
    "v_max_f32 v145, v145, v149\n\t"
    "v_mov_b32 v148, v144\n\t"
    "v_mov_b32 v149, v145\n\t"
    "s_nop 0\n\t"
    "v_permlane16_swap_b32 v144, v148\n\t"
    "v_permlane16_swap_b32 v145, v149\n\t"
    "v_max_f32 v144, v144, v148\n\t"
    "v_max_f32 v145, v145, v149\n\t"
    "v_xor_b32 v156, 0x80000000, v144\n\t"
    "v_xor_b32 v160, 0x80000000, v145\n\t"
    "v_xor_b32 v157, 0x80000000, v144\n\t"
    "v_xor_b32 v161, 0x80000000, v145\n\t"
    "v_xor_b32 v158, 0x80000000, v144\n\t"
    "v_xor_b32 v162, 0x80000000, v145\n\t"
    "v_xor_b32 v159, 0x80000000, v144\n\t"
    "v_xor_b32 v163, 0x80000000, v145\n\t"
    "v_pk_add_f32 v[108:109], v[108:109], v[156:157]\n\t"
    "v_pk_add_f32 v[110:111], v[110:111], v[156:157]\n\t"
    "v_pk_add_f32 v[112:113], v[112:113], v[160:161]\n\t"
    "v_pk_add_f32 v[114:115], v[114:115], v[160:161]\n\t"
    "v_pk_add_f32 v[120:121], v[120:121], v[156:157]\n\t"
    "v_pk_add_f32 v[122:123], v[122:123], v[156:157]\n\t"
    "v_pk_add_f32 v[124:125], v[124:125], v[160:161]\n\t"
    "v_pk_add_f32 v[126:127], v[126:127], v[160:161]\n\t"
    "v_mfma_f32_32x32x16_bf16 v[32:47], v[96:99], v[96:99], 0\n\t"
    "v_max3_f32 v146, v116, v117, v118\n\t"
    "v_exp_f32 v108, v108\n\t"
    "v_max3_f32 v146, v146, v119, v128\n\t"
    "v_exp_f32 v109, v109\n\t"
    "v_max3_f32 v146, v146, v129, v130\n\t"
    "v_exp_f32 v110, v110\n\t"
    "v_mfma_f32_32x32x16_bf16 v[48:63], v[96:99], v[96:99], 0\n\t"
    "v_max_f32 v146, v146, v131\n\t"
    "v_cvt_pk_bf16_f32 v132, v108, v109\n\t"
    "v_mov_b32 v150, v146\n\t"
    "v_exp_f32 v111, v111\n\t"
    "v_exp_f32 v112, v112\n\t"
    "v_permlane32_swap_b32 v146, v150\n\t"
    "v_mfma_f32_32x32x16_bf16 v[64:79], v[96:99], v[96:99], 0\n\t"
    "v_cvt_pk_bf16_f32 v133, v110, v111\n\t"
    "v_exp_f32 v113, v113\n\t"
    "v_max_f32 v146, v146, v150\n\t"
    "v_exp_f32 v114, v114\n\t"
    "v_mov_b32 v150, v146\n\t"
    "v_cvt_pk_bf16_f32 v136, v112, v113\n\t"
    "v_mfma_f32_32x32x16_bf16 v[80:95], v[96:99], v[96:99], 0\n\t"
    "v_exp_f32 v115, v115\n\t"
    "v_permlane16_swap_b32 v146, v150\n\t"
    "v_cvt_pk_bf16_f32 v137, v114, v115\n\t"
    "v_exp_f32 v120, v120\n\t"
    "v_max_f32 v146, v146, v150\n\t"
    "v_exp_f32 v121, v121\n\t"
    "v_xor_b32 v164, 0x80000000, v146\n\t"
    "v_exp_f32 v122, v122\n\t"
    "v_xor_b32 v165, 0x80000000, v146\n\t"
    "v_cvt_pk_bf16_f32 v134, v120, v121\n\t"
    "v_xor_b32 v166, 0x80000000, v146\n\t"
    "v_exp_f32 v123, v123\n\t"
    "v_xor_b32 v167, 0x80000000, v146\n\t"
    "v_exp_f32 v124, v124\n\t"
    "v_cvt_pk_bf16_f32 v135, v122, v123\n\t"
    "v_exp_f32 v125, v125\n\t"
    "v_exp_f32 v126, v126\n\t"
    "v_cvt_pk_bf16_f32 v138, v124, v125\n\t"
    "v_add_u32 v192, 0x4000, v192\n\t"
    "v_add_u32 v193, 0x4000, v193\n\t"
    "v_pk_add_f32 v[116:117], v[116:117], v[164:165]\n\t"
    "v_pk_add_f32 v[118:119], v[118:119], v[164:165]\n\t"
    "v_pk_add_f32 v[128:129], v[128:129], v[164:165]\n\t"
    "v_pk_add_f32 v[130:131], v[130:131], v[164:165]\n\t"
    "v_exp_f32 v116, v116\n\t"
    "v_exp_f32 v117, v117\n\t"
    "v_exp_f32 v118, v118\n\t"
    "v_exp_f32 v119, v119\n\t"
    "v_cvt_pk_bf16_f32 v140, v116, v117\n\t"
    "v_cvt_pk_bf16_f32 v141, v118, v119\n\t"
    "s_mov_b32 s67, 0xe000\n\t"
    "v_mul_u32_u24 v254, 0xa000, v244\n\t"
    "buffer_load_dword a255, v254, s[52:55], 0 offen\n\t"
    "v_lshlrev_b32 v253, 17, v244\n\t"
    "buffer_load_dword a253, v253, s[56:59], 0 offen sc1\n\t"
    "s_add_u32 s39, s98, 0x38e\n\t"
    "s_mov_b32 s18, 0xfffe7960\n\t"
    "s_mov_b32 s77, 0\n\t"
    "s_memrealtime s[70:71]\n\t"
    "s_bitcmp1_b32 s20, 0\n\t"
    "s_call_b64 s[36:37], Lcom%=\n\t"
    "s_barrier\n\t"
    "v_mfma_f32_16x16x32_bf16 v[108:111], a[0:3], v[196:199], v[156:159]\n\t"
    "ds_read_b64_tr_b16 a[32:33], v184 offset:57344\n\t"
    "ds_read_b64_tr_b16 a[34:35], v184 offset:61440\n\t"
    "v_mfma_f32_16x16x32_bf16 v[108:111], a[4:7], v[200:203], v[108:111]\n\t"
    "s_sub_u32 s75, s39, s70\n\t"
    "v_exp_f32 v127, v127\n\t"
    "v_mfma_f32_16x16x32_bf16 v[108:111], a[8:11], v[204:207], v[108:111]\n\t"
    "s_max_i32 s39, s75, 0\n\t"
    "v_exp_f32 v128, v128\n\t"
    "v_mfma_f32_16x16x32_bf16 v[108:111], a[12:15], v[208:211], v[108:111]\n\t"
    "s_add_u32 m0, s68, 0x18000\n\t"
    "ds_read_b64_tr_b16 a[36:37], v185 offset:57344\n\t"
    "v_cvt_pk_bf16_f32 v151, v126, v127\n\t"
    "v_mfma_f32_16x16x32_bf16 v[112:115], a[0:3], v[212:215], v[160:163]\n\t"
    "s_sub_u32 s21, s39, s75\n\t"
    "buffer_load_dwordx4 v192, s[40:43], s67 offen nt lds\n\t"
    "v_mfma_f32_16x16x32_bf16 v[112:115], a[4:7], v[216:219], v[112:115]\n\t"
    "s_add_u32 s39, s39, s70\n\t"
    "v_exp_f32 v129, v129\n\t"
    "v_mfma_f32_16x16x32_bf16 v[112:115], a[8:11], v[220:223], v[112:115]\n\t"
    "s_max_i32 s75, s75, 0\n\t"
    "v_exp_f32 v130, v130\n\t"
    "v_mfma_f32_16x16x32_bf16 v[112:115], a[12:15], v[224:227], v[112:115]\n\t"
    "s_min_i32 s21, s21, s77\n\t"
    "ds_read_b64_tr_b16 a[38:39], v185 offset:61440\n\t"
    "v_cvt_pk_bf16_f32 v154, v128, v129\n\t"
    "v_mfma_f32_16x16x32_bf16 v[116:119], a[0:3], v[228:231], v[164:167]\n\t"
    "s_add_u32 s39, s39, s19\n\t"
    "v_exp_f32 v131, v131\n\t"
    "v_mfma_f32_16x16x32_bf16 v[116:119], a[4:7], v[232:235], v[116:119]\n\t"
    "s_add_u32 s18, s18, s21\n\t"
    "ds_read_b64_tr_b16 a[40:41], v186 offset:57344\n\t"
    "v_cvt_pk_bf16_f32 v155, v130, v131\n\t"
    "v_mfma_f32_16x16x32_bf16 v[116:119], a[8:11], v[236:239], v[116:119]\n\t"
    "s_sub_u32 s18, s18, 6\n\t"
    "ds_read_b64_tr_b16 a[42:43], v186 offset:61440\n\t"
    "ds_read_b64_tr_b16 a[44:45], v187 offset:57344\n\t"
    "v_mfma_f32_16x16x32_bf16 v[116:119], a[12:15], v[240:243], v[116:119]\n\t"
    "s_max_i32 s18, s18, 0\n\t"
    "v_exp_f32 v108, v108\n\t"
    "v_mfma_f32_16x16x32_bf16 v[120:123], a[16:19], v[196:199], v[156:159]\n\t"
    "ds_read_b64_tr_b16 a[46:47], v187 offset:61440\n\t"
    "ds_read_b64_tr_b16 a[48:49], v188 offset:57344\n\t"
    "v_mfma_f32_16x16x32_bf16 v[120:123], a[20:23], v[200:203], v[120:123]\n\t"
    "s_min_i32 s18, s18, 60\n\t"
    "v_exp_f32 v109, v109\n\t"
    "v_mfma_f32_16x16x32_bf16 v[120:123], a[24:27], v[204:207], v[120:123]\n\t"
    "ds_read_b64_tr_b16 a[50:51], v188 offset:61440\n\t"
    "ds_read_b64_tr_b16 a[52:53], v189 offset:57344\n\t"
    "v_mfma_f32_16x16x32_bf16 v[120:123], a[28:31], v[208:211], v[120:123]\n\t"
    "s_lshr_b32 s21, s18, 1\n\t"
    "v_exp_f32 v110, v110\n\t"
    "v_mfma_f32_16x16x32_bf16 v[124:127], a[16:19], v[212:215], v[160:163]\n\t"
    "ds_read_b64_tr_b16 a[54:55], v189 offset:61440\n\t"
    "v_cvt_pk_bf16_f32 v132, v108, v109\n\t"
    "v_mfma_f32_16x16x32_bf16 v[124:127], a[20:23], v[216:219], v[124:127]\n\t"
    "s_add_u32 s39, s39, s21\n\t"
    "v_exp_f32 v111, v111\n\t"
    "v_mfma_f32_16x16x32_bf16 v[124:127], a[24:27], v[220:223], v[124:127]\n\t"
    "ds_read_b64_tr_b16 a[56:57], v190 offset:57344\n\t"
    "ds_read_b64_tr_b16 a[58:59], v190 offset:61440\n\t"
    "v_mfma_f32_16x16x32_bf16 v[124:127], a[28:31], v[224:227], v[124:127]\n\t"
    "v_exp_f32 v112, v112\n\t"
    "v_mfma_f32_16x16x32_bf16 v[128:131], a[16:19], v[228:231], v[164:167]\n\t"
    "ds_read_b64_tr_b16 a[60:61], v191 offset:57344\n\t"
    "v_cvt_pk_bf16_f32 v133, v110, v111\n\t"
    "v_mfma_f32_16x16x32_bf16 v[128:131], a[20:23], v[232:235], v[128:131]\n\t"
    "ds_read_b64_tr_b16 a[62:63], v191 offset:61440\n\t"
    "ds_read_b128 a[0:3], v172 offset:49152\n\t"
    "v_mfma_f32_16x16x32_bf16 v[128:131], a[24:27], v[236:239], v[128:131]\n\t"
    "v_exp_f32 v113, v113\n\t"
    "v_mfma_f32_16x16x32_bf16 v[128:131], a[28:31], v[240:243], v[128:131]\n\t"
    "v_exp_f32 v114, v114\n\t"
    "v_mfma_f32_16x16x32_bf16 v[96:99], a[240:243], v[144:147], v[96:99]\n\t"
    "ds_read_b128 a[4:7], v173 offset:49152\n\t"
    "v_cvt_pk_bf16_f32 v136, v112, v113\n\t"
    "v_mfma_f32_16x16x32_bf16 v[96:99], a[244:247], v[148:151], v[96:99]\n\t"
    "Lw107%=:\n\t"
    "s_sub_u32 s75, s75, 2\n\t"
    "s_cbranch_scc0 Lw107%=\n\t"
    "v_mfma_f32_16x16x32_bf16 v[96:99], a[248:251], v[152:155], v[96:99]\n\t"
    "s_waitcnt lgkmcnt(2)\n\t"
    "v_exp_f32 v115, v115\n\t"
    "v_mfma_f32_16x16x32_bf16 v[64:67], a[32:35], v[152:155], v[64:67]\n\t"
    "buffer_load_dwordx4 v193, s[40:43], s67 offen offset:1024 nt lds\n\t"
    "v_mfma_f32_16x16x32_bf16 v[32:35], a[32:35], v[148:151], v[32:35]\n\t"
    "v_exp_f32 v116, v116\n\t"
    "v_mfma_f32_16x16x32_bf16 v[0:3], a[32:35], v[144:147], v[0:3]\n\t"
    "ds_read_b128 a[8:11], v174 offset:49152\n\t"
    "v_cvt_pk_bf16_f32 v137, v114, v115\n\t"
    "v_mfma_f32_16x16x32_bf16 v[4:7], a[36:39], v[144:147], v[4:7]\n\t"
    "v_exp_f32 v117, v117\n\t"
    "v_mfma_f32_16x16x32_bf16 v[36:39], a[36:39], v[148:151], v[36:39]\n\t"
    "v_exp_f32 v118, v118\n\t"
    "v_mfma_f32_16x16x32_bf16 v[68:71], a[36:39], v[152:155], v[68:71]\n\t"
    "ds_read_b128 a[12:15], v175 offset:49152\n\t"
    "v_cvt_pk_bf16_f32 v140, v116, v117\n\t"
    "v_mfma_f32_16x16x32_bf16 v[72:75], a[40:43], v[152:155], v[72:75]\n\t"
    "v_exp_f32 v119, v119\n\t"
    "v_mfma_f32_16x16x32_bf16 v[40:43], a[40:43], v[148:151], v[40:43]\n\t"
    "s_add_u32 m0, s68, 0x1a000\n\t"
    "v_exp_f32 v120, v120\n\t"
    "v_mfma_f32_16x16x32_bf16 v[8:11], a[40:43], v[144:147], v[8:11]\n\t"
    "ds_read_b128 a[16:19], v172 offset:53248\n\t"
    "v_cvt_pk_bf16_f32 v141, v118, v119\n\t"
    "v_mfma_f32_16x16x32_bf16 v[12:15], a[44:47], v[144:147], v[12:15]\n\t"
    "buffer_load_dwordx4 v194, s[44:47], s67 offen nt lds\n\t"
    "v_mfma_f32_16x16x32_bf16 v[44:47], a[44:47], v[148:151], v[44:47]\n\t"
    "v_exp_f32 v121, v121\n\t"
    "v_mfma_f32_16x16x32_bf16 v[76:79], a[44:47], v[152:155], v[76:79]\n\t"
    "s_memrealtime s[70:71]\n\t"
    "v_exp_f32 v122, v122\n\t"
    "v_mfma_f32_16x16x32_bf16 v[80:83], a[48:51], v[152:155], v[80:83]\n\t"
    "ds_read_b128 a[20:23], v173 offset:53248\n\t"
    "v_cvt_pk_bf16_f32 v134, v120, v121\n\t"
    "v_mfma_f32_16x16x32_bf16 v[48:51], a[48:51], v[148:151], v[48:51]\n\t"
    "v_exp_f32 v123, v123\n\t"
    "v_mfma_f32_16x16x32_bf16 v[16:19], a[48:51], v[144:147], v[16:19]\n\t"
    "v_exp_f32 v124, v124\n\t"
    "v_mfma_f32_16x16x32_bf16 v[20:23], a[52:55], v[144:147], v[20:23]\n\t"
    "ds_read_b128 a[24:27], v174 offset:53248\n\t"
    "v_cvt_pk_bf16_f32 v135, v122, v123\n\t"
    "v_mfma_f32_16x16x32_bf16 v[52:55], a[52:55], v[148:151], v[52:55]\n\t"
    "v_exp_f32 v125, v125\n\t"
    "v_mfma_f32_16x16x32_bf16 v[84:87], a[52:55], v[152:155], v[84:87]\n\t"
    "v_exp_f32 v126, v126\n\t"
    "v_mfma_f32_16x16x32_bf16 v[88:91], a[56:59], v[152:155], v[88:91]\n\t"
    "ds_read_b128 a[28:31], v175 offset:53248\n\t"
    "v_cvt_pk_bf16_f32 v138, v124, v125\n\t"
    "v_mfma_f32_16x16x32_bf16 v[56:59], a[56:59], v[148:151], v[56:59]\n\t"
    "buffer_load_dwordx4 v195, s[44:47], s67 offen offset:1024 nt lds\n\t"
    "v_mfma_f32_16x16x32_bf16 v[24:27], a[56:59], v[144:147], v[24:27]\n\t"
    "s_add_u32 s67, s67, 0x2000\n\t"
    "v_mfma_f32_16x16x32_bf16 v[28:31], a[60:63], v[144:147], v[28:31]\n\t"
    "s_add_u32 s19, s19, 4\n\t"
    "v_mfma_f32_16x16x32_bf16 v[60:63], a[60:63], v[148:151], v[60:63]\n\t"
    "s_waitcnt vmcnt(12) lgkmcnt(0)\n\t"
    "v_mfma_f32_16x16x32_bf16 v[92:95], a[60:63], v[152:155], v[92:95]\n\t"
    "s_mov_b32 s77, 56\n\t"
    "s_bitcmp1_b32 s20, 1\n\t"
    "s_call_b64 s[36:37], Lcom%=\n\t"
    "s_barrier\n\t"
    "v_mfma_f32_16x16x32_bf16 v[108:111], a[0:3], v[196:199], v[156:159]\n\t"
    "ds_read_b64_tr_b16 a[32:33], v184 offset:57344\n\t"
    "ds_read_b64_tr_b16 a[34:35], v184 offset:61440\n\t"
    "v_mfma_f32_16x16x32_bf16 v[108:111], a[4:7], v[200:203], v[108:111]\n\t"
    "s_sub_u32 s75, s39, s70\n\t"
    "v_exp_f32 v127, v127\n\t"
    "v_mfma_f32_16x16x32_bf16 v[108:111], a[8:11], v[204:207], v[108:111]\n\t"
    "s_max_i32 s39, s75, 0\n\t"
    "v_exp_f32 v128, v128\n\t"
    "v_mfma_f32_16x16x32_bf16 v[108:111], a[12:15], v[208:211], v[108:111]\n\t"
    "s_add_u32 m0, s68, 0x18000\n\t"
    "ds_read_b64_tr_b16 a[36:37], v185 offset:57344\n\t"
    "v_cvt_pk_bf16_f32 v151, v126, v127\n\t"
    "v_mfma_f32_16x16x32_bf16 v[112:115], a[0:3], v[212:215], v[160:163]\n\t"
    "s_sub_u32 s21, s39, s75\n\t"
    "buffer_load_dwordx4 v192, s[40:43], s67 offen nt lds\n\t"
    "v_mfma_f32_16x16x32_bf16 v[112:115], a[4:7], v[216:219], v[112:115]\n\t"
    "s_add_u32 s39, s39, s70\n\t"
    "v_exp_f32 v129, v129\n\t"
    "v_mfma_f32_16x16x32_bf16 v[112:115], a[8:11], v[220:223], v[112:115]\n\t"
    "s_max_i32 s75, s75, 0\n\t"
    "v_exp_f32 v130, v130\n\t"
    "v_mfma_f32_16x16x32_bf16 v[112:115], a[12:15], v[224:227], v[112:115]\n\t"
    "s_min_i32 s21, s21, s77\n\t"
    "ds_read_b64_tr_b16 a[38:39], v185 offset:61440\n\t"
    "v_cvt_pk_bf16_f32 v154, v128, v129\n\t"
    "v_mfma_f32_16x16x32_bf16 v[116:119], a[0:3], v[228:231], v[164:167]\n\t"
    "s_add_u32 s39, s39, s19\n\t"
    "v_exp_f32 v131, v131\n\t"
    "v_mfma_f32_16x16x32_bf16 v[116:119], a[4:7], v[232:235], v[116:119]\n\t"
    "s_add_u32 s18, s18, s21\n\t"
    "ds_read_b64_tr_b16 a[40:41], v186 offset:57344\n\t"
    "v_cvt_pk_bf16_f32 v155, v130, v131\n\t"
    "v_mfma_f32_16x16x32_bf16 v[116:119], a[8:11], v[236:239], v[116:119]\n\t"
    "s_sub_u32 s18, s18, 6\n\t"
    "ds_read_b64_tr_b16 a[42:43], v186 offset:61440\n\t"
    "ds_read_b64_tr_b16 a[44:45], v187 offset:57344\n\t"
    "v_mfma_f32_16x16x32_bf16 v[116:119], a[12:15], v[240:243], v[116:119]\n\t"
    "s_max_i32 s18, s18, 0\n\t"
    "v_exp_f32 v108, v108\n\t"
    "v_mfma_f32_16x16x32_bf16 v[120:123], a[16:19], v[196:199], v[156:159]\n\t"
    "ds_read_b64_tr_b16 a[46:47], v187 offset:61440\n\t"
    "ds_read_b64_tr_b16 a[48:49], v188 offset:57344\n\t"
    "v_mfma_f32_16x16x32_bf16 v[120:123], a[20:23], v[200:203], v[120:123]\n\t"
    "s_min_i32 s18, s18, 60\n\t"
    "v_exp_f32 v109, v109\n\t"
    "v_mfma_f32_16x16x32_bf16 v[120:123], a[24:27], v[204:207], v[120:123]\n\t"
    "ds_read_b64_tr_b16 a[50:51], v188 offset:61440\n\t"
    "ds_read_b64_tr_b16 a[52:53], v189 offset:57344\n\t"
    "v_mfma_f32_16x16x32_bf16 v[120:123], a[28:31], v[208:211], v[120:123]\n\t"
    "s_lshr_b32 s21, s18, 1\n\t"
    "v_exp_f32 v110, v110\n\t"
    "v_mfma_f32_16x16x32_bf16 v[124:127], a[16:19], v[212:215], v[160:163]\n\t"
    "ds_read_b64_tr_b16 a[54:55], v189 offset:61440\n\t"
    "v_cvt_pk_bf16_f32 v132, v108, v109\n\t"
    "v_mfma_f32_16x16x32_bf16 v[124:127], a[20:23], v[216:219], v[124:127]\n\t"
    "s_add_u32 s39, s39, s21\n\t"
    "v_exp_f32 v111, v111\n\t"
    "v_mfma_f32_16x16x32_bf16 v[124:127], a[24:27], v[220:223], v[124:127]\n\t"
    "ds_read_b64_tr_b16 a[56:57], v190 offset:57344\n\t"
    "ds_read_b64_tr_b16 a[58:59], v190 offset:61440\n\t"
    "v_mfma_f32_16x16x32_bf16 v[124:127], a[28:31], v[224:227], v[124:127]\n\t"
    "v_exp_f32 v112, v112\n\t"
    "v_mfma_f32_16x16x32_bf16 v[128:131], a[16:19], v[228:231], v[164:167]\n\t"
    "ds_read_b64_tr_b16 a[60:61], v191 offset:57344\n\t"
    "v_cvt_pk_bf16_f32 v133, v110, v111\n\t"
    "v_mfma_f32_16x16x32_bf16 v[128:131], a[20:23], v[232:235], v[128:131]\n\t"
    "ds_read_b64_tr_b16 a[62:63], v191 offset:61440\n\t"
    "ds_read_b128 a[0:3], v172 offset:49152\n\t"
    "v_mfma_f32_16x16x32_bf16 v[128:131], a[24:27], v[236:239], v[128:131]\n\t"
    "v_exp_f32 v113, v113\n\t"
    "v_mfma_f32_16x16x32_bf16 v[128:131], a[28:31], v[240:243], v[128:131]\n\t"
    "v_exp_f32 v114, v114\n\t"
    "v_mfma_f32_16x16x32_bf16 v[96:99], a[240:243], v[144:147], v[96:99]\n\t"
    "ds_read_b128 a[4:7], v173 offset:49152\n\t"
    "v_cvt_pk_bf16_f32 v136, v112, v113\n\t"
    "v_mfma_f32_16x16x32_bf16 v[96:99], a[244:247], v[148:151], v[96:99]\n\t"
    "Lw115%=:\n\t"
    "s_sub_u32 s75, s75, 2\n\t"
    "s_cbranch_scc0 Lw115%=\n\t"
    "v_mfma_f32_16x16x32_bf16 v[96:99], a[248:251], v[152:155], v[96:99]\n\t"
    "s_waitcnt lgkmcnt(2)\n\t"
    "v_exp_f32 v115, v115\n\t"
    "v_mfma_f32_16x16x32_bf16 v[64:67], a[32:35], v[152:155], v[64:67]\n\t"
    "buffer_load_dwordx4 v193, s[40:43], s67 offen offset:1024 nt lds\n\t"
    "v_mfma_f32_16x16x32_bf16 v[32:35], a[32:35], v[148:151], v[32:35]\n\t"
    "v_exp_f32 v116, v116\n\t"
    "v_mfma_f32_16x16x32_bf16 v[0:3], a[32:35], v[144:147], v[0:3]\n\t"
    "ds_read_b128 a[8:11], v174 offset:49152\n\t"
    "v_cvt_pk_bf16_f32 v137, v114, v115\n\t"
    "v_mfma_f32_16x16x32_bf16 v[4:7], a[36:39], v[144:147], v[4:7]\n\t"
    "v_exp_f32 v117, v117\n\t"
    "v_mfma_f32_16x16x32_bf16 v[36:39], a[36:39], v[148:151], v[36:39]\n\t"
    "v_exp_f32 v118, v118\n\t"
    "v_mfma_f32_16x16x32_bf16 v[68:71], a[36:39], v[152:155], v[68:71]\n\t"
    "ds_read_b128 a[12:15], v175 offset:49152\n\t"
    "v_cvt_pk_bf16_f32 v140, v116, v117\n\t"
    "v_mfma_f32_16x16x32_bf16 v[72:75], a[40:43], v[152:155], v[72:75]\n\t"
    "v_exp_f32 v119, v119\n\t"
    "v_mfma_f32_16x16x32_bf16 v[40:43], a[40:43], v[148:151], v[40:43]\n\t"
    "s_add_u32 m0, s68, 0x1a000\n\t"
    "v_exp_f32 v120, v120\n\t"
    "v_mfma_f32_16x16x32_bf16 v[8:11], a[40:43], v[144:147], v[8:11]\n\t"
    "ds_read_b128 a[16:19], v172 offset:53248\n\t"
    "v_cvt_pk_bf16_f32 v141, v118, v119\n\t"
    "v_mfma_f32_16x16x32_bf16 v[12:15], a[44:47], v[144:147], v[12:15]\n\t"
    "buffer_load_dwordx4 v194, s[44:47], s67 offen nt lds\n\t"
    "v_mfma_f32_16x16x32_bf16 v[44:47], a[44:47], v[148:151], v[44:47]\n\t"
    "v_exp_f32 v121, v121\n\t"
    "v_mfma_f32_16x16x32_bf16 v[76:79], a[44:47], v[152:155], v[76:79]\n\t"
    "s_memrealtime s[70:71]\n\t"
    "v_exp_f32 v122, v122\n\t"
    "v_mfma_f32_16x16x32_bf16 v[80:83], a[48:51], v[152:155], v[80:83]\n\t"
    "ds_read_b128 a[20:23], v173 offset:53248\n\t"
    "v_cvt_pk_bf16_f32 v134, v120, v121\n\t"
    "v_mfma_f32_16x16x32_bf16 v[48:51], a[48:51], v[148:151], v[48:51]\n\t"
    "v_exp_f32 v123, v123\n\t"
    "s_branch Lcs4%=\n\t"
    ".p2align 12\n\t"
    "Lc4%=:\n\t"
    "s_setpc_b64 s[22:23]\n\t"
    "Lcs4%=:\n\t"
    "v_mfma_f32_16x16x32_bf16 v[16:19], a[48:51], v[144:147], v[16:19]\n\t"
    "v_exp_f32 v124, v124\n\t"
    "v_mfma_f32_16x16x32_bf16 v[20:23], a[52:55], v[144:147], v[20:23]\n\t"
    "ds_read_b128 a[24:27], v174 offset:53248\n\t"
    "v_cvt_pk_bf16_f32 v135, v122, v123\n\t"
    "v_mfma_f32_16x16x32_bf16 v[52:55], a[52:55], v[148:151], v[52:55]\n\t"
    "v_exp_f32 v125, v125\n\t"
    "v_mfma_f32_16x16x32_bf16 v[84:87], a[52:55], v[152:155], v[84:87]\n\t"
    "v_exp_f32 v126, v126\n\t"
    "v_mfma_f32_16x16x32_bf16 v[88:91], a[56:59], v[152:155], v[88:91]\n\t"
    "ds_read_b128 a[28:31], v175 offset:53248\n\t"
    "v_cvt_pk_bf16_f32 v138, v124, v125\n\t"
    "v_mfma_f32_16x16x32_bf16 v[56:59], a[56:59], v[148:151], v[56:59]\n\t"
    "buffer_load_dwordx4 v195, s[44:47], s67 offen offset:1024 nt lds\n\t"
    "v_mfma_f32_16x16x32_bf16 v[24:27], a[56:59], v[144:147], v[24:27]\n\t"
    "s_add_u32 s67, s67, 0x2000\n\t"
    "v_mfma_f32_16x16x32_bf16 v[28:31], a[60:63], v[144:147], v[28:31]\n\t"
    "s_add_u32 s19, s19, 1\n\t"
    "v_mfma_f32_16x16x32_bf16 v[60:63], a[60:63], v[148:151], v[60:63]\n\t"
    "s_waitcnt vmcnt(12) lgkmcnt(0)\n\t"
    "v_mfma_f32_16x16x32_bf16 v[92:95], a[60:63], v[152:155], v[92:95]\n\t"
    "s_bitcmp1_b32 s20, 2\n\t"
    "s_call_b64 s[36:37], Lcom%=\n\t"
    "s_barrier\n\t"
    "v_mfma_f32_16x16x32_bf16 v[108:111], a[0:3], v[196:199], v[156:159]\n\t"
    "ds_read_b64_tr_b16 a[32:33], v184 offset:57344\n\t"
    "ds_read_b64_tr_b16 a[34:35], v184 offset:61440\n\t"
    "v_mfma_f32_16x16x32_bf16 v[108:111], a[4:7], v[200:203], v[108:111]\n\t"
    "s_sub_u32 s75, s39, s70\n\t"
    "v_exp_f32 v127, v127\n\t"
    "v_mfma_f32_16x16x32_bf16 v[108:111], a[8:11], v[204:207], v[108:111]\n\t"
    "s_max_i32 s39, s75, 0\n\t"
    "v_exp_f32 v128, v128\n\t"
    "v_mfma_f32_16x16x32_bf16 v[108:111], a[12:15], v[208:211], v[108:111]\n\t"
    "s_add_u32 m0, s68, 0x18000\n\t"
    "ds_read_b64_tr_b16 a[36:37], v185 offset:57344\n\t"
    "v_cvt_pk_bf16_f32 v151, v126, v127\n\t"
    "v_mfma_f32_16x16x32_bf16 v[112:115], a[0:3], v[212:215], v[160:163]\n\t"
    "s_sub_u32 s21, s39, s75\n\t"
    "buffer_load_dwordx4 v192, s[40:43], s67 offen nt lds\n\t"
    "v_mfma_f32_16x16x32_bf16 v[112:115], a[4:7], v[216:219], v[112:115]\n\t"
    "s_add_u32 s39, s39, s70\n\t"
    "v_exp_f32 v129, v129\n\t"
    "v_mfma_f32_16x16x32_bf16 v[112:115], a[8:11], v[220:223], v[112:115]\n\t"
    "s_max_i32 s75, s75, 0\n\t"
    "v_exp_f32 v130, v130\n\t"
    "v_mfma_f32_16x16x32_bf16 v[112:115], a[12:15], v[224:227], v[112:115]\n\t"
    "s_min_i32 s21, s21, s77\n\t"
    "ds_read_b64_tr_b16 a[38:39], v185 offset:61440\n\t"
    "v_cvt_pk_bf16_f32 v154, v128, v129\n\t"
    "v_mfma_f32_16x16x32_bf16 v[116:119], a[0:3], v[228:231], v[164:167]\n\t"
    "s_add_u32 s39, s39, s19\n\t"
    "v_exp_f32 v131, v131\n\t"
    "v_mfma_f32_16x16x32_bf16 v[116:119], a[4:7], v[232:235], v[116:119]\n\t"
    "s_add_u32 s18, s18, s21\n\t"
    "ds_read_b64_tr_b16 a[40:41], v186 offset:57344\n\t"
    "v_cvt_pk_bf16_f32 v155, v130, v131\n\t"
    "v_mfma_f32_16x16x32_bf16 v[116:119], a[8:11], v[236:239], v[116:119]\n\t"
    "s_sub_u32 s18, s18, 6\n\t"
    "ds_read_b64_tr_b16 a[42:43], v186 offset:61440\n\t"
    "ds_read_b64_tr_b16 a[44:45], v187 offset:57344\n\t"
    "v_mfma_f32_16x16x32_bf16 v[116:119], a[12:15], v[240:243], v[116:119]\n\t"
    "s_max_i32 s18, s18, 0\n\t"
    "v_exp_f32 v108, v108\n\t"
    "v_mfma_f32_16x16x32_bf16 v[120:123], a[16:19], v[196:199], v[156:159]\n\t"
    "ds_read_b64_tr_b16 a[46:47], v187 offset:61440\n\t"
    "ds_read_b64_tr_b16 a[48:49], v188 offset:57344\n\t"
    "v_mfma_f32_16x16x32_bf16 v[120:123], a[20:23], v[200:203], v[120:123]\n\t"
    "s_min_i32 s18, s18, 60\n\t"
    "v_exp_f32 v109, v109\n\t"
    "v_mfma_f32_16x16x32_bf16 v[120:123], a[24:27], v[204:207], v[120:123]\n\t"
    "ds_read_b64_tr_b16 a[50:51], v188 offset:61440\n\t"
    "ds_read_b64_tr_b16 a[52:53], v189 offset:57344\n\t"
    "v_mfma_f32_16x16x32_bf16 v[120:123], a[28:31], v[208:211], v[120:123]\n\t"
    "s_lshr_b32 s21, s18, 1\n\t"
    "v_exp_f32 v110, v110\n\t"
    "v_mfma_f32_16x16x32_bf16 v[124:127], a[16:19], v[212:215], v[160:163]\n\t"
    "ds_read_b64_tr_b16 a[54:55], v189 offset:61440\n\t"
    "v_cvt_pk_bf16_f32 v132, v108, v109\n\t"
    "v_mfma_f32_16x16x32_bf16 v[124:127], a[20:23], v[216:219], v[124:127]\n\t"
    "s_add_u32 s39, s39, s21\n\t"
    "v_exp_f32 v111, v111\n\t"
    "v_mfma_f32_16x16x32_bf16 v[124:127], a[24:27], v[220:223], v[124:127]\n\t"
    "ds_read_b64_tr_b16 a[56:57], v190 offset:57344\n\t"
    "ds_read_b64_tr_b16 a[58:59], v190 offset:61440\n\t"
    "v_mfma_f32_16x16x32_bf16 v[124:127], a[28:31], v[224:227], v[124:127]\n\t"
    "v_exp_f32 v112, v112\n\t"
    "v_mfma_f32_16x16x32_bf16 v[128:131], a[16:19], v[228:231], v[164:167]\n\t"
    "ds_read_b64_tr_b16 a[60:61], v191 offset:57344\n\t"
    "v_cvt_pk_bf16_f32 v133, v110, v111\n\t"
    "v_mfma_f32_16x16x32_bf16 v[128:131], a[20:23], v[232:235], v[128:131]\n\t"
    "ds_read_b64_tr_b16 a[62:63], v191 offset:61440\n\t"
    "ds_read_b128 a[0:3], v172 offset:49152\n\t"
    "v_mfma_f32_16x16x32_bf16 v[128:131], a[24:27], v[236:239], v[128:131]\n\t"
    "v_exp_f32 v113, v113\n\t"
    "v_mfma_f32_16x16x32_bf16 v[128:131], a[28:31], v[240:243], v[128:131]\n\t"
    "v_exp_f32 v114, v114\n\t"
    "v_mfma_f32_16x16x32_bf16 v[96:99], a[240:243], v[144:147], v[96:99]\n\t"
    "ds_read_b128 a[4:7], v173 offset:49152\n\t"
    "v_cvt_pk_bf16_f32 v136, v112, v113\n\t"
    "v_mfma_f32_16x16x32_bf16 v[96:99], a[244:247], v[148:151], v[96:99]\n\t"
    "Lw123%=:\n\t"
    "s_sub_u32 s75, s75, 2\n\t"
    "s_cbranch_scc0 Lw123%=\n\t"
    "v_mfma_f32_16x16x32_bf16 v[96:99], a[248:251], v[152:155], v[96:99]\n\t"
    "s_waitcnt lgkmcnt(2)\n\t"
    "v_exp_f32 v115, v115\n\t"
    "v_mfma_f32_16x16x32_bf16 v[64:67], a[32:35], v[152:155], v[64:67]\n\t"
    "buffer_load_dwordx4 v193, s[40:43], s67 offen offset:1024 nt lds\n\t"
    "v_mfma_f32_16x16x32_bf16 v[32:35], a[32:35], v[148:151], v[32:35]\n\t"
    "v_exp_f32 v116, v116\n\t"
    "v_mfma_f32_16x16x32_bf16 v[0:3], a[32:35], v[144:147], v[0:3]\n\t"
    "ds_read_b128 a[8:11], v174 offset:49152\n\t"
    "v_cvt_pk_bf16_f32 v137, v114, v115\n\t"
    "v_mfma_f32_16x16x32_bf16 v[4:7], a[36:39], v[144:147], v[4:7]\n\t"
    "v_exp_f32 v117, v117\n\t"
    "v_mfma_f32_16x16x32_bf16 v[36:39], a[36:39], v[148:151], v[36:39]\n\t"
    "v_exp_f32 v118, v118\n\t"
    "v_mfma_f32_16x16x32_bf16 v[68:71], a[36:39], v[152:155], v[68:71]\n\t"
    "ds_read_b128 a[12:15], v175 offset:49152\n\t"
    "v_cvt_pk_bf16_f32 v140, v116, v117\n\t"
    "v_mfma_f32_16x16x32_bf16 v[72:75], a[40:43], v[152:155], v[72:75]\n\t"
    "v_exp_f32 v119, v119\n\t"
    "v_mfma_f32_16x16x32_bf16 v[40:43], a[40:43], v[148:151], v[40:43]\n\t"
    "s_add_u32 m0, s68, 0x1a000\n\t"
    "v_exp_f32 v120, v120\n\t"
    "v_mfma_f32_16x16x32_bf16 v[8:11], a[40:43], v[144:147], v[8:11]\n\t"
    "ds_read_b128 a[16:19], v172 offset:53248\n\t"
    "v_cvt_pk_bf16_f32 v141, v118, v119\n\t"
    "v_mfma_f32_16x16x32_bf16 v[12:15], a[44:47], v[144:147], v[12:15]\n\t"
    "buffer_load_dwordx4 v194, s[44:47], s67 offen nt lds\n\t"
    "v_mfma_f32_16x16x32_bf16 v[44:47], a[44:47], v[148:151], v[44:47]\n\t"
    "v_exp_f32 v121, v121\n\t"
    "v_mfma_f32_16x16x32_bf16 v[76:79], a[44:47], v[152:155], v[76:79]\n\t"
    "s_memrealtime s[70:71]\n\t"
    "v_exp_f32 v122, v122\n\t"
    "v_mfma_f32_16x16x32_bf16 v[80:83], a[48:51], v[152:155], v[80:83]\n\t"
    "ds_read_b128 a[20:23], v173 offset:53248\n\t"
    "v_cvt_pk_bf16_f32 v134, v120, v121\n\t"
    "v_mfma_f32_16x16x32_bf16 v[48:51], a[48:51], v[148:151], v[48:51]\n\t"
    "v_exp_f32 v123, v123\n\t"
    "v_mfma_f32_16x16x32_bf16 v[16:19], a[48:51], v[144:147], v[16:19]\n\t"
    "v_exp_f32 v124, v124\n\t"
    "v_mfma_f32_16x16x32_bf16 v[20:23], a[52:55], v[144:147], v[20:23]\n\t"
    "ds_read_b128 a[24:27], v174 offset:53248\n\t"
    "v_cvt_pk_bf16_f32 v135, v122, v123\n\t"
    "v_mfma_f32_16x16x32_bf16 v[52:55], a[52:55], v[148:151], v[52:55]\n\t"
    "v_exp_f32 v125, v125\n\t"
    "v_mfma_f32_16x16x32_bf16 v[84:87], a[52:55], v[152:155], v[84:87]\n\t"
    "v_exp_f32 v126, v126\n\t"
    "v_mfma_f32_16x16x32_bf16 v[88:91], a[56:59], v[152:155], v[88:91]\n\t"
    "ds_read_b128 a[28:31], v175 offset:53248\n\t"
    "v_cvt_pk_bf16_f32 v138, v124, v125\n\t"
    "v_mfma_f32_16x16x32_bf16 v[56:59], a[56:59], v[148:151], v[56:59]\n\t"
    "buffer_load_dwordx4 v195, s[44:47], s67 offen offset:1024 nt lds\n\t"
    "v_mfma_f32_16x16x32_bf16 v[24:27], a[56:59], v[144:147], v[24:27]\n\t"
    "s_add_u32 s67, s67, 0x2000\n\t"
    "v_mfma_f32_16x16x32_bf16 v[28:31], a[60:63], v[144:147], v[28:31]\n\t"
    "v_mfma_f32_16x16x32_bf16 v[60:63], a[60:63], v[148:151], v[60:63]\n\t"
    "s_waitcnt vmcnt(12) lgkmcnt(0)\n\t"
    "v_mfma_f32_16x16x32_bf16 v[92:95], a[60:63], v[152:155], v[92:95]\n\t"
    "s_bitcmp1_b32 s20, 3\n\t"
    "s_call_b64 s[36:37], Lcom%=\n\t"
    "s_barrier\n\t"
    "v_mfma_f32_16x16x32_bf16 v[108:111], a[0:3], v[196:199], v[156:159]\n\t"
    "ds_read_b64_tr_b16 a[32:33], v184 offset:57344\n\t"
    "ds_read_b64_tr_b16 a[34:35], v184 offset:61440\n\t"
    "v_mfma_f32_16x16x32_bf16 v[108:111], a[4:7], v[200:203], v[108:111]\n\t"
    "s_sub_u32 s75, s39, s70\n\t"
    "v_exp_f32 v127, v127\n\t"
    "v_mfma_f32_16x16x32_bf16 v[108:111], a[8:11], v[204:207], v[108:111]\n\t"
    "s_max_i32 s39, s75, 0\n\t"
    "v_exp_f32 v128, v128\n\t"
    "v_mfma_f32_16x16x32_bf16 v[108:111], a[12:15], v[208:211], v[108:111]\n\t"
    "s_add_u32 m0, s68, 0x18000\n\t"
    "ds_read_b64_tr_b16 a[36:37], v185 offset:57344\n\t"
    "v_cvt_pk_bf16_f32 v151, v126, v127\n\t"
    "v_mfma_f32_16x16x32_bf16 v[112:115], a[0:3], v[212:215], v[160:163]\n\t"
    "s_sub_u32 s21, s39, s75\n\t"
    "buffer_load_dwordx4 v192, s[40:43], s67 offen nt lds\n\t"
    "v_mfma_f32_16x16x32_bf16 v[112:115], a[4:7], v[216:219], v[112:115]\n\t"
    "s_add_u32 s39, s39, s70\n\t"
    "v_exp_f32 v129, v129\n\t"
    "v_mfma_f32_16x16x32_bf16 v[112:115], a[8:11], v[220:223], v[112:115]\n\t"
    "s_max_i32 s75, s75, 0\n\t"
    "v_exp_f32 v130, v130\n\t"
    "v_mfma_f32_16x16x32_bf16 v[112:115], a[12:15], v[224:227], v[112:115]\n\t"
    "s_min_i32 s21, s21, s77\n\t"
    "ds_read_b64_tr_b16 a[38:39], v185 offset:61440\n\t"
    "v_cvt_pk_bf16_f32 v154, v128, v129\n\t"
    "v_mfma_f32_16x16x32_bf16 v[116:119], a[0:3], v[228:231], v[164:167]\n\t"
    "s_add_u32 s39, s39, s19\n\t"
    "v_exp_f32 v131, v131\n\t"
    "v_mfma_f32_16x16x32_bf16 v[116:119], a[4:7], v[232:235], v[116:119]\n\t"
    "s_add_u32 s18, s18, s21\n\t"
    "ds_read_b64_tr_b16 a[40:41], v186 offset:57344\n\t"
    "v_cvt_pk_bf16_f32 v155, v130, v131\n\t"
    "v_mfma_f32_16x16x32_bf16 v[116:119], a[8:11], v[236:239], v[116:119]\n\t"
    "s_sub_u32 s18, s18, 6\n\t"
    "ds_read_b64_tr_b16 a[42:43], v186 offset:61440\n\t"
    "ds_read_b64_tr_b16 a[44:45], v187 offset:57344\n\t"
    "v_mfma_f32_16x16x32_bf16 v[116:119], a[12:15], v[240:243], v[116:119]\n\t"
    "s_max_i32 s18, s18, 0\n\t"
    "v_exp_f32 v108, v108\n\t"
    "v_mfma_f32_16x16x32_bf16 v[120:123], a[16:19], v[196:199], v[156:159]\n\t"
    "ds_read_b64_tr_b16 a[46:47], v187 offset:61440\n\t"
    "ds_read_b64_tr_b16 a[48:49], v188 offset:57344\n\t"
    "v_mfma_f32_16x16x32_bf16 v[120:123], a[20:23], v[200:203], v[120:123]\n\t"
    "s_min_i32 s18, s18, 60\n\t"
    "v_exp_f32 v109, v109\n\t"
    "v_mfma_f32_16x16x32_bf16 v[120:123], a[24:27], v[204:207], v[120:123]\n\t"
    "ds_read_b64_tr_b16 a[50:51], v188 offset:61440\n\t"
    "ds_read_b64_tr_b16 a[52:53], v189 offset:57344\n\t"
    "v_mfma_f32_16x16x32_bf16 v[120:123], a[28:31], v[208:211], v[120:123]\n\t"
    "s_lshr_b32 s21, s18, 1\n\t"
    "v_exp_f32 v110, v110\n\t"
    "v_mfma_f32_16x16x32_bf16 v[124:127], a[16:19], v[212:215], v[160:163]\n\t"
    "ds_read_b64_tr_b16 a[54:55], v189 offset:61440\n\t"
    "v_cvt_pk_bf16_f32 v132, v108, v109\n\t"
    "v_mfma_f32_16x16x32_bf16 v[124:127], a[20:23], v[216:219], v[124:127]\n\t"
    "s_add_u32 s39, s39, s21\n\t"
    "v_exp_f32 v111, v111\n\t"
    "v_mfma_f32_16x16x32_bf16 v[124:127], a[24:27], v[220:223], v[124:127]\n\t"
    "ds_read_b64_tr_b16 a[56:57], v190 offset:57344\n\t"
    "ds_read_b64_tr_b16 a[58:59], v190 offset:61440\n\t"
    "v_mfma_f32_16x16x32_bf16 v[124:127], a[28:31], v[224:227], v[124:127]\n\t"
    "v_exp_f32 v112, v112\n\t"
    "v_mfma_f32_16x16x32_bf16 v[128:131], a[16:19], v[228:231], v[164:167]\n\t"
    "ds_read_b64_tr_b16 a[60:61], v191 offset:57344\n\t"
    "v_cvt_pk_bf16_f32 v133, v110, v111\n\t"
    "v_mfma_f32_16x16x32_bf16 v[128:131], a[20:23], v[232:235], v[128:131]\n\t"
    "ds_read_b64_tr_b16 a[62:63], v191 offset:61440\n\t"
    "ds_read_b128 a[0:3], v172 offset:49152\n\t"
    "v_mfma_f32_16x16x32_bf16 v[128:131], a[24:27], v[236:239], v[128:131]\n\t"
    "v_exp_f32 v113, v113\n\t"
    "v_mfma_f32_16x16x32_bf16 v[128:131], a[28:31], v[240:243], v[128:131]\n\t"
    "v_exp_f32 v114, v114\n\t"
    "v_mfma_f32_16x16x32_bf16 v[96:99], a[240:243], v[144:147], v[96:99]\n\t"
    "ds_read_b128 a[4:7], v173 offset:49152\n\t"
    "v_cvt_pk_bf16_f32 v136, v112, v113\n\t"
    "v_mfma_f32_16x16x32_bf16 v[96:99], a[244:247], v[148:151], v[96:99]\n\t"
    "Lw131%=:\n\t"
    "s_sub_u32 s75, s75, 2\n\t"
    "s_cbranch_scc0 Lw131%=\n\t"
    "v_mfma_f32_16x16x32_bf16 v[96:99], a[248:251], v[152:155], v[96:99]\n\t"
    "s_waitcnt lgkmcnt(2)\n\t"
    "v_exp_f32 v115, v115\n\t"
    "v_mfma_f32_16x16x32_bf16 v[64:67], a[32:35], v[152:155], v[64:67]\n\t"
    "buffer_load_dwordx4 v193, s[40:43], s67 offen offset:1024 nt lds\n\t"
    "v_mfma_f32_16x16x32_bf16 v[32:35], a[32:35], v[148:151], v[32:35]\n\t"
    "v_exp_f32 v116, v116\n\t"
    "v_mfma_f32_16x16x32_bf16 v[0:3], a[32:35], v[144:147], v[0:3]\n\t"
    "ds_read_b128 a[8:11], v174 offset:49152\n\t"
    "v_cvt_pk_bf16_f32 v137, v114, v115\n\t"
    "v_mfma_f32_16x16x32_bf16 v[4:7], a[36:39], v[144:147], v[4:7]\n\t"
    "v_exp_f32 v117, v117\n\t"
    "v_mfma_f32_16x16x32_bf16 v[36:39], a[36:39], v[148:151], v[36:39]\n\t"
    "v_exp_f32 v118, v118\n\t"
    "v_mfma_f32_16x16x32_bf16 v[68:71], a[36:39], v[152:155], v[68:71]\n\t"
    "ds_read_b128 a[12:15], v175 offset:49152\n\t"
    "v_cvt_pk_bf16_f32 v140, v116, v117\n\t"
    "v_mfma_f32_16x16x32_bf16 v[72:75], a[40:43], v[152:155], v[72:75]\n\t"
    "v_exp_f32 v119, v119\n\t"
    "v_mfma_f32_16x16x32_bf16 v[40:43], a[40:43], v[148:151], v[40:43]\n\t"
    "s_add_u32 m0, s68, 0x1a000\n\t"
    "v_exp_f32 v120, v120\n\t"
    "v_mfma_f32_16x16x32_bf16 v[8:11], a[40:43], v[144:147], v[8:11]\n\t"
    "ds_read_b128 a[16:19], v172 offset:53248\n\t"
    "v_cvt_pk_bf16_f32 v141, v118, v119\n\t"
    "v_mfma_f32_16x16x32_bf16 v[12:15], a[44:47], v[144:147], v[12:15]\n\t"
    "buffer_load_dwordx4 v194, s[44:47], s67 offen nt lds\n\t"
    "v_mfma_f32_16x16x32_bf16 v[44:47], a[44:47], v[148:151], v[44:47]\n\t"
    "v_exp_f32 v121, v121\n\t"
    "v_mfma_f32_16x16x32_bf16 v[76:79], a[44:47], v[152:155], v[76:79]\n\t"
    "s_memrealtime s[70:71]\n\t"
    "v_exp_f32 v122, v122\n\t"
    "v_mfma_f32_16x16x32_bf16 v[80:83], a[48:51], v[152:155], v[80:83]\n\t"
    "ds_read_b128 a[20:23], v173 offset:53248\n\t"
    "v_cvt_pk_bf16_f32 v134, v120, v121\n\t"
    "v_mfma_f32_16x16x32_bf16 v[48:51], a[48:51], v[148:151], v[48:51]\n\t"
    "v_exp_f32 v123, v123\n\t"
    "v_mfma_f32_16x16x32_bf16 v[16:19], a[48:51], v[144:147], v[16:19]\n\t"
    "v_exp_f32 v124, v124\n\t"
    "v_mfma_f32_16x16x32_bf16 v[20:23], a[52:55], v[144:147], v[20:23]\n\t"
    "ds_read_b128 a[24:27], v174 offset:53248\n\t"
    "v_cvt_pk_bf16_f32 v135, v122, v123\n\t"
    "v_mfma_f32_16x16x32_bf16 v[52:55], a[52:55], v[148:151], v[52:55]\n\t"
    "v_exp_f32 v125, v125\n\t"
    "v_mfma_f32_16x16x32_bf16 v[84:87], a[52:55], v[152:155], v[84:87]\n\t"
    "v_exp_f32 v126, v126\n\t"
    "v_mfma_f32_16x16x32_bf16 v[88:91], a[56:59], v[152:155], v[88:91]\n\t"
    "ds_read_b128 a[28:31], v175 offset:53248\n\t"
    "v_cvt_pk_bf16_f32 v138, v124, v125\n\t"
    "v_mfma_f32_16x16x32_bf16 v[56:59], a[56:59], v[148:151], v[56:59]\n\t"
    "buffer_load_dwordx4 v195, s[44:47], s67 offen offset:1024 nt lds\n\t"
    "v_mfma_f32_16x16x32_bf16 v[24:27], a[56:59], v[144:147], v[24:27]\n\t"
    "s_add_u32 s67, s67, 0x2000\n\t"
    "v_mfma_f32_16x16x32_bf16 v[28:31], a[60:63], v[144:147], v[28:31]\n\t"
    "v_mfma_f32_16x16x32_bf16 v[60:63], a[60:63], v[148:151], v[60:63]\n\t"
    "s_waitcnt vmcnt(12) lgkmcnt(0)\n\t"
    "v_mfma_f32_16x16x32_bf16 v[92:95], a[60:63], v[152:155], v[92:95]\n\t"
    "s_bitcmp1_b32 s20, 4\n\t"
    "s_call_b64 s[36:37], Lcom%=\n\t"
    "s_barrier\n\t"
    "v_mfma_f32_16x16x32_bf16 v[108:111], a[0:3], v[196:199], v[156:159]\n\t"
    "ds_read_b64_tr_b16 a[32:33], v184 offset:57344\n\t"
    "ds_read_b64_tr_b16 a[34:35], v184 offset:61440\n\t"
    "v_mfma_f32_16x16x32_bf16 v[108:111], a[4:7], v[200:203], v[108:111]\n\t"
    "s_sub_u32 s75, s39, s70\n\t"
    "v_exp_f32 v127, v127\n\t"
    "v_mfma_f32_16x16x32_bf16 v[108:111], a[8:11], v[204:207], v[108:111]\n\t"
    "s_max_i32 s39, s75, 0\n\t"
    "v_exp_f32 v128, v128\n\t"
    "v_mfma_f32_16x16x32_bf16 v[108:111], a[12:15], v[208:211], v[108:111]\n\t"
    "s_add_u32 m0, s68, 0x18000\n\t"
    "ds_read_b64_tr_b16 a[36:37], v185 offset:57344\n\t"
    "v_cvt_pk_bf16_f32 v151, v126, v127\n\t"
    "v_mfma_f32_16x16x32_bf16 v[112:115], a[0:3], v[212:215], v[160:163]\n\t"
    "s_sub_u32 s21, s39, s75\n\t"
    "buffer_load_dwordx4 v192, s[40:43], s67 offen nt lds\n\t"
    "v_mfma_f32_16x16x32_bf16 v[112:115], a[4:7], v[216:219], v[112:115]\n\t"
    "s_add_u32 s39, s39, s70\n\t"
    "v_exp_f32 v129, v129\n\t"
    "v_mfma_f32_16x16x32_bf16 v[112:115], a[8:11], v[220:223], v[112:115]\n\t"
    "s_max_i32 s75, s75, 0\n\t"
    "v_exp_f32 v130, v130\n\t"
    "v_mfma_f32_16x16x32_bf16 v[112:115], a[12:15], v[224:227], v[112:115]\n\t"
    "s_min_i32 s21, s21, s77\n\t"
    "ds_read_b64_tr_b16 a[38:39], v185 offset:61440\n\t"
    "v_cvt_pk_bf16_f32 v154, v128, v129\n\t"
    "v_mfma_f32_16x16x32_bf16 v[116:119], a[0:3], v[228:231], v[164:167]\n\t"
    "s_add_u32 s39, s39, s19\n\t"
    "v_exp_f32 v131, v131\n\t"
    "v_mfma_f32_16x16x32_bf16 v[116:119], a[4:7], v[232:235], v[116:119]\n\t"
    "s_add_u32 s18, s18, s21\n\t"
    "ds_read_b64_tr_b16 a[40:41], v186 offset:57344\n\t"
    "v_cvt_pk_bf16_f32 v155, v130, v131\n\t"
    "v_mfma_f32_16x16x32_bf16 v[116:119], a[8:11], v[236:239], v[116:119]\n\t"
    "s_sub_u32 s18, s18, 6\n\t"
    "ds_read_b64_tr_b16 a[42:43], v186 offset:61440\n\t"
    "ds_read_b64_tr_b16 a[44:45], v187 offset:57344\n\t"
    "v_mfma_f32_16x16x32_bf16 v[116:119], a[12:15], v[240:243], v[116:119]\n\t"
    "s_max_i32 s18, s18, 0\n\t"
    "v_exp_f32 v108, v108\n\t"
    "v_mfma_f32_16x16x32_bf16 v[120:123], a[16:19], v[196:199], v[156:159]\n\t"
    "ds_read_b64_tr_b16 a[46:47], v187 offset:61440\n\t"
    "ds_read_b64_tr_b16 a[48:49], v188 offset:57344\n\t"
    "v_mfma_f32_16x16x32_bf16 v[120:123], a[20:23], v[200:203], v[120:123]\n\t"
    "s_min_i32 s18, s18, 60\n\t"
    "v_exp_f32 v109, v109\n\t"
    "v_mfma_f32_16x16x32_bf16 v[120:123], a[24:27], v[204:207], v[120:123]\n\t"
    "ds_read_b64_tr_b16 a[50:51], v188 offset:61440\n\t"
    "ds_read_b64_tr_b16 a[52:53], v189 offset:57344\n\t"
    "v_mfma_f32_16x16x32_bf16 v[120:123], a[28:31], v[208:211], v[120:123]\n\t"
    "s_lshr_b32 s21, s18, 1\n\t"
    "v_exp_f32 v110, v110\n\t"
    "v_mfma_f32_16x16x32_bf16 v[124:127], a[16:19], v[212:215], v[160:163]\n\t"
    "ds_read_b64_tr_b16 a[54:55], v189 offset:61440\n\t"
    "v_cvt_pk_bf16_f32 v132, v108, v109\n\t"
    "v_mfma_f32_16x16x32_bf16 v[124:127], a[20:23], v[216:219], v[124:127]\n\t"
    "s_add_u32 s39, s39, s21\n\t"
    "v_exp_f32 v111, v111\n\t"
    "v_mfma_f32_16x16x32_bf16 v[124:127], a[24:27], v[220:223], v[124:127]\n\t"
    "ds_read_b64_tr_b16 a[56:57], v190 offset:57344\n\t"
    "ds_read_b64_tr_b16 a[58:59], v190 offset:61440\n\t"
    "v_mfma_f32_16x16x32_bf16 v[124:127], a[28:31], v[224:227], v[124:127]\n\t"
    "v_exp_f32 v112, v112\n\t"
    "v_mfma_f32_16x16x32_bf16 v[128:131], a[16:19], v[228:231], v[164:167]\n\t"
    "ds_read_b64_tr_b16 a[60:61], v191 offset:57344\n\t"
    "v_cvt_pk_bf16_f32 v133, v110, v111\n\t"
    "v_mfma_f32_16x16x32_bf16 v[128:131], a[20:23], v[232:235], v[128:131]\n\t"
    "ds_read_b64_tr_b16 a[62:63], v191 offset:61440\n\t"
    "ds_read_b128 a[0:3], v172 offset:49152\n\t"
    "v_mfma_f32_16x16x32_bf16 v[128:131], a[24:27], v[236:239], v[128:131]\n\t"
    "v_exp_f32 v113, v113\n\t"
    "v_mfma_f32_16x16x32_bf16 v[128:131], a[28:31], v[240:243], v[128:131]\n\t"
    "v_exp_f32 v114, v114\n\t"
    "v_mfma_f32_16x16x32_bf16 v[96:99], a[240:243], v[144:147], v[96:99]\n\t"
    "ds_read_b128 a[4:7], v173 offset:49152\n\t"
    "v_cvt_pk_bf16_f32 v136, v112, v113\n\t"
    "v_mfma_f32_16x16x32_bf16 v[96:99], a[244:247], v[148:151], v[96:99]\n\t"
    "Lw139%=:\n\t"
    "s_sub_u32 s75, s75, 2\n\t"
    "s_cbranch_scc0 Lw139%=\n\t"
    "v_mfma_f32_16x16x32_bf16 v[96:99], a[248:251], v[152:155], v[96:99]\n\t"
    "s_waitcnt lgkmcnt(2)\n\t"
    "v_exp_f32 v115, v115\n\t"
    "v_mfma_f32_16x16x32_bf16 v[64:67], a[32:35], v[152:155], v[64:67]\n\t"
    "buffer_load_dwordx4 v193, s[40:43], s67 offen offset:1024 nt lds\n\t"
    "v_mfma_f32_16x16x32_bf16 v[32:35], a[32:35], v[148:151], v[32:35]\n\t"
    "v_exp_f32 v116, v116\n\t"
    "v_mfma_f32_16x16x32_bf16 v[0:3], a[32:35], v[144:147], v[0:3]\n\t"
    "ds_read_b128 a[8:11], v174 offset:49152\n\t"
    "v_cvt_pk_bf16_f32 v137, v114, v115\n\t"
    "v_mfma_f32_16x16x32_bf16 v[4:7], a[36:39], v[144:147], v[4:7]\n\t"
    "v_exp_f32 v117, v117\n\t"
    "v_mfma_f32_16x16x32_bf16 v[36:39], a[36:39], v[148:151], v[36:39]\n\t"
    "v_exp_f32 v118, v118\n\t"
    "v_mfma_f32_16x16x32_bf16 v[68:71], a[36:39], v[152:155], v[68:71]\n\t"
    "ds_read_b128 a[12:15], v175 offset:49152\n\t"
    "v_cvt_pk_bf16_f32 v140, v116, v117\n\t"
    "v_mfma_f32_16x16x32_bf16 v[72:75], a[40:43], v[152:155], v[72:75]\n\t"
    "v_exp_f32 v119, v119\n\t"
    "v_mfma_f32_16x16x32_bf16 v[40:43], a[40:43], v[148:151], v[40:43]\n\t"
    "s_add_u32 m0, s68, 0x1a000\n\t"
    "v_exp_f32 v120, v120\n\t"
    "v_mfma_f32_16x16x32_bf16 v[8:11], a[40:43], v[144:147], v[8:11]\n\t"
    "ds_read_b128 a[16:19], v172 offset:53248\n\t"
    "v_cvt_pk_bf16_f32 v141, v118, v119\n\t"
    "v_mfma_f32_16x16x32_bf16 v[12:15], a[44:47], v[144:147], v[12:15]\n\t"
    "buffer_load_dwordx4 v194, s[44:47], s67 offen nt lds\n\t"
    "v_mfma_f32_16x16x32_bf16 v[44:47], a[44:47], v[148:151], v[44:47]\n\t"
    "v_exp_f32 v121, v121\n\t"
    "v_mfma_f32_16x16x32_bf16 v[76:79], a[44:47], v[152:155], v[76:79]\n\t"
    "s_memrealtime s[70:71]\n\t"
    "v_exp_f32 v122, v122\n\t"
    "v_mfma_f32_16x16x32_bf16 v[80:83], a[48:51], v[152:155], v[80:83]\n\t"
    "ds_read_b128 a[20:23], v173 offset:53248\n\t"
    "v_cvt_pk_bf16_f32 v134, v120, v121\n\t"
    "v_mfma_f32_16x16x32_bf16 v[48:51], a[48:51], v[148:151], v[48:51]\n\t"
    "v_exp_f32 v123, v123\n\t"
    "v_mfma_f32_16x16x32_bf16 v[16:19], a[48:51], v[144:147], v[16:19]\n\t"
    "v_exp_f32 v124, v124\n\t"
    "v_mfma_f32_16x16x32_bf16 v[20:23], a[52:55], v[144:147], v[20:23]\n\t"
    "ds_read_b128 a[24:27], v174 offset:53248\n\t"
    "v_cvt_pk_bf16_f32 v135, v122, v123\n\t"
    "v_mfma_f32_16x16x32_bf16 v[52:55], a[52:55], v[148:151], v[52:55]\n\t"
    "v_exp_f32 v125, v125\n\t"
    "v_mfma_f32_16x16x32_bf16 v[84:87], a[52:55], v[152:155], v[84:87]\n\t"
    "v_exp_f32 v126, v126\n\t"
    "v_mfma_f32_16x16x32_bf16 v[88:91], a[56:59], v[152:155], v[88:91]\n\t"
    "ds_read_b128 a[28:31], v175 offset:53248\n\t"
    "v_cvt_pk_bf16_f32 v138, v124, v125\n\t"
    "v_mfma_f32_16x16x32_bf16 v[56:59], a[56:59], v[148:151], v[56:59]\n\t"
    "buffer_load_dwordx4 v195, s[44:47], s67 offen offset:1024 nt lds\n\t"
    "v_mfma_f32_16x16x32_bf16 v[24:27], a[56:59], v[144:147], v[24:27]\n\t"
    "s_add_u32 s67, s67, 0x2000\n\t"
    "v_mfma_f32_16x16x32_bf16 v[28:31], a[60:63], v[144:147], v[28:31]\n\t"
    "v_mfma_f32_16x16x32_bf16 v[60:63], a[60:63], v[148:151], v[60:63]\n\t"
    "s_waitcnt vmcnt(12) lgkmcnt(0)\n\t"
    "v_mfma_f32_16x16x32_bf16 v[92:95], a[60:63], v[152:155], v[92:95]\n\t"
    "s_bitcmp1_b32 s20, 5\n\t"
    "s_call_b64 s[36:37], Lcom%=\n\t"
    "s_barrier\n\t"
    "v_mfma_f32_16x16x32_bf16 v[108:111], a[0:3], v[196:199], v[156:159]\n\t"
    "ds_read_b64_tr_b16 a[32:33], v184 offset:57344\n\t"
    "ds_read_b64_tr_b16 a[34:35], v184 offset:61440\n\t"
    "v_mfma_f32_16x16x32_bf16 v[108:111], a[4:7], v[200:203], v[108:111]\n\t"
    "s_sub_u32 s75, s39, s70\n\t"
    "v_exp_f32 v127, v127\n\t"
    "v_mfma_f32_16x16x32_bf16 v[108:111], a[8:11], v[204:207], v[108:111]\n\t"
    "s_max_i32 s39, s75, 0\n\t"
    "v_exp_f32 v128, v128\n\t"
    "v_mfma_f32_16x16x32_bf16 v[108:111], a[12:15], v[208:211], v[108:111]\n\t"
    "s_add_u32 m0, s68, 0x18000\n\t"
    "ds_read_b64_tr_b16 a[36:37], v185 offset:57344\n\t"
    "v_cvt_pk_bf16_f32 v151, v126, v127\n\t"
    "v_mfma_f32_16x16x32_bf16 v[112:115], a[0:3], v[212:215], v[160:163]\n\t"
    "s_sub_u32 s21, s39, s75\n\t"
    "buffer_load_dwordx4 v192, s[40:43], s67 offen nt lds\n\t"
    "v_mfma_f32_16x16x32_bf16 v[112:115], a[4:7], v[216:219], v[112:115]\n\t"
    "s_add_u32 s39, s39, s70\n\t"
    "v_exp_f32 v129, v129\n\t"
    "v_mfma_f32_16x16x32_bf16 v[112:115], a[8:11], v[220:223], v[112:115]\n\t"
    "s_max_i32 s75, s75, 0\n\t"
    "v_exp_f32 v130, v130\n\t"
    "v_mfma_f32_16x16x32_bf16 v[112:115], a[12:15], v[224:227], v[112:115]\n\t"
    "s_min_i32 s21, s21, s77\n\t"
    "ds_read_b64_tr_b16 a[38:39], v185 offset:61440\n\t"
    "v_cvt_pk_bf16_f32 v154, v128, v129\n\t"
    "v_mfma_f32_16x16x32_bf16 v[116:119], a[0:3], v[228:231], v[164:167]\n\t"
    "s_add_u32 s39, s39, s19\n\t"
    "v_exp_f32 v131, v131\n\t"
    "v_mfma_f32_16x16x32_bf16 v[116:119], a[4:7], v[232:235], v[116:119]\n\t"
    "s_add_u32 s18, s18, s21\n\t"
    "ds_read_b64_tr_b16 a[40:41], v186 offset:57344\n\t"
    "v_cvt_pk_bf16_f32 v155, v130, v131\n\t"
    "v_mfma_f32_16x16x32_bf16 v[116:119], a[8:11], v[236:239], v[116:119]\n\t"
    "s_sub_u32 s18, s18, 6\n\t"
    "ds_read_b64_tr_b16 a[42:43], v186 offset:61440\n\t"
    "ds_read_b64_tr_b16 a[44:45], v187 offset:57344\n\t"
    "v_mfma_f32_16x16x32_bf16 v[116:119], a[12:15], v[240:243], v[116:119]\n\t"
    "s_max_i32 s18, s18, 0\n\t"
    "v_exp_f32 v108, v108\n\t"
    "v_mfma_f32_16x16x32_bf16 v[120:123], a[16:19], v[196:199], v[156:159]\n\t"
    "ds_read_b64_tr_b16 a[46:47], v187 offset:61440\n\t"
    "ds_read_b64_tr_b16 a[48:49], v188 offset:57344\n\t"
    "v_mfma_f32_16x16x32_bf16 v[120:123], a[20:23], v[200:203], v[120:123]\n\t"
    "s_min_i32 s18, s18, 60\n\t"
    "v_exp_f32 v109, v109\n\t"
    "v_mfma_f32_16x16x32_bf16 v[120:123], a[24:27], v[204:207], v[120:123]\n\t"
    "ds_read_b64_tr_b16 a[50:51], v188 offset:61440\n\t"
    "ds_read_b64_tr_b16 a[52:53], v189 offset:57344\n\t"
    "v_mfma_f32_16x16x32_bf16 v[120:123], a[28:31], v[208:211], v[120:123]\n\t"
    "s_lshr_b32 s21, s18, 1\n\t"
    "v_exp_f32 v110, v110\n\t"
    "v_mfma_f32_16x16x32_bf16 v[124:127], a[16:19], v[212:215], v[160:163]\n\t"
    "ds_read_b64_tr_b16 a[54:55], v189 offset:61440\n\t"
    "v_cvt_pk_bf16_f32 v132, v108, v109\n\t"
    "v_mfma_f32_16x16x32_bf16 v[124:127], a[20:23], v[216:219], v[124:127]\n\t"
    "s_add_u32 s39, s39, s21\n\t"
    "v_exp_f32 v111, v111\n\t"
    "v_mfma_f32_16x16x32_bf16 v[124:127], a[24:27], v[220:223], v[124:127]\n\t"
    "ds_read_b64_tr_b16 a[56:57], v190 offset:57344\n\t"
    "ds_read_b64_tr_b16 a[58:59], v190 offset:61440\n\t"
    "v_mfma_f32_16x16x32_bf16 v[124:127], a[28:31], v[224:227], v[124:127]\n\t"
    "v_exp_f32 v112, v112\n\t"
    "v_mfma_f32_16x16x32_bf16 v[128:131], a[16:19], v[228:231], v[164:167]\n\t"
    "ds_read_b64_tr_b16 a[60:61], v191 offset:57344\n\t"
    "v_cvt_pk_bf16_f32 v133, v110, v111\n\t"
    "v_mfma_f32_16x16x32_bf16 v[128:131], a[20:23], v[232:235], v[128:131]\n\t"
    "ds_read_b64_tr_b16 a[62:63], v191 offset:61440\n\t"
    "ds_read_b128 a[0:3], v172 offset:49152\n\t"
    "v_mfma_f32_16x16x32_bf16 v[128:131], a[24:27], v[236:239], v[128:131]\n\t"
    "v_exp_f32 v113, v113\n\t"
    "v_mfma_f32_16x16x32_bf16 v[128:131], a[28:31], v[240:243], v[128:131]\n\t"
    "v_exp_f32 v114, v114\n\t"
    "v_mfma_f32_16x16x32_bf16 v[96:99], a[240:243], v[144:147], v[96:99]\n\t"
    "ds_read_b128 a[4:7], v173 offset:49152\n\t"
    "v_cvt_pk_bf16_f32 v136, v112, v113\n\t"
    "v_mfma_f32_16x16x32_bf16 v[96:99], a[244:247], v[148:151], v[96:99]\n\t"
    "Lw147%=:\n\t"
    "s_sub_u32 s75, s75, 2\n\t"
    "s_cbranch_scc0 Lw147%=\n\t"
    "v_mfma_f32_16x16x32_bf16 v[96:99], a[248:251], v[152:155], v[96:99]\n\t"
    "s_waitcnt lgkmcnt(2)\n\t"
    "v_exp_f32 v115, v115\n\t"
    "v_mfma_f32_16x16x32_bf16 v[64:67], a[32:35], v[152:155], v[64:67]\n\t"
    "buffer_load_dwordx4 v193, s[40:43], s67 offen offset:1024 nt lds\n\t"
    "v_mfma_f32_16x16x32_bf16 v[32:35], a[32:35], v[148:151], v[32:35]\n\t"
    "v_exp_f32 v116, v116\n\t"
    "v_mfma_f32_16x16x32_bf16 v[0:3], a[32:35], v[144:147], v[0:3]\n\t"
    "ds_read_b128 a[8:11], v174 offset:49152\n\t"
    "v_cvt_pk_bf16_f32 v137, v114, v115\n\t"
    "v_mfma_f32_16x16x32_bf16 v[4:7], a[36:39], v[144:147], v[4:7]\n\t"
    "v_exp_f32 v117, v117\n\t"
    "v_mfma_f32_16x16x32_bf16 v[36:39], a[36:39], v[148:151], v[36:39]\n\t"
    "v_exp_f32 v118, v118\n\t"
    "v_mfma_f32_16x16x32_bf16 v[68:71], a[36:39], v[152:155], v[68:71]\n\t"
    "ds_read_b128 a[12:15], v175 offset:49152\n\t"
    "v_cvt_pk_bf16_f32 v140, v116, v117\n\t"
    "v_mfma_f32_16x16x32_bf16 v[72:75], a[40:43], v[152:155], v[72:75]\n\t"
    "v_exp_f32 v119, v119\n\t"
    "v_mfma_f32_16x16x32_bf16 v[40:43], a[40:43], v[148:151], v[40:43]\n\t"
    "s_add_u32 m0, s68, 0x1a000\n\t"
    "v_exp_f32 v120, v120\n\t"
    "v_mfma_f32_16x16x32_bf16 v[8:11], a[40:43], v[144:147], v[8:11]\n\t"
    "ds_read_b128 a[16:19], v172 offset:53248\n\t"
    "v_cvt_pk_bf16_f32 v141, v118, v119\n\t"
    "v_mfma_f32_16x16x32_bf16 v[12:15], a[44:47], v[144:147], v[12:15]\n\t"
    "buffer_load_dwordx4 v194, s[44:47], s67 offen nt lds\n\t"
    "v_mfma_f32_16x16x32_bf16 v[44:47], a[44:47], v[148:151], v[44:47]\n\t"
    "v_exp_f32 v121, v121\n\t"
    "v_mfma_f32_16x16x32_bf16 v[76:79], a[44:47], v[152:155], v[76:79]\n\t"
    "s_memrealtime s[70:71]\n\t"
    "v_exp_f32 v122, v122\n\t"
    "v_mfma_f32_16x16x32_bf16 v[80:83], a[48:51], v[152:155], v[80:83]\n\t"
    "ds_read_b128 a[20:23], v173 offset:53248\n\t"
    "v_cvt_pk_bf16_f32 v134, v120, v121\n\t"
    "v_mfma_f32_16x16x32_bf16 v[48:51], a[48:51], v[148:151], v[48:51]\n\t"
    "v_exp_f32 v123, v123\n\t"
    "v_mfma_f32_16x16x32_bf16 v[16:19], a[48:51], v[144:147], v[16:19]\n\t"
    "v_exp_f32 v124, v124\n\t"
    "v_mfma_f32_16x16x32_bf16 v[20:23], a[52:55], v[144:147], v[20:23]\n\t"
    "ds_read_b128 a[24:27], v174 offset:53248\n\t"
    "v_cvt_pk_bf16_f32 v135, v122, v123\n\t"
    "v_mfma_f32_16x16x32_bf16 v[52:55], a[52:55], v[148:151], v[52:55]\n\t"
    "v_exp_f32 v125, v125\n\t"
    "v_mfma_f32_16x16x32_bf16 v[84:87], a[52:55], v[152:155], v[84:87]\n\t"
    "v_exp_f32 v126, v126\n\t"
    "v_mfma_f32_16x16x32_bf16 v[88:91], a[56:59], v[152:155], v[88:91]\n\t"
    "ds_read_b128 a[28:31], v175 offset:53248\n\t"
    "v_cvt_pk_bf16_f32 v138, v124, v125\n\t"
    "v_mfma_f32_16x16x32_bf16 v[56:59], a[56:59], v[148:151], v[56:59]\n\t"
    "buffer_load_dwordx4 v195, s[44:47], s67 offen offset:1024 nt lds\n\t"
    "v_mfma_f32_16x16x32_bf16 v[24:27], a[56:59], v[144:147], v[24:27]\n\t"
    "s_add_u32 s67, s67, 0x2000\n\t"
    "v_mfma_f32_16x16x32_bf16 v[28:31], a[60:63], v[144:147], v[28:31]\n\t"
    "v_mfma_f32_16x16x32_bf16 v[60:63], a[60:63], v[148:151], v[60:63]\n\t"
    "s_waitcnt vmcnt(12) lgkmcnt(0)\n\t"
    "v_mfma_f32_16x16x32_bf16 v[92:95], a[60:63], v[152:155], v[92:95]\n\t"
    "s_bitcmp1_b32 s20, 6\n\t"
    "s_call_b64 s[36:37], Lcom%=\n\t"
    "s_barrier\n\t"
    "v_mfma_f32_16x16x32_bf16 v[108:111], a[0:3], v[196:199], v[156:159]\n\t"
    "ds_read_b64_tr_b16 a[32:33], v184 offset:57344\n\t"
    "ds_read_b64_tr_b16 a[34:35], v184 offset:61440\n\t"
    "v_mfma_f32_16x16x32_bf16 v[108:111], a[4:7], v[200:203], v[108:111]\n\t"
    "s_add_u32 m0, s72, 0x24000\n\t"
    "v_exp_f32 v127, v127\n\t"
    "v_mfma_f32_16x16x32_bf16 v[108:111], a[8:11], v[204:207], v[108:111]\n\t"
    "ds_read_b64_tr_b16 a[36:37], v185 offset:57344\n\t"
    "ds_read_b64_tr_b16 a[38:39], v185 offset:61440\n\t"
    "v_mfma_f32_16x16x32_bf16 v[108:111], a[12:15], v[208:211], v[108:111]\n\t"
    "buffer_load_dwordx4 v104, s[52:55], s32 offen lds\n\t"
    "v_mfma_f32_16x16x32_bf16 v[112:115], a[0:3], v[212:215], v[160:163]\n\t"
    "v_exp_f32 v128, v128\n\t"
    "v_mfma_f32_16x16x32_bf16 v[112:115], a[4:7], v[216:219], v[112:115]\n\t"
    "ds_read_b64_tr_b16 a[40:41], v186 offset:57344\n\t"
    "v_cvt_pk_bf16_f32 v151, v126, v127\n\t"
    "v_mfma_f32_16x16x32_bf16 v[112:115], a[8:11], v[220:223], v[112:115]\n\t"
    "v_exp_f32 v129, v129\n\t"
    "v_mfma_f32_16x16x32_bf16 v[112:115], a[12:15], v[224:227], v[112:115]\n\t"
    "ds_read_b64_tr_b16 a[42:43], v186 offset:61440\n\t"
    "v_cvt_pk_bf16_f32 v154, v128, v129\n\t"
    "v_mfma_f32_16x16x32_bf16 v[116:119], a[0:3], v[228:231], v[164:167]\n\t"
    "v_exp_f32 v130, v130\n\t"
    "v_mfma_f32_16x16x32_bf16 v[116:119], a[4:7], v[232:235], v[116:119]\n\t"
    "ds_read_b64_tr_b16 a[44:45], v187 offset:57344\n\t"
    "s_branch Lcs5%=\n\t"
    ".p2align 12\n\t"
    "Lc5%=:\n\t"
    "s_setpc_b64 s[22:23]\n\t"
    "Lcs5%=:\n\t"
    "ds_read_b64_tr_b16 a[46:47], v187 offset:61440\n\t"
    "v_mfma_f32_16x16x32_bf16 v[116:119], a[8:11], v[236:239], v[116:119]\n\t"
    "buffer_load_dwordx4 v105, s[52:55], s32 offen offset:1024 lds\n\t"
    "v_mfma_f32_16x16x32_bf16 v[116:119], a[12:15], v[240:243], v[116:119]\n\t"
    "v_exp_f32 v131, v131\n\t"
    "v_mfma_f32_16x16x32_bf16 v[120:123], a[16:19], v[196:199], v[156:159]\n\t"
    "ds_read_b64_tr_b16 a[48:49], v188 offset:57344\n\t"
    "v_cvt_pk_bf16_f32 v155, v130, v131\n\t"
    "v_mfma_f32_16x16x32_bf16 v[120:123], a[20:23], v[200:203], v[120:123]\n\t"
    "ds_read_b64_tr_b16 a[50:51], v188 offset:61440\n\t"
    "ds_read_b64_tr_b16 a[52:53], v189 offset:57344\n\t"
    "v_mfma_f32_16x16x32_bf16 v[120:123], a[24:27], v[204:207], v[120:123]\n\t"
    "v_exp_f32 v108, v108\n\t"
    "v_mfma_f32_16x16x32_bf16 v[120:123], a[28:31], v[208:211], v[120:123]\n\t"
    "buffer_load_dwordx4 v106, s[52:55], s32 offen offset:2048 lds\n\t"
    "v_mfma_f32_16x16x32_bf16 v[124:127], a[16:19], v[212:215], v[160:163]\n\t"
    "ds_read_b64_tr_b16 a[54:55], v189 offset:61440\n\t"
    "ds_read_b64_tr_b16 a[56:57], v190 offset:57344\n\t"
    "v_mfma_f32_16x16x32_bf16 v[124:127], a[20:23], v[216:219], v[124:127]\n\t"
    "v_exp_f32 v109, v109\n\t"
    "v_mfma_f32_16x16x32_bf16 v[124:127], a[24:27], v[220:223], v[124:127]\n\t"
    "ds_read_b64_tr_b16 a[58:59], v190 offset:61440\n\t"
    "v_cvt_pk_bf16_f32 v132, v108, v109\n\t"
    "v_mfma_f32_16x16x32_bf16 v[124:127], a[28:31], v[224:227], v[124:127]\n\t"
    "v_exp_f32 v110, v110\n\t"
    "v_mfma_f32_16x16x32_bf16 v[128:131], a[16:19], v[228:231], v[164:167]\n\t"
    "ds_read_b64_tr_b16 a[60:61], v191 offset:57344\n\t"
    "ds_read_b64_tr_b16 a[62:63], v191 offset:61440\n\t"
    "v_mfma_f32_16x16x32_bf16 v[128:131], a[20:23], v[232:235], v[128:131]\n\t"
    "v_exp_f32 v111, v111\n\t"
    "v_mfma_f32_16x16x32_bf16 v[128:131], a[24:27], v[236:239], v[128:131]\n\t"
    "buffer_load_dwordx4 v107, s[52:55], s32 offen offset:3072 lds\n\t"
    "v_mfma_f32_16x16x32_bf16 v[128:131], a[28:31], v[240:243], v[128:131]\n\t"
    "s_add_u32 s32, s34, 1\n\t"
    "v_exp_f32 v112, v112\n\t"
    "v_mfma_f32_16x16x32_bf16 v[96:99], a[240:243], v[144:147], v[96:99]\n\t"
    "s_and_b32 s32, s32, 31\n\t"
    "ds_read_b128 a[0:3], v172 offset:49152\n\t"
    "v_cvt_pk_bf16_f32 v133, v110, v111\n\t"
    "v_mfma_f32_16x16x32_bf16 v[96:99], a[244:247], v[148:151], v[96:99]\n\t"
    "s_mul_i32 s32, s32, 0xa000\n\t"
    "v_exp_f32 v113, v113\n\t"
    "v_mfma_f32_16x16x32_bf16 v[96:99], a[248:251], v[152:155], v[96:99]\n\t"
    "s_waitcnt lgkmcnt(15)\n\t"
    "v_exp_f32 v114, v114\n\t"
    "v_mfma_f32_16x16x32_bf16 v[64:67], a[32:35], v[152:155], v[64:67]\n\t"
    "s_add_u32 m0, s68, 0x1a000\n\t"
    "ds_read_b128 a[4:7], v173 offset:49152\n\t"
    "v_cvt_pk_bf16_f32 v136, v112, v113\n\t"
    "v_mfma_f32_16x16x32_bf16 v[32:35], a[32:35], v[148:151], v[32:35]\n\t"
    "v_exp_f32 v115, v115\n\t"
    "v_mfma_f32_16x16x32_bf16 v[0:3], a[32:35], v[144:147], v[0:3]\n\t"
    "s_waitcnt lgkmcnt(14)\n\t"
    "v_exp_f32 v116, v116\n\t"
    "v_mfma_f32_16x16x32_bf16 v[4:7], a[36:39], v[144:147], v[4:7]\n\t"
    "ds_read_b128 a[8:11], v174 offset:49152\n\t"
    "v_cvt_pk_bf16_f32 v137, v114, v115\n\t"
    "v_mfma_f32_16x16x32_bf16 v[36:39], a[36:39], v[148:151], v[36:39]\n\t"
    "buffer_load_dwordx4 v194, s[44:47], s67 offen nt lds\n\t"
    "v_mfma_f32_16x16x32_bf16 v[68:71], a[36:39], v[152:155], v[68:71]\n\t"
    "s_waitcnt lgkmcnt(13)\n\t"
    "v_exp_f32 v117, v117\n\t"
    "v_mfma_f32_16x16x32_bf16 v[72:75], a[40:43], v[152:155], v[72:75]\n\t"
    "ds_read_b128 a[12:15], v175 offset:49152\n\t"
    "v_cvt_pk_bf16_f32 v140, v116, v117\n\t"
    "v_mfma_f32_16x16x32_bf16 v[40:43], a[40:43], v[148:151], v[40:43]\n\t"
    "v_exp_f32 v118, v118\n\t"
    "v_mfma_f32_16x16x32_bf16 v[8:11], a[40:43], v[144:147], v[8:11]\n\t"
    "s_waitcnt lgkmcnt(12)\n\t"
    "ds_read_b128 a[16:19], v172 offset:53248\n\t"
    "ds_read_b128 a[20:23], v173 offset:53248\n\t"
    "v_mfma_f32_16x16x32_bf16 v[12:15], a[44:47], v[144:147], v[12:15]\n\t"
    "v_exp_f32 v119, v119\n\t"
    "v_mfma_f32_16x16x32_bf16 v[44:47], a[44:47], v[148:151], v[44:47]\n\t"
    "v_exp_f32 v120, v120\n\t"
    "v_mfma_f32_16x16x32_bf16 v[76:79], a[44:47], v[152:155], v[76:79]\n\t"
    "s_waitcnt lgkmcnt(12)\n\t"
    "ds_read_b128 a[24:27], v174 offset:53248\n\t"
    "v_cvt_pk_bf16_f32 v141, v118, v119\n\t"
    "v_mfma_f32_16x16x32_bf16 v[80:83], a[48:51], v[152:155], v[80:83]\n\t"
    "v_exp_f32 v121, v121\n\t"
    "v_mfma_f32_16x16x32_bf16 v[48:51], a[48:51], v[148:151], v[48:51]\n\t"
    "ds_read_b128 a[28:31], v175 offset:53248\n\t"
    "v_cvt_pk_bf16_f32 v134, v120, v121\n\t"
    "v_mfma_f32_16x16x32_bf16 v[16:19], a[48:51], v[144:147], v[16:19]\n\t"
    "s_waitcnt lgkmcnt(12)\n\t"
    "buffer_load_dwordx4 v195, s[44:47], s67 offen offset:1024 nt lds\n\t"
    "v_mfma_f32_16x16x32_bf16 v[20:23], a[52:55], v[144:147], v[20:23]\n\t"
    "s_add_u32 s67, s67, 0x2000\n\t"
    "v_exp_f32 v122, v122\n\t"
    "v_mfma_f32_16x16x32_bf16 v[52:55], a[52:55], v[148:151], v[52:55]\n\t"
    "v_exp_f32 v123, v123\n\t"
    "v_mfma_f32_16x16x32_bf16 v[84:87], a[52:55], v[152:155], v[84:87]\n\t"
    "s_waitcnt lgkmcnt(10)\n\t"
    "v_exp_f32 v124, v124\n\t"
    "v_mfma_f32_16x16x32_bf16 v[88:91], a[56:59], v[152:155], v[88:91]\n\t"
    "v_cvt_pk_bf16_f32 v135, v122, v123\n\t"
    "v_mfma_f32_16x16x32_bf16 v[56:59], a[56:59], v[148:151], v[56:59]\n\t"
    "v_exp_f32 v125, v125\n\t"
    "v_mfma_f32_16x16x32_bf16 v[24:27], a[56:59], v[144:147], v[24:27]\n\t"
    "s_waitcnt lgkmcnt(8)\n\t"
    "v_exp_f32 v126, v126\n\t"
    "v_mfma_f32_16x16x32_bf16 v[28:31], a[60:63], v[144:147], v[28:31]\n\t"
    "v_cvt_pk_bf16_f32 v138, v124, v125\n\t"
    "v_mfma_f32_16x16x32_bf16 v[60:63], a[60:63], v[148:151], v[60:63]\n\t"
    "v_mfma_f32_16x16x32_bf16 v[92:95], a[60:63], v[152:155], v[92:95]\n\t"
    "s_waitcnt lgkmcnt(0)\n\t"
    "s_barrier\n\t"
    "v_mfma_f32_16x16x32_bf16 v[108:111], a[0:3], v[196:199], v[156:159]\n\t"
    "ds_read_b64_tr_b16 a[32:33], v176 offset:8192\n\t"
    "ds_read_b64_tr_b16 a[34:35], v176 offset:12288\n\t"
    "v_mfma_f32_16x16x32_bf16 v[108:111], a[4:7], v[200:203], v[108:111]\n\t"
    "s_add_u32 m0, s72, 0x20000\n\t"
    "v_exp_f32 v127, v127\n\t"
    "v_mfma_f32_16x16x32_bf16 v[108:111], a[8:11], v[204:207], v[108:111]\n\t"
    "ds_read_b64_tr_b16 a[36:37], v177 offset:8192\n\t"
    "ds_read_b64_tr_b16 a[38:39], v177 offset:12288\n\t"
    "v_mfma_f32_16x16x32_bf16 v[108:111], a[12:15], v[208:211], v[108:111]\n\t"
    "buffer_load_dwordx4 v104, s[52:55], s32 offen lds\n\t"
    "v_mfma_f32_16x16x32_bf16 v[112:115], a[0:3], v[212:215], v[160:163]\n\t"
    "v_exp_f32 v128, v128\n\t"
    "v_mfma_f32_16x16x32_bf16 v[112:115], a[4:7], v[216:219], v[112:115]\n\t"
    "ds_read_b64_tr_b16 a[40:41], v178 offset:8192\n\t"
    "v_cvt_pk_bf16_f32 v139, v126, v127\n\t"
    "v_mfma_f32_16x16x32_bf16 v[112:115], a[8:11], v[220:223], v[112:115]\n\t"
    "v_exp_f32 v129, v129\n\t"
    "v_mfma_f32_16x16x32_bf16 v[112:115], a[12:15], v[224:227], v[112:115]\n\t"
    "ds_read_b64_tr_b16 a[42:43], v178 offset:12288\n\t"
    "v_cvt_pk_bf16_f32 v142, v128, v129\n\t"
    "v_mfma_f32_16x16x32_bf16 v[116:119], a[0:3], v[228:231], v[164:167]\n\t"
    "v_exp_f32 v130, v130\n\t"
    "v_mfma_f32_16x16x32_bf16 v[116:119], a[4:7], v[232:235], v[116:119]\n\t"
    "ds_read_b64_tr_b16 a[44:45], v179 offset:8192\n\t"
    "ds_read_b64_tr_b16 a[46:47], v179 offset:12288\n\t"
    "v_mfma_f32_16x16x32_bf16 v[116:119], a[8:11], v[236:239], v[116:119]\n\t"
    "buffer_load_dwordx4 v105, s[52:55], s32 offen offset:1024 lds\n\t"
    "v_mfma_f32_16x16x32_bf16 v[116:119], a[12:15], v[240:243], v[116:119]\n\t"
    "v_exp_f32 v131, v131\n\t"
    "v_mfma_f32_16x16x32_bf16 v[120:123], a[16:19], v[196:199], v[156:159]\n\t"
    "ds_read_b64_tr_b16 a[48:49], v180 offset:8192\n\t"
    "v_cvt_pk_bf16_f32 v143, v130, v131\n\t"
    "v_mfma_f32_16x16x32_bf16 v[120:123], a[20:23], v[200:203], v[120:123]\n\t"
    "ds_read_b64_tr_b16 a[50:51], v180 offset:12288\n\t"
    "ds_read_b64_tr_b16 a[52:53], v181 offset:8192\n\t"
    "v_mfma_f32_16x16x32_bf16 v[120:123], a[24:27], v[204:207], v[120:123]\n\t"
    "v_exp_f32 v108, v108\n\t"
    "v_mfma_f32_16x16x32_bf16 v[120:123], a[28:31], v[208:211], v[120:123]\n\t"
    "buffer_load_dwordx4 v106, s[52:55], s32 offen offset:2048 lds\n\t"
    "v_mfma_f32_16x16x32_bf16 v[124:127], a[16:19], v[212:215], v[160:163]\n\t"
    "ds_read_b64_tr_b16 a[54:55], v181 offset:12288\n\t"
    "ds_read_b64_tr_b16 a[56:57], v182 offset:8192\n\t"
    "v_mfma_f32_16x16x32_bf16 v[124:127], a[20:23], v[216:219], v[124:127]\n\t"
    "v_exp_f32 v109, v109\n\t"
    "v_mfma_f32_16x16x32_bf16 v[124:127], a[24:27], v[220:223], v[124:127]\n\t"
    "ds_read_b64_tr_b16 a[58:59], v182 offset:12288\n\t"
    "v_cvt_pk_bf16_f32 v144, v108, v109\n\t"
    "v_mfma_f32_16x16x32_bf16 v[124:127], a[28:31], v[224:227], v[124:127]\n\t"
    "v_exp_f32 v110, v110\n\t"
    "v_mfma_f32_16x16x32_bf16 v[128:131], a[16:19], v[228:231], v[164:167]\n\t"
    "ds_read_b64_tr_b16 a[60:61], v183 offset:8192\n\t"
    "ds_read_b64_tr_b16 a[62:63], v183 offset:12288\n\t"
    "v_mfma_f32_16x16x32_bf16 v[128:131], a[20:23], v[232:235], v[128:131]\n\t"
    "v_exp_f32 v111, v111\n\t"
    "v_mfma_f32_16x16x32_bf16 v[128:131], a[24:27], v[236:239], v[128:131]\n\t"
    "buffer_load_dwordx4 v107, s[52:55], s32 offen offset:3072 lds\n\t"
    "v_mfma_f32_16x16x32_bf16 v[128:131], a[28:31], v[240:243], v[128:131]\n\t"
    "s_add_u32 s32, s34, 2\n\t"
    "v_exp_f32 v112, v112\n\t"
    "v_mfma_f32_16x16x32_bf16 v[96:99], a[240:243], v[132:135], v[96:99]\n\t"
    "s_and_b32 s32, s32, 31\n\t"
    "ds_read_b128 a[0:3], v168\n\t"
    "v_cvt_pk_bf16_f32 v145, v110, v111\n\t"
    "v_mfma_f32_16x16x32_bf16 v[96:99], a[244:247], v[136:139], v[96:99]\n\t"
    "s_mul_i32 s32, s32, 0xa000\n\t"
    "v_exp_f32 v113, v113\n\t"
    "v_mfma_f32_16x16x32_bf16 v[96:99], a[248:251], v[140:143], v[96:99]\n\t"
    "s_waitcnt lgkmcnt(15)\n\t"
    "v_exp_f32 v114, v114\n\t"
    "v_mfma_f32_16x16x32_bf16 v[64:67], a[32:35], v[140:143], v[64:67]\n\t"
    "s_add_u32 m0, s68, 0x1e000\n\t"
    "ds_read_b128 a[4:7], v169\n\t"
    "v_cvt_pk_bf16_f32 v148, v112, v113\n\t"
    "v_mfma_f32_16x16x32_bf16 v[32:35], a[32:35], v[136:139], v[32:35]\n\t"
    "v_exp_f32 v115, v115\n\t"
    "v_mfma_f32_16x16x32_bf16 v[0:3], a[32:35], v[132:135], v[0:3]\n\t"
    "s_waitcnt lgkmcnt(14)\n\t"
    "v_exp_f32 v116, v116\n\t"
    "v_mfma_f32_16x16x32_bf16 v[4:7], a[36:39], v[132:135], v[4:7]\n\t"
    "ds_read_b128 a[8:11], v170\n\t"
    "v_cvt_pk_bf16_f32 v149, v114, v115\n\t"
    "v_mfma_f32_16x16x32_bf16 v[36:39], a[36:39], v[136:139], v[36:39]\n\t"
    "buffer_load_dwordx4 v194, s[44:47], s67 offen nt lds\n\t"
    "v_mfma_f32_16x16x32_bf16 v[68:71], a[36:39], v[140:143], v[68:71]\n\t"
    "s_waitcnt lgkmcnt(13)\n\t"
    "v_exp_f32 v117, v117\n\t"
    "v_mfma_f32_16x16x32_bf16 v[72:75], a[40:43], v[140:143], v[72:75]\n\t"
    "ds_read_b128 a[12:15], v171\n\t"
    "v_cvt_pk_bf16_f32 v152, v116, v117\n\t"
    "v_mfma_f32_16x16x32_bf16 v[40:43], a[40:43], v[136:139], v[40:43]\n\t"
    "v_exp_f32 v118, v118\n\t"
    "v_mfma_f32_16x16x32_bf16 v[8:11], a[40:43], v[132:135], v[8:11]\n\t"
    "s_waitcnt lgkmcnt(12)\n\t"
    "ds_read_b128 a[16:19], v168 offset:4096\n\t"
    "ds_read_b128 a[20:23], v169 offset:4096\n\t"
    "v_mfma_f32_16x16x32_bf16 v[12:15], a[44:47], v[132:135], v[12:15]\n\t"
    "v_exp_f32 v119, v119\n\t"
    "v_mfma_f32_16x16x32_bf16 v[44:47], a[44:47], v[136:139], v[44:47]\n\t"
    "v_exp_f32 v120, v120\n\t"
    "v_mfma_f32_16x16x32_bf16 v[76:79], a[44:47], v[140:143], v[76:79]\n\t"
    "s_waitcnt lgkmcnt(12)\n\t"
    "ds_read_b128 a[24:27], v170 offset:4096\n\t"
    "v_cvt_pk_bf16_f32 v153, v118, v119\n\t"
    "v_mfma_f32_16x16x32_bf16 v[80:83], a[48:51], v[140:143], v[80:83]\n\t"
    "v_exp_f32 v121, v121\n\t"
    "v_mfma_f32_16x16x32_bf16 v[48:51], a[48:51], v[136:139], v[48:51]\n\t"
    "ds_read_b128 a[28:31], v171 offset:4096\n\t"
    "v_cvt_pk_bf16_f32 v146, v120, v121\n\t"
    "v_mfma_f32_16x16x32_bf16 v[16:19], a[48:51], v[132:135], v[16:19]\n\t"
    "s_waitcnt lgkmcnt(12)\n\t"
    "buffer_load_dwordx4 v195, s[44:47], s67 offen offset:1024 nt lds\n\t"
    "v_mfma_f32_16x16x32_bf16 v[20:23], a[52:55], v[132:135], v[20:23]\n\t"
    "s_add_u32 s67, s67, 0x2000\n\t"
    "v_exp_f32 v122, v122\n\t"
    "v_mfma_f32_16x16x32_bf16 v[52:55], a[52:55], v[136:139], v[52:55]\n\t"
    "v_exp_f32 v123, v123\n\t"
    "v_mfma_f32_16x16x32_bf16 v[84:87], a[52:55], v[140:143], v[84:87]\n\t"
    "s_waitcnt lgkmcnt(10)\n\t"
    "v_exp_f32 v124, v124\n\t"
    "v_mfma_f32_16x16x32_bf16 v[88:91], a[56:59], v[140:143], v[88:91]\n\t"
    "v_cvt_pk_bf16_f32 v147, v122, v123\n\t"
    "v_mfma_f32_16x16x32_bf16 v[56:59], a[56:59], v[136:139], v[56:59]\n\t"
    "v_exp_f32 v125, v125\n\t"
    "v_mfma_f32_16x16x32_bf16 v[24:27], a[56:59], v[132:135], v[24:27]\n\t"
    "s_waitcnt lgkmcnt(8)\n\t"
    "v_exp_f32 v126, v126\n\t"
    "v_mfma_f32_16x16x32_bf16 v[28:31], a[60:63], v[132:135], v[28:31]\n\t"
    "v_cvt_pk_bf16_f32 v150, v124, v125\n\t"
    "v_mfma_f32_16x16x32_bf16 v[60:63], a[60:63], v[136:139], v[60:63]\n\t"
    "v_mfma_f32_16x16x32_bf16 v[92:95], a[60:63], v[140:143], v[92:95]\n\t"
    "s_waitcnt lgkmcnt(0)\n\t"
    "s_barrier\n\t"
    "v_mfma_f32_16x16x32_bf16 v[108:111], a[0:3], v[196:199], v[156:159]\n\t"
    "ds_read_b64_tr_b16 a[32:33], v176 offset:24576\n\t"
    "ds_read_b64_tr_b16 a[34:35], v176 offset:28672\n\t"
    "v_mfma_f32_16x16x32_bf16 v[108:111], a[4:7], v[200:203], v[108:111]\n\t"
    "s_add_u32 m0, s72, 0\n\t"
    "v_exp_f32 v127, v127\n\t"
    "v_mfma_f32_16x16x32_bf16 v[108:111], a[8:11], v[204:207], v[108:111]\n\t"
    "ds_read_b64_tr_b16 a[36:37], v177 offset:24576\n\t"
    "ds_read_b64_tr_b16 a[38:39], v177 offset:28672\n\t"
    "v_mfma_f32_16x16x32_bf16 v[108:111], a[12:15], v[208:211], v[108:111]\n\t"
    "buffer_load_dwordx4 v104, s[52:55], s32 offen lds\n\t"
    "v_mfma_f32_16x16x32_bf16 v[112:115], a[0:3], v[212:215], v[160:163]\n\t"
    "v_exp_f32 v128, v128\n\t"
    "v_mfma_f32_16x16x32_bf16 v[112:115], a[4:7], v[216:219], v[112:115]\n\t"
    "ds_read_b64_tr_b16 a[40:41], v178 offset:24576\n\t"
    "v_cvt_pk_bf16_f32 v151, v126, v127\n\t"
    "v_mfma_f32_16x16x32_bf16 v[112:115], a[8:11], v[220:223], v[112:115]\n\t"
    "v_exp_f32 v129, v129\n\t"
    "v_mfma_f32_16x16x32_bf16 v[112:115], a[12:15], v[224:227], v[112:115]\n\t"
    "buffer_load_dwordx4 v105, s[52:55], s32 offen offset:1024 lds\n\t"
    "v_mfma_f32_16x16x32_bf16 v[116:119], a[0:3], v[228:231], v[164:167]\n\t"
    "ds_read_b64_tr_b16 a[42:43], v178 offset:28672\n\t"
    "v_cvt_pk_bf16_f32 v154, v128, v129\n\t"
    "v_mfma_f32_16x16x32_bf16 v[116:119], a[4:7], v[232:235], v[116:119]\n\t"
    "v_exp_f32 v130, v130\n\t"
    "v_mfma_f32_16x16x32_bf16 v[116:119], a[8:11], v[236:239], v[116:119]\n\t"
    "v_exp_f32 v131, v131\n\t"
    "v_mfma_f32_16x16x32_bf16 v[116:119], a[12:15], v[240:243], v[116:119]\n\t"
    "ds_read_b64_tr_b16 a[44:45], v179 offset:24576\n\t"
    "v_cvt_pk_bf16_f32 v155, v130, v131\n\t"
    "v_mfma_f32_16x16x32_bf16 v[120:123], a[16:19], v[196:199], v[156:159]\n\t"
    "ds_read_b64_tr_b16 a[46:47], v179 offset:28672\n\t"
    "ds_read_b64_tr_b16 a[48:49], v180 offset:24576\n\t"
    "v_mfma_f32_16x16x32_bf16 v[120:123], a[20:23], v[200:203], v[120:123]\n\t"
    "ds_read_b64_tr_b16 a[50:51], v180 offset:28672\n\t"
    "ds_read_b64_tr_b16 a[52:53], v181 offset:24576\n\t"
    "v_mfma_f32_16x16x32_bf16 v[120:123], a[24:27], v[204:207], v[120:123]\n\t"
    "v_exp_f32 v108, v108\n\t"
    "v_mfma_f32_16x16x32_bf16 v[120:123], a[28:31], v[208:211], v[120:123]\n\t"
    "buffer_load_dwordx4 v106, s[52:55], s32 offen offset:2048 lds\n\t"
    "v_mfma_f32_16x16x32_bf16 v[124:127], a[16:19], v[212:215], v[160:163]\n\t"
    "ds_read_b64_tr_b16 a[54:55], v181 offset:28672\n\t"
    "ds_read_b64_tr_b16 a[56:57], v182 offset:24576\n\t"
    "v_mfma_f32_16x16x32_bf16 v[124:127], a[20:23], v[216:219], v[124:127]\n\t"
    "v_exp_f32 v109, v109\n\t"
    "v_mfma_f32_16x16x32_bf16 v[124:127], a[24:27], v[220:223], v[124:127]\n\t"
    "ds_read_b64_tr_b16 a[58:59], v182 offset:28672\n\t"
    "v_cvt_pk_bf16_f32 v132, v108, v109\n\t"
    "v_mfma_f32_16x16x32_bf16 v[124:127], a[28:31], v[224:227], v[124:127]\n\t"
    "v_exp_f32 v110, v110\n\t"
    "v_mfma_f32_16x16x32_bf16 v[128:131], a[16:19], v[228:231], v[164:167]\n\t"
    "buffer_load_dwordx4 v107, s[52:55], s32 offen offset:3072 lds\n\t"
    "v_mfma_f32_16x16x32_bf16 v[128:131], a[20:23], v[232:235], v[128:131]\n\t"
    "s_add_u32 s32, s34, 5\n\t"
    "ds_read_b64_tr_b16 a[60:61], v183 offset:24576\n\t"
    "ds_read_b64_tr_b16 a[62:63], v183 offset:28672\n\t"
    "v_mfma_f32_16x16x32_bf16 v[128:131], a[24:27], v[236:239], v[128:131]\n\t"
    "s_and_b32 s32, s32, 31\n\t"
    "v_exp_f32 v111, v111\n\t"
    "v_mfma_f32_16x16x32_bf16 v[128:131], a[28:31], v[240:243], v[128:131]\n\t"
    "s_mul_i32 s32, s32, 0xa000\n\t"
    "v_exp_f32 v112, v112\n\t"
    "v_mfma_f32_16x16x32_bf16 v[96:99], a[240:243], v[144:147], v[96:99]\n\t"
    "v_cvt_pk_bf16_f32 v133, v110, v111\n\t"
    "v_subrev_u32 v192, 0x20000, v100\n\t"
    "v_mfma_f32_16x16x32_bf16 v[96:99], a[244:247], v[148:151], v[96:99]\n\t"
    "v_exp_f32 v113, v113\n\t"
    "v_mfma_f32_16x16x32_bf16 v[96:99], a[248:251], v[152:155], v[96:99]\n\t"
    "s_waitcnt lgkmcnt(14)\n\t"
    "ds_read_b128 a[0:3], v168 offset:16384\n\t"
    "v_subrev_u32 v193, 0x20000, v101\n\t"
    "v_mfma_f32_16x16x32_bf16 v[64:67], a[32:35], v[152:155], v[64:67]\n\t"
    "ds_read_b128 a[4:7], v169 offset:16384\n\t"
    "v_subrev_u32 v194, 0x20000, v102\n\t"
    "v_mfma_f32_16x16x32_bf16 v[32:35], a[32:35], v[148:151], v[32:35]\n\t"
    "v_exp_f32 v114, v114\n\t"
    "v_mfma_f32_16x16x32_bf16 v[0:3], a[32:35], v[144:147], v[0:3]\n\t"
    "s_waitcnt lgkmcnt(14)\n\t"
    "ds_read_b128 a[8:11], v170 offset:16384\n\t"
    "v_cvt_pk_bf16_f32 v136, v112, v113\n\t"
    "v_mfma_f32_16x16x32_bf16 v[4:7], a[36:39], v[144:147], v[4:7]\n\t"
    "ds_read_b128 a[12:15], v171 offset:16384\n\t"
    "v_subrev_u32 v195, 0x20000, v103\n\t"
    "v_mfma_f32_16x16x32_bf16 v[36:39], a[36:39], v[148:151], v[36:39]\n\t"
    "v_exp_f32 v115, v115\n\t"
    "v_mfma_f32_16x16x32_bf16 v[68:71], a[36:39], v[152:155], v[68:71]\n\t"
    "s_waitcnt lgkmcnt(14)\n\t"
    "v_exp_f32 v116, v116\n\t"
    "v_mfma_f32_16x16x32_bf16 v[72:75], a[40:43], v[152:155], v[72:75]\n\t"
    "ds_read_b128 a[16:19], v168 offset:20480\n\t"
    "v_cvt_pk_bf16_f32 v137, v114, v115\n\t"
    "v_mfma_f32_16x16x32_bf16 v[40:43], a[40:43], v[148:151], v[40:43]\n\t"
    "v_exp_f32 v117, v117\n\t"
    "v_mfma_f32_16x16x32_bf16 v[8:11], a[40:43], v[144:147], v[8:11]\n\t"
    "s_waitcnt lgkmcnt(13)\n\t"
    "v_exp_f32 v118, v118\n\t"
    "v_mfma_f32_16x16x32_bf16 v[12:15], a[44:47], v[144:147], v[12:15]\n\t"
    "ds_read_b128 a[20:23], v169 offset:20480\n\t"
    "v_cvt_pk_bf16_f32 v140, v116, v117\n\t"
    "v_mfma_f32_16x16x32_bf16 v[44:47], a[44:47], v[148:151], v[44:47]\n\t"
    "v_exp_f32 v119, v119\n\t"
    "v_mfma_f32_16x16x32_bf16 v[76:79], a[44:47], v[152:155], v[76:79]\n\t"
    "s_waitcnt lgkmcnt(12)\n\t"
    "v_exp_f32 v120, v120\n\t"
    "v_mfma_f32_16x16x32_bf16 v[80:83], a[48:51], v[152:155], v[80:83]\n\t"
    "ds_read_b128 a[24:27], v170 offset:20480\n\t"
    "v_cvt_pk_bf16_f32 v141, v118, v119\n\t"
    "v_mfma_f32_16x16x32_bf16 v[48:51], a[48:51], v[148:151], v[48:51]\n\t"
    "v_exp_f32 v121, v121\n\t"
    "v_mfma_f32_16x16x32_bf16 v[16:19], a[48:51], v[144:147], v[16:19]\n\t"
    "s_waitcnt lgkmcnt(11)\n\t"
    "ds_read_b128 a[28:31], v171 offset:20480\n\t"
    "v_cvt_pk_bf16_f32 v134, v120, v121\n\t"
    "v_mfma_f32_16x16x32_bf16 v[20:23], a[52:55], v[144:147], v[20:23]\n\t"
    "v_exp_f32 v122, v122\n\t"
    "v_mfma_f32_16x16x32_bf16 v[52:55], a[52:55], v[148:151], v[52:55]\n\t"
    "v_exp_f32 v123, v123\n\t"
    "v_mfma_f32_16x16x32_bf16 v[84:87], a[52:55], v[152:155], v[84:87]\n\t"
    "s_waitcnt lgkmcnt(10)\n\t"
    "v_exp_f32 v124, v124\n\t"
    "v_mfma_f32_16x16x32_bf16 v[88:91], a[56:59], v[152:155], v[88:91]\n\t"
    "v_cvt_pk_bf16_f32 v135, v122, v123\n\t"
    "v_mfma_f32_16x16x32_bf16 v[56:59], a[56:59], v[148:151], v[56:59]\n\t"
    "v_exp_f32 v125, v125\n\t"
    "v_mfma_f32_16x16x32_bf16 v[24:27], a[56:59], v[144:147], v[24:27]\n\t"
    "s_waitcnt lgkmcnt(8)\n\t"
    "v_exp_f32 v126, v126\n\t"
    "v_mfma_f32_16x16x32_bf16 v[28:31], a[60:63], v[144:147], v[28:31]\n\t"
    "v_cvt_pk_bf16_f32 v138, v124, v125\n\t"
    "v_mfma_f32_16x16x32_bf16 v[60:63], a[60:63], v[148:151], v[60:63]\n\t"
    "v_mfma_f32_16x16x32_bf16 v[92:95], a[60:63], v[152:155], v[92:95]\n\t"
    "s_waitcnt lgkmcnt(0)\n\t"
    "s_barrier\n\t"
    "v_mfma_f32_16x16x32_bf16 v[108:111], a[0:3], v[196:199], v[156:159]\n\t"
    "ds_read_b64_tr_b16 a[32:33], v176 offset:40960\n\t"
    "ds_read_b64_tr_b16 a[34:35], v176 offset:45056\n\t"
    "v_mfma_f32_16x16x32_bf16 v[108:111], a[4:7], v[200:203], v[108:111]\n\t"
    "v_exp_f32 v127, v127\n\t"
    "v_mfma_f32_16x16x32_bf16 v[108:111], a[8:11], v[204:207], v[108:111]\n\t"
    "s_waitcnt vmcnt(12)\n\t"
    "ds_read_b64_tr_b16 a[64:65], v100 offset:16384\n\t"
    "ds_read_b64_tr_b16 a[36:37], v177 offset:40960\n\t"
    "v_mfma_f32_16x16x32_bf16 v[108:111], a[12:15], v[208:211], v[108:111]\n\t"
    "s_add_u32 m0, s72, 0x4000\n\t"
    "ds_read_b64_tr_b16 a[38:39], v177 offset:45056\n\t"
    "v_cvt_pk_bf16_f32 v139, v126, v127\n\t"
    "v_mfma_f32_16x16x32_bf16 v[112:115], a[0:3], v[212:215], v[160:163]\n\t"
    "v_exp_f32 v128, v128\n\t"
    "v_mfma_f32_16x16x32_bf16 v[112:115], a[4:7], v[216:219], v[112:115]\n\t"
    "buffer_load_dwordx4 v104, s[52:55], s32 offen lds\n\t"
    "v_mfma_f32_16x16x32_bf16 v[112:115], a[8:11], v[220:223], v[112:115]\n\t"
    "ds_read_b64_tr_b16 a[66:67], v100 offset:18432\n\t"
    "ds_read_b64_tr_b16 a[68:69], v101 offset:16384\n\t"
    "v_mfma_f32_16x16x32_bf16 v[112:115], a[12:15], v[224:227], v[112:115]\n\t"
    "v_exp_f32 v129, v129\n\t"
    "v_mfma_f32_16x16x32_bf16 v[116:119], a[0:3], v[228:231], v[164:167]\n\t"
    "ds_read_b64_tr_b16 a[40:41], v178 offset:40960\n\t"
    "v_cvt_pk_bf16_f32 v142, v128, v129\n\t"
    "v_mfma_f32_16x16x32_bf16 v[116:119], a[4:7], v[232:235], v[116:119]\n\t"
    "v_exp_f32 v130, v130\n\t"
    "v_mfma_f32_16x16x32_bf16 v[116:119], a[8:11], v[236:239], v[116:119]\n\t"
    "ds_read_b64_tr_b16 a[70:71], v101 offset:18432\n\t"
    "ds_read_b64_tr_b16 a[42:43], v178 offset:45056\n\t"
    "v_mfma_f32_16x16x32_bf16 v[116:119], a[12:15], v[240:243], v[116:119]\n\t"
    "buffer_load_dwordx4 v105, s[52:55], s32 offen offset:1024 lds\n\t"
    "v_mfma_f32_16x16x32_bf16 v[120:123], a[16:19], v[196:199], v[156:159]\n\t"
    "v_exp_f32 v131, v131\n\t"
    "v_mfma_f32_16x16x32_bf16 v[120:123], a[20:23], v[200:203], v[120:123]\n\t"
    "ds_read_b64_tr_b16 a[72:73], v102 offset:16384\n\t"
    "v_cvt_pk_bf16_f32 v143, v130, v131\n\t"
    "v_mfma_f32_16x16x32_bf16 v[120:123], a[24:27], v[204:207], v[120:123]\n\t"
    "ds_read_b64_tr_b16 a[44:45], v179 offset:40960\n\t"
    "ds_read_b64_tr_b16 a[46:47], v179 offset:45056\n\t"
    "v_mfma_f32_16x16x32_bf16 v[120:123], a[28:31], v[208:211], v[120:123]\n\t"
    "ds_read_b64_tr_b16 a[74:75], v102 offset:18432\n\t"
    "ds_read_b64_tr_b16 a[76:77], v103 offset:16384\n\t"
    "v_mfma_f32_16x16x32_bf16 v[124:127], a[16:19], v[212:215], v[160:163]\n\t"
    "v_exp_f32 v108, v108\n\t"
    "v_mfma_f32_16x16x32_bf16 v[124:127], a[20:23], v[216:219], v[124:127]\n\t"
    "ds_read_b64_tr_b16 a[48:49], v180 offset:40960\n\t"
    "ds_read_b64_tr_b16 a[50:51], v180 offset:45056\n\t"
    "v_mfma_f32_16x16x32_bf16 v[124:127], a[24:27], v[220:223], v[124:127]\n\t"
    "s_waitcnt vmcnt(8)\n\t"
    "ds_read_b64_tr_b16 a[52:53], v181 offset:40960\n\t"
    "ds_read_b64_tr_b16 a[78:79], v103 offset:18432\n\t"
    "v_mfma_f32_16x16x32_bf16 v[124:127], a[28:31], v[224:227], v[124:127]\n\t"
    "v_exp_f32 v109, v109\n\t"
    "v_mfma_f32_16x16x32_bf16 v[128:131], a[16:19], v[228:231], v[164:167]\n\t"
    "ds_read_b64_tr_b16 a[54:55], v181 offset:45056\n\t"
    "ds_read_b64_tr_b16 a[56:57], v182 offset:40960\n\t"
    "v_mfma_f32_16x16x32_bf16 v[128:131], a[20:23], v[232:235], v[128:131]\n\t"
    "buffer_load_dwordx4 v106, s[52:55], s32 offen offset:2048 lds\n\t"
    "v_mfma_f32_16x16x32_bf16 v[128:131], a[24:27], v[236:239], v[128:131]\n\t"
    "v_exp_f32 v110, v110\n\t"
    "v_mfma_f32_16x16x32_bf16 v[128:131], a[28:31], v[240:243], v[128:131]\n\t"
    "ds_read_b64_tr_b16 a[80:81], v100\n\t"
    "v_cvt_pk_bf16_f32 v144, v108, v109\n\t"
    "v_mfma_f32_16x16x32_bf16 v[96:99], a[240:243], v[132:135], v[96:99]\n\t"
    "v_exp_f32 v111, v111\n\t"
    "v_mfma_f32_16x16x32_bf16 v[96:99], a[244:247], v[136:139], v[96:99]\n\t"
    "ds_read_b64_tr_b16 a[82:83], v100 offset:2048\n\t"
    "ds_read_b64_tr_b16 a[58:59], v182 offset:45056\n\t"
    "v_mfma_f32_16x16x32_bf16 v[96:99], a[248:251], v[140:143], v[96:99]\n\t"
    "s_waitcnt lgkmcnt(15)\n\t"
    "buffer_load_dwordx4 v107, s[52:55], s32 offen offset:3072 lds\n\t"
    "v_exp_f32 v112, v112\n\t"
    "v_mfma_f32_16x16x32_bf16 v[64:67], a[32:35], v[140:143], v[64:67]\n\t"
    "s_add_u32 s32, s34, 6\n\t"
    "ds_read_b64_tr_b16 a[84:85], v101\n\t"
    "ds_read_b64_tr_b16 a[60:61], v183 offset:40960\n\t"
    "v_cvt_pk_bf16_f32 v145, v110, v111\n\t"
    "v_mfma_f32_16x16x32_bf16 v[32:35], a[32:35], v[136:139], v[32:35]\n\t"
    "s_and_b32 s32, s32, 31\n\t"
    "v_exp_f32 v113, v113\n\t"
    "v_mfma_f32_16x16x32_bf16 v[0:3], a[32:35], v[132:135], v[0:3]\n\t"
    "s_mul_i32 s32, s32, 0xa000\n\t"
    "ds_read_b64_tr_b16 a[86:87], v101 offset:2048\n\t"
    "v_mfma_f32_16x16x32_bf16 v[4:7], a[36:39], v[132:135], v[4:7]\n\t"
    "s_add_u32 m0, s72, 0x24000\n\t"
    "v_exp_f32 v114, v114\n\t"
    "v_cvt_pk_bf16_f32 v148, v112, v113\n\t"
    "v_mfma_f32_16x16x32_bf16 v[36:39], a[36:39], v[136:139], v[36:39]\n\t"
    "s_waitcnt lgkmcnt(8)\n\t"
    "ds_read_b64_tr_b16 a[62:63], v183 offset:45056\n\t"
    "ds_read_b64_tr_b16 a[88:89], v102\n\t"
    "v_mfma_f32_16x16x32_bf16 v[68:71], a[36:39], v[140:143], v[68:71]\n\t"
    "v_exp_f32 v115, v115\n\t"
    "v_mfma_f32_16x16x32_bf16 v[72:75], a[40:43], v[140:143], v[72:75]\n\t"
    "ds_read_b64_tr_b16 a[90:91], v102 offset:2048\n\t"
    "ds_read_b64_tr_b16 a[92:93], v103\n\t"
    "ds_read_b64_tr_b16 a[94:95], v103 offset:2048\n\t"
    "buffer_load_dwordx4 v104, s[52:55], s32 offen lds\n\t"
    "v_mfma_f32_16x16x32_bf16 v[40:43], a[40:43], v[136:139], v[40:43]\n\t"
    "buffer_load_dwordx4 v105, s[52:55], s32 offen offset:1024 lds\n\t"
    "v_exp_f32 v116, v116\n\t"
    "v_mfma_f32_16x16x32_bf16 v[8:11], a[40:43], v[132:135], v[8:11]\n\t"
    "ds_read_b128 a[0:3], v168 offset:32768\n\t"
    "v_cvt_pk_bf16_f32 v149, v114, v115\n\t"
    "v_mfma_f32_16x16x32_bf16 v[12:15], a[44:47], v[132:135], v[12:15]\n\t"
    "v_exp_f32 v117, v117\n\t"
    "v_mfma_f32_16x16x32_bf16 v[44:47], a[44:47], v[136:139], v[44:47]\n\t"
    "s_waitcnt lgkmcnt(1)\n\t"
    "ds_read_b128 a[4:7], v169 offset:32768\n\t"
    "v_cvt_pk_bf16_f32 v152, v116, v117\n\t"
    "v_mfma_f32_16x16x32_bf16 v[76:79], a[44:47], v[140:143], v[76:79]\n\t"
    "buffer_load_dwordx4 v106, s[52:55], s32 offen offset:2048 lds\n\t"
    "buffer_load_dwordx4 v107, s[52:55], s32 offen offset:3072 lds\n\t"
    "v_exp_f32 v118, v118\n\t"
    "v_mfma_f32_16x16x32_bf16 v[80:83], a[48:51], v[140:143], v[80:83]\n\t"
    "s_add_u32 s32, s34, 3\n\t"
    "ds_read_b128 a[8:11], v170 offset:32768\n\t"
    "ds_read_b128 a[12:15], v171 offset:32768\n\t"
    "v_mfma_f32_16x16x32_bf16 v[48:51], a[48:51], v[136:139], v[48:51]\n\t"
    "s_and_b32 s32, s32, 31\n\t"
    "v_exp_f32 v119, v119\n\t"
    "v_mfma_f32_16x16x32_bf16 v[16:19], a[48:51], v[132:135], v[16:19]\n\t"
    "s_mul_i32 s32, s32, 0xa000\n\t"
    "s_add_u32 m0, s72, 0x20000\n\t"
    "ds_read_b128 a[16:19], v168 offset:36864\n\t"
    "v_mfma_f32_16x16x32_bf16 v[20:23], a[52:55], v[132:135], v[20:23]\n\t"
    "buffer_load_dwordx4 v104, s[52:55], s32 offen lds\n\t"
    "v_exp_f32 v120, v120\n\t"
    "v_mfma_f32_16x16x32_bf16 v[52:55], a[52:55], v[136:139], v[52:55]\n\t"
    "ds_read_b128 a[20:23], v169 offset:36864\n\t"
    "v_cvt_pk_bf16_f32 v153, v118, v119\n\t"
    "v_mfma_f32_16x16x32_bf16 v[84:87], a[52:55], v[140:143], v[84:87]\n\t"
    "ds_read_b128 a[24:27], v170 offset:36864\n\t"
    "buffer_load_dwordx4 v105, s[52:55], s32 offen offset:1024 lds\n\t"
    "v_exp_f32 v121, v121\n\t"
    "v_mfma_f32_16x16x32_bf16 v[88:91], a[56:59], v[140:143], v[88:91]\n\t"
    "ds_read_b128 a[28:31], v171 offset:36864\n\t"
    "v_cvt_pk_bf16_f32 v146, v120, v121\n\t"
    "v_mfma_f32_16x16x32_bf16 v[56:59], a[56:59], v[136:139], v[56:59]\n\t"
    "buffer_load_dwordx4 v106, s[52:55], s32 offen offset:2048 lds\n\t"
    "v_exp_f32 v122, v122\n\t"
    "v_mfma_f32_16x16x32_bf16 v[24:27], a[56:59], v[132:135], v[24:27]\n\t"
    "v_exp_f32 v123, v123\n\t"
    "v_mfma_f32_16x16x32_bf16 v[28:31], a[60:63], v[132:135], v[28:31]\n\t"
    "v_exp_f32 v124, v124\n\t"
    "v_mfma_f32_16x16x32_bf16 v[60:63], a[60:63], v[136:139], v[60:63]\n\t"
    "buffer_load_dwordx4 v107, s[52:55], s32 offen offset:3072 lds\n\t"
    "s_add_u32 s32, s34, 10\n\t"
    "s_and_b32 s32, s32, 31\n\t"
    "s_mul_i32 s32, s32, 0xa000\n\t"
    "v_cvt_pk_bf16_f32 v147, v122, v123\n\t"
    "v_exp_f32 v125, v125\n\t"
    "v_exp_f32 v126, v126\n\t"
    "v_cvt_pk_bf16_f32 v150, v124, v125\n\t"
    "v_mfma_f32_16x16x32_bf16 v[92:95], a[60:63], v[140:143], v[92:95]\n\t"
    "s_waitcnt lgkmcnt(0)\n\t"
    "s_barrier\n\t"
    "v_mfma_f32_16x16x32_bf16 v[108:111], a[0:3], v[196:199], v[156:159]\n\t"
    "ds_read_b64_tr_b16 a[32:33], v176 offset:57344\n\t"
    "ds_read_b64_tr_b16 a[34:35], v176 offset:61440\n\t"
    "v_mfma_f32_16x16x32_bf16 v[108:111], a[4:7], v[200:203], v[108:111]\n\t"
    "v_exp_f32 v127, v127\n\t"
    "v_mfma_f32_16x16x32_bf16 v[108:111], a[8:11], v[204:207], v[108:111]\n\t"
    "s_waitcnt vmcnt(12)\n\t"
    "ds_read_b64_tr_b16 a[96:97], v192\n\t"
    "ds_read_b64_tr_b16 a[36:37], v177 offset:57344\n\t"
    "v_mfma_f32_16x16x32_bf16 v[108:111], a[12:15], v[208:211], v[108:111]\n\t"
    "s_add_u32 m0, s72, 0x8000\n\t"
    "v_exp_f32 v128, v128\n\t"
    "v_mfma_f32_16x16x32_bf16 v[112:115], a[0:3], v[212:215], v[160:163]\n\t"
    "ds_read_b64_tr_b16 a[38:39], v177 offset:61440\n\t"
    "v_cvt_pk_bf16_f32 v151, v126, v127\n\t"
    "v_mfma_f32_16x16x32_bf16 v[112:115], a[4:7], v[216:219], v[112:115]\n\t"
    "buffer_load_dwordx4 v104, s[52:55], s32 offen lds\n\t"
    "v_mfma_f32_16x16x32_bf16 v[112:115], a[8:11], v[220:223], v[112:115]\n\t"
    "v_exp_f32 v129, v129\n\t"
    "v_mfma_f32_16x16x32_bf16 v[112:115], a[12:15], v[224:227], v[112:115]\n\t"
    "ds_read_b64_tr_b16 a[98:99], v192 offset:2048\n\t"
    "v_cvt_pk_bf16_f32 v154, v128, v129\n\t"
    "v_mfma_f32_16x16x32_bf16 v[116:119], a[0:3], v[228:231], v[164:167]\n\t"
    "v_exp_f32 v130, v130\n\t"
    "v_mfma_f32_16x16x32_bf16 v[116:119], a[4:7], v[232:235], v[116:119]\n\t"
    "ds_read_b64_tr_b16 a[40:41], v178 offset:57344\n\t"
    "ds_read_b64_tr_b16 a[42:43], v178 offset:61440\n\t"
    "v_mfma_f32_16x16x32_bf16 v[116:119], a[8:11], v[236:239], v[116:119]\n\t"
    "buffer_load_dwordx4 v105, s[52:55], s32 offen offset:1024 lds\n\t"
    "v_mfma_f32_16x16x32_bf16 v[116:119], a[12:15], v[240:243], v[116:119]\n\t"
    "v_exp_f32 v131, v131\n\t"
    "v_mfma_f32_16x16x32_bf16 v[120:123], a[16:19], v[196:199], v[156:159]\n\t"
    "ds_read_b64_tr_b16 a[100:101], v193\n\t"
    "v_cvt_pk_bf16_f32 v155, v130, v131\n\t"
    "v_mfma_f32_16x16x32_bf16 v[120:123], a[20:23], v[200:203], v[120:123]\n\t"
    "ds_read_b64_tr_b16 a[44:45], v179 offset:57344\n\t"
    "ds_read_b64_tr_b16 a[46:47], v179 offset:61440\n\t"
    "v_mfma_f32_16x16x32_bf16 v[120:123], a[24:27], v[204:207], v[120:123]\n\t"
    "ds_read_b64_tr_b16 a[102:103], v193 offset:2048\n\t"
    "ds_read_b64_tr_b16 a[48:49], v180 offset:57344\n\t"
    "v_mfma_f32_16x16x32_bf16 v[120:123], a[28:31], v[208:211], v[120:123]\n\t"
    "v_exp_f32 v108, v108\n\t"
    "v_mfma_f32_16x16x32_bf16 v[124:127], a[16:19], v[212:215], v[160:163]\n\t"
    "ds_read_b64_tr_b16 a[50:51], v180 offset:61440\n\t"
    "ds_read_b64_tr_b16 a[52:53], v181 offset:57344\n\t"
    "v_mfma_f32_16x16x32_bf16 v[124:127], a[20:23], v[216:219], v[124:127]\n\t"
    "v_exp_f32 v109, v109\n\t"
    "v_mfma_f32_16x16x32_bf16 v[124:127], a[24:27], v[220:223], v[124:127]\n\t"
    "ds_read_b64_tr_b16 a[54:55], v181 offset:61440\n\t"
    "ds_read_b64_tr_b16 a[56:57], v182 offset:57344\n\t"
    "v_mfma_f32_16x16x32_bf16 v[124:127], a[28:31], v[224:227], v[124:127]\n\t"
    "v_exp_f32 v110, v110\n\t"
    "v_mfma_f32_16x16x32_bf16 v[128:131], a[16:19], v[228:231], v[164:167]\n\t"
    "buffer_load_dwordx4 v106, s[52:55], s32 offen offset:2048 lds\n\t"
    "s_branch Lcs6%=\n\t"
    ".p2align 12\n\t"
    "Lc6%=:\n\t"
    "s_setpc_b64 s[22:23]\n\t"
    "Lcs6%=:\n\t"
    "v_mfma_f32_16x16x32_bf16 v[128:131], a[20:23], v[232:235], v[128:131]\n\t"
    "ds_read_b64_tr_b16 a[104:105], v194\n\t"
    "v_cvt_pk_bf16_f32 v132, v108, v109\n\t"
    "v_mfma_f32_16x16x32_bf16 v[128:131], a[24:27], v[236:239], v[128:131]\n\t"
    "v_exp_f32 v111, v111\n\t"
    "v_mfma_f32_16x16x32_bf16 v[128:131], a[28:31], v[240:243], v[128:131]\n\t"
    "ds_read_b64_tr_b16 a[58:59], v182 offset:61440\n\t"
    "v_cvt_pk_bf16_f32 v133, v110, v111\n\t"
    "v_mfma_f32_16x16x32_bf16 v[96:99], a[240:243], v[144:147], v[96:99]\n\t"
    "v_exp_f32 v112, v112\n\t"
    "v_mfma_f32_16x16x32_bf16 v[96:99], a[244:247], v[148:151], v[96:99]\n\t"
    "buffer_load_dwordx4 v107, s[52:55], s32 offen offset:3072 lds\n\t"
    "v_mfma_f32_16x16x32_bf16 v[96:99], a[248:251], v[152:155], v[96:99]\n\t"
    "s_waitcnt lgkmcnt(15)\n\t"
    "ds_read_b64_tr_b16 a[106:107], v194 offset:2048\n\t"
    "ds_read_b64_tr_b16 a[60:61], v183 offset:57344\n\t"
    "v_mfma_f32_16x16x32_bf16 v[64:67], a[32:35], v[152:155], v[64:67]\n\t"
    "s_add_u32 s32, s34, 4\n\t"
    "v_exp_f32 v113, v113\n\t"
    "v_mfma_f32_16x16x32_bf16 v[32:35], a[32:35], v[148:151], v[32:35]\n\t"
    "s_and_b32 s32, s32, 31\n\t"
    "ds_read_b64_tr_b16 a[62:63], v183 offset:61440\n\t"
    "ds_read_b64_tr_b16 a[108:109], v195\n\t"
    "v_mfma_f32_16x16x32_bf16 v[0:3], a[32:35], v[144:147], v[0:3]\n\t"
    "s_waitcnt lgkmcnt(15)\n\t"
    "s_mul_i32 s32, s32, 0xa000\n\t"
    "v_cvt_pk_bf16_f32 v136, v112, v113\n\t"
    "v_mfma_f32_16x16x32_bf16 v[4:7], a[36:39], v[144:147], v[4:7]\n\t"
    "s_add_u32 m0, s72, 0\n\t"
    "ds_read_b64_tr_b16 a[110:111], v195 offset:2048\n\t"
    "v_exp_f32 v114, v114\n\t"
    "v_mfma_f32_16x16x32_bf16 v[36:39], a[36:39], v[148:151], v[36:39]\n\t"
    "v_exp_f32 v115, v115\n\t"
    "v_mfma_f32_16x16x32_bf16 v[68:71], a[36:39], v[152:155], v[68:71]\n\t"
    "s_waitcnt lgkmcnt(0)\n\t"
    "buffer_load_dwordx4 v104, s[52:55], s32 offen lds\n\t"
    "v_mfma_f32_16x16x32_bf16 v[72:75], a[40:43], v[152:155], v[72:75]\n\t"
    "ds_read_b128 a[0:3], v168 offset:49152\n\t"
    "v_cvt_pk_bf16_f32 v137, v114, v115\n\t"
    "v_mfma_f32_16x16x32_bf16 v[40:43], a[40:43], v[148:151], v[40:43]\n\t"
    "v_exp_f32 v116, v116\n\t"
    "v_mfma_f32_16x16x32_bf16 v[8:11], a[40:43], v[144:147], v[8:11]\n\t"
    "v_exp_f32 v117, v117\n\t"
    "v_mfma_f32_16x16x32_bf16 v[12:15], a[44:47], v[144:147], v[12:15]\n\t"
    "buffer_load_dwordx4 v105, s[52:55], s32 offen offset:1024 lds\n\t"
    "v_mfma_f32_16x16x32_bf16 v[44:47], a[44:47], v[148:151], v[44:47]\n\t"
    "ds_read_b128 a[4:7], v169 offset:49152\n\t"
    "ds_read_b128 a[8:11], v170 offset:49152\n\t"
    "v_mfma_f32_16x16x32_bf16 v[76:79], a[44:47], v[152:155], v[76:79]\n\t"
    "v_exp_f32 v118, v118\n\t"
    "v_cvt_pk_bf16_f32 v140, v116, v117\n\t"
    "v_mfma_f32_16x16x32_bf16 v[80:83], a[48:51], v[152:155], v[80:83]\n\t"
    "ds_read_b128 a[12:15], v171 offset:49152\n\t"
    "ds_read_b128 a[16:19], v168 offset:53248\n\t"
    "v_mfma_f32_16x16x32_bf16 v[48:51], a[48:51], v[148:151], v[48:51]\n\t"
    "v_exp_f32 v119, v119\n\t"
    "v_mfma_f32_16x16x32_bf16 v[16:19], a[48:51], v[144:147], v[16:19]\n\t"
    "ds_read_b128 a[20:23], v169 offset:53248\n\t"
    "v_cvt_pk_bf16_f32 v141, v118, v119\n\t"
    "v_mfma_f32_16x16x32_bf16 v[20:23], a[52:55], v[144:147], v[20:23]\n\t"
    "buffer_load_dwordx4 v106, s[52:55], s32 offen offset:2048 lds\n\t"
    "v_exp_f32 v120, v120\n\t"
    "v_mfma_f32_16x16x32_bf16 v[52:55], a[52:55], v[148:151], v[52:55]\n\t"
    "ds_read_b128 a[24:27], v170 offset:53248\n\t"
    "ds_read_b128 a[28:31], v171 offset:53248\n\t"
    "v_mfma_f32_16x16x32_bf16 v[84:87], a[52:55], v[152:155], v[84:87]\n\t"
    "v_exp_f32 v121, v121\n\t"
    "v_mfma_f32_16x16x32_bf16 v[88:91], a[56:59], v[152:155], v[88:91]\n\t"
    "v_exp_f32 v122, v122\n\t"
    "v_mfma_f32_16x16x32_bf16 v[56:59], a[56:59], v[148:151], v[56:59]\n\t"
    "buffer_load_dwordx4 v107, s[52:55], s32 offen offset:3072 lds\n\t"
    "v_mfma_f32_16x16x32_bf16 v[24:27], a[56:59], v[144:147], v[24:27]\n\t"
    "v_cvt_pk_bf16_f32 v134, v120, v121\n\t"
    "v_exp_f32 v123, v123\n\t"
    "v_mfma_f32_16x16x32_bf16 v[28:31], a[60:63], v[144:147], v[28:31]\n\t"
    "s_add_u32 s32, s34, 11\n\t"
    "v_exp_f32 v124, v124\n\t"
    "v_mfma_f32_16x16x32_bf16 v[60:63], a[60:63], v[148:151], v[60:63]\n\t"
    "s_and_b32 s32, s32, 31\n\t"
    "s_mul_i32 s32, s32, 0xa000\n\t"
    "v_cvt_pk_bf16_f32 v135, v122, v123\n\t"
    "v_exp_f32 v125, v125\n\t"
    "v_exp_f32 v126, v126\n\t"
    "v_cvt_pk_bf16_f32 v138, v124, v125\n\t"
    "v_mfma_f32_16x16x32_bf16 v[92:95], a[60:63], v[152:155], v[92:95]\n\t"
    "s_waitcnt lgkmcnt(0)\n\t"
    "s_barrier\n\t"
    "v_mfma_f32_16x16x32_bf16 v[108:111], a[0:3], v[196:199], v[156:159]\n\t"
    "ds_read_b64_tr_b16 a[32:33], v184 offset:8192\n\t"
    "ds_read_b64_tr_b16 a[34:35], v184 offset:12288\n\t"
    "v_mfma_f32_16x16x32_bf16 v[108:111], a[4:7], v[200:203], v[108:111]\n\t"
    "v_exp_f32 v127, v127\n\t"
    "v_mfma_f32_16x16x32_bf16 v[108:111], a[8:11], v[204:207], v[108:111]\n\t"
    "s_waitcnt vmcnt(12)\n\t"
    "ds_read_b64_tr_b16 a[112:113], v100 offset:16384\n\t"
    "ds_read_b64_tr_b16 a[36:37], v185 offset:8192\n\t"
    "v_mfma_f32_16x16x32_bf16 v[108:111], a[12:15], v[208:211], v[108:111]\n\t"
    "s_add_u32 m0, s72, 0xc000\n\t"
    "ds_read_b64_tr_b16 a[38:39], v185 offset:12288\n\t"
    "v_cvt_pk_bf16_f32 v139, v126, v127\n\t"
    "v_mfma_f32_16x16x32_bf16 v[112:115], a[0:3], v[212:215], v[160:163]\n\t"
    "v_exp_f32 v128, v128\n\t"
    "v_mfma_f32_16x16x32_bf16 v[112:115], a[4:7], v[216:219], v[112:115]\n\t"
    "buffer_load_dwordx4 v104, s[52:55], s32 offen lds\n\t"
    "v_mfma_f32_16x16x32_bf16 v[112:115], a[8:11], v[220:223], v[112:115]\n\t"
    "ds_read_b64_tr_b16 a[114:115], v100 offset:18432\n\t"
    "ds_read_b64_tr_b16 a[116:117], v101 offset:16384\n\t"
    "v_mfma_f32_16x16x32_bf16 v[112:115], a[12:15], v[224:227], v[112:115]\n\t"
    "v_exp_f32 v129, v129\n\t"
    "v_mfma_f32_16x16x32_bf16 v[116:119], a[0:3], v[228:231], v[164:167]\n\t"
    "ds_read_b64_tr_b16 a[40:41], v186 offset:8192\n\t"
    "v_cvt_pk_bf16_f32 v142, v128, v129\n\t"
    "v_mfma_f32_16x16x32_bf16 v[116:119], a[4:7], v[232:235], v[116:119]\n\t"
    "v_exp_f32 v130, v130\n\t"
    "v_mfma_f32_16x16x32_bf16 v[116:119], a[8:11], v[236:239], v[116:119]\n\t"
    "ds_read_b64_tr_b16 a[118:119], v101 offset:18432\n\t"
    "ds_read_b64_tr_b16 a[42:43], v186 offset:12288\n\t"
    "v_mfma_f32_16x16x32_bf16 v[116:119], a[12:15], v[240:243], v[116:119]\n\t"
    "buffer_load_dwordx4 v105, s[52:55], s32 offen offset:1024 lds\n\t"
    "v_mfma_f32_16x16x32_bf16 v[120:123], a[16:19], v[196:199], v[156:159]\n\t"
    "v_exp_f32 v131, v131\n\t"
    "v_mfma_f32_16x16x32_bf16 v[120:123], a[20:23], v[200:203], v[120:123]\n\t"
    "ds_read_b64_tr_b16 a[120:121], v102 offset:16384\n\t"
    "v_cvt_pk_bf16_f32 v143, v130, v131\n\t"
    "v_mfma_f32_16x16x32_bf16 v[120:123], a[24:27], v[204:207], v[120:123]\n\t"
    "ds_read_b64_tr_b16 a[44:45], v187 offset:8192\n\t"
    "ds_read_b64_tr_b16 a[46:47], v187 offset:12288\n\t"
    "v_mfma_f32_16x16x32_bf16 v[120:123], a[28:31], v[208:211], v[120:123]\n\t"
    "ds_read_b64_tr_b16 a[122:123], v102 offset:18432\n\t"
    "ds_read_b64_tr_b16 a[124:125], v103 offset:16384\n\t"
    "v_mfma_f32_16x16x32_bf16 v[124:127], a[16:19], v[212:215], v[160:163]\n\t"
    "v_exp_f32 v108, v108\n\t"
    "v_mfma_f32_16x16x32_bf16 v[124:127], a[20:23], v[216:219], v[124:127]\n\t"
    "ds_read_b64_tr_b16 a[48:49], v188 offset:8192\n\t"
    "ds_read_b64_tr_b16 a[50:51], v188 offset:12288\n\t"
    "v_mfma_f32_16x16x32_bf16 v[124:127], a[24:27], v[220:223], v[124:127]\n\t"
    "s_waitcnt vmcnt(10)\n\t"
    "ds_read_b64_tr_b16 a[52:53], v189 offset:8192\n\t"
    "ds_read_b64_tr_b16 a[126:127], v103 offset:18432\n\t"
    "v_mfma_f32_16x16x32_bf16 v[124:127], a[28:31], v[224:227], v[124:127]\n\t"
    "v_exp_f32 v109, v109\n\t"
    "v_mfma_f32_16x16x32_bf16 v[128:131], a[16:19], v[228:231], v[164:167]\n\t"
    "ds_read_b64_tr_b16 a[54:55], v189 offset:12288\n\t"
    "ds_read_b64_tr_b16 a[56:57], v190 offset:8192\n\t"
    "v_mfma_f32_16x16x32_bf16 v[128:131], a[20:23], v[232:235], v[128:131]\n\t"
    "buffer_load_dwordx4 v106, s[52:55], s32 offen offset:2048 lds\n\t"
    "v_mfma_f32_16x16x32_bf16 v[128:131], a[24:27], v[236:239], v[128:131]\n\t"
    "v_exp_f32 v110, v110\n\t"
    "v_mfma_f32_16x16x32_bf16 v[128:131], a[28:31], v[240:243], v[128:131]\n\t"
    "ds_read_b64_tr_b16 a[128:129], v100\n\t"
    "v_cvt_pk_bf16_f32 v144, v108, v109\n\t"
    "v_mfma_f32_16x16x32_bf16 v[96:99], a[240:243], v[132:135], v[96:99]\n\t"
    "v_exp_f32 v111, v111\n\t"
    "v_mfma_f32_16x16x32_bf16 v[96:99], a[244:247], v[136:139], v[96:99]\n\t"
    "ds_read_b64_tr_b16 a[130:131], v100 offset:2048\n\t"
    "ds_read_b64_tr_b16 a[58:59], v190 offset:12288\n\t"
    "v_mfma_f32_16x16x32_bf16 v[96:99], a[248:251], v[140:143], v[96:99]\n\t"
    "s_waitcnt lgkmcnt(15)\n\t"
    "buffer_load_dwordx4 v107, s[52:55], s32 offen offset:3072 lds\n\t"
    "v_exp_f32 v112, v112\n\t"
    "v_mfma_f32_16x16x32_bf16 v[64:67], a[32:35], v[140:143], v[64:67]\n\t"
    "s_add_u32 s32, s34, 7\n\t"
    "ds_read_b64_tr_b16 a[132:133], v101\n\t"
    "ds_read_b64_tr_b16 a[60:61], v191 offset:8192\n\t"
    "v_cvt_pk_bf16_f32 v145, v110, v111\n\t"
    "v_mfma_f32_16x16x32_bf16 v[32:35], a[32:35], v[136:139], v[32:35]\n\t"
    "s_and_b32 s32, s32, 31\n\t"
    "v_exp_f32 v113, v113\n\t"
    "v_mfma_f32_16x16x32_bf16 v[0:3], a[32:35], v[132:135], v[0:3]\n\t"
    "s_mul_i32 s32, s32, 0xa000\n\t"
    "ds_read_b64_tr_b16 a[134:135], v101 offset:2048\n\t"
    "v_mfma_f32_16x16x32_bf16 v[4:7], a[36:39], v[132:135], v[4:7]\n\t"
    "s_add_u32 m0, s72, 0x24000\n\t"
    "v_exp_f32 v114, v114\n\t"
    "v_cvt_pk_bf16_f32 v148, v112, v113\n\t"
    "v_mfma_f32_16x16x32_bf16 v[36:39], a[36:39], v[136:139], v[36:39]\n\t"
    "s_waitcnt lgkmcnt(8)\n\t"
    "ds_read_b64_tr_b16 a[62:63], v191 offset:12288\n\t"
    "ds_read_b64_tr_b16 a[136:137], v102\n\t"
    "v_mfma_f32_16x16x32_bf16 v[68:71], a[36:39], v[140:143], v[68:71]\n\t"
    "v_exp_f32 v115, v115\n\t"
    "v_mfma_f32_16x16x32_bf16 v[72:75], a[40:43], v[140:143], v[72:75]\n\t"
    "ds_read_b64_tr_b16 a[138:139], v102 offset:2048\n\t"
    "ds_read_b64_tr_b16 a[140:141], v103\n\t"
    "ds_read_b64_tr_b16 a[142:143], v103 offset:2048\n\t"
    "buffer_load_dwordx4 v104, s[52:55], s32 offen lds\n\t"
    "v_mfma_f32_16x16x32_bf16 v[40:43], a[40:43], v[136:139], v[40:43]\n\t"
    "buffer_load_dwordx4 v105, s[52:55], s32 offen offset:1024 lds\n\t"
    "v_exp_f32 v116, v116\n\t"
    "v_mfma_f32_16x16x32_bf16 v[8:11], a[40:43], v[132:135], v[8:11]\n\t"
    "ds_read_b128 a[0:3], v172\n\t"
    "v_cvt_pk_bf16_f32 v149, v114, v115\n\t"
    "v_mfma_f32_16x16x32_bf16 v[12:15], a[44:47], v[132:135], v[12:15]\n\t"
    "v_exp_f32 v117, v117\n\t"
    "v_mfma_f32_16x16x32_bf16 v[44:47], a[44:47], v[136:139], v[44:47]\n\t"
    "s_waitcnt lgkmcnt(1)\n\t"
    "ds_read_b128 a[4:7], v173\n\t"
    "v_cvt_pk_bf16_f32 v152, v116, v117\n\t"
    "v_mfma_f32_16x16x32_bf16 v[76:79], a[44:47], v[140:143], v[76:79]\n\t"
    "buffer_load_dwordx4 v106, s[52:55], s32 offen offset:2048 lds\n\t"
    "buffer_load_dwordx4 v107, s[52:55], s32 offen offset:3072 lds\n\t"
    "v_exp_f32 v118, v118\n\t"
    "v_mfma_f32_16x16x32_bf16 v[80:83], a[48:51], v[140:143], v[80:83]\n\t"
    "s_add_u32 s32, s34, 12\n\t"
    "ds_read_b128 a[8:11], v174\n\t"
    "ds_read_b128 a[12:15], v175\n\t"
    "v_mfma_f32_16x16x32_bf16 v[48:51], a[48:51], v[136:139], v[48:51]\n\t"
    "s_and_b32 s32, s32, 31\n\t"
    "v_exp_f32 v119, v119\n\t"
    "v_mfma_f32_16x16x32_bf16 v[16:19], a[48:51], v[132:135], v[16:19]\n\t"
    "s_mul_i32 s32, s32, 0xa000\n\t"
    "s_add_u32 m0, s72, 0x20000\n\t"
    "ds_read_b128 a[16:19], v172 offset:4096\n\t"
    "v_mfma_f32_16x16x32_bf16 v[20:23], a[52:55], v[132:135], v[20:23]\n\t"
    "buffer_load_dwordx4 v104, s[52:55], s32 offen lds\n\t"
    "v_exp_f32 v120, v120\n\t"
    "v_mfma_f32_16x16x32_bf16 v[52:55], a[52:55], v[136:139], v[52:55]\n\t"
    "ds_read_b128 a[20:23], v173 offset:4096\n\t"
    "v_cvt_pk_bf16_f32 v153, v118, v119\n\t"
    "v_mfma_f32_16x16x32_bf16 v[84:87], a[52:55], v[140:143], v[84:87]\n\t"
    "ds_read_b128 a[24:27], v174 offset:4096\n\t"
    "buffer_load_dwordx4 v105, s[52:55], s32 offen offset:1024 lds\n\t"
    "v_exp_f32 v121, v121\n\t"
    "v_mfma_f32_16x16x32_bf16 v[88:91], a[56:59], v[140:143], v[88:91]\n\t"
    "ds_read_b128 a[28:31], v175 offset:4096\n\t"
    "v_cvt_pk_bf16_f32 v146, v120, v121\n\t"
    "v_mfma_f32_16x16x32_bf16 v[56:59], a[56:59], v[136:139], v[56:59]\n\t"
    "buffer_load_dwordx4 v106, s[52:55], s32 offen offset:2048 lds\n\t"
    "v_exp_f32 v122, v122\n\t"
    "v_mfma_f32_16x16x32_bf16 v[24:27], a[56:59], v[132:135], v[24:27]\n\t"
    "v_exp_f32 v123, v123\n\t"
    "v_mfma_f32_16x16x32_bf16 v[28:31], a[60:63], v[132:135], v[28:31]\n\t"
    "v_exp_f32 v124, v124\n\t"
    "v_mfma_f32_16x16x32_bf16 v[60:63], a[60:63], v[136:139], v[60:63]\n\t"
    "buffer_load_dwordx4 v107, s[52:55], s32 offen offset:3072 lds\n\t"
    "s_add_u32 s32, s34, 15\n\t"
    "s_and_b32 s32, s32, 31\n\t"
    "s_mul_i32 s32, s32, 0xa000\n\t"
    "v_cvt_pk_bf16_f32 v147, v122, v123\n\t"
    "v_exp_f32 v125, v125\n\t"
    "v_exp_f32 v126, v126\n\t"
    "v_cvt_pk_bf16_f32 v150, v124, v125\n\t"
    "v_mfma_f32_16x16x32_bf16 v[92:95], a[60:63], v[140:143], v[92:95]\n\t"
    "s_waitcnt lgkmcnt(0)\n\t"
    "s_barrier\n\t"
    "v_mfma_f32_16x16x32_bf16 v[108:111], a[0:3], v[196:199], v[156:159]\n\t"
    "ds_read_b64_tr_b16 a[32:33], v184 offset:24576\n\t"
    "ds_read_b64_tr_b16 a[34:35], v184 offset:28672\n\t"
    "v_mfma_f32_16x16x32_bf16 v[108:111], a[4:7], v[200:203], v[108:111]\n\t"
    "v_exp_f32 v127, v127\n\t"
    "v_mfma_f32_16x16x32_bf16 v[108:111], a[8:11], v[204:207], v[108:111]\n\t"
    "ds_read_b64_tr_b16 a[144:145], v192 offset:16384\n\t"
    "ds_read_b64_tr_b16 a[36:37], v185 offset:24576\n\t"
    "v_mfma_f32_16x16x32_bf16 v[108:111], a[12:15], v[208:211], v[108:111]\n\t"
    "s_add_u32 m0, s72, 0x10000\n\t"
    "ds_read_b64_tr_b16 a[38:39], v185 offset:28672\n\t"
    "v_cvt_pk_bf16_f32 v151, v126, v127\n\t"
    "v_mfma_f32_16x16x32_bf16 v[112:115], a[0:3], v[212:215], v[160:163]\n\t"
    "v_exp_f32 v128, v128\n\t"
    "v_mfma_f32_16x16x32_bf16 v[112:115], a[4:7], v[216:219], v[112:115]\n\t"
    "buffer_load_dwordx4 v104, s[52:55], s32 offen lds\n\t"
    "v_mfma_f32_16x16x32_bf16 v[112:115], a[8:11], v[220:223], v[112:115]\n\t"
    "ds_read_b64_tr_b16 a[146:147], v192 offset:18432\n\t"
    "ds_read_b64_tr_b16 a[148:149], v193 offset:16384\n\t"
    "v_mfma_f32_16x16x32_bf16 v[112:115], a[12:15], v[224:227], v[112:115]\n\t"
    "v_exp_f32 v129, v129\n\t"
    "v_mfma_f32_16x16x32_bf16 v[116:119], a[0:3], v[228:231], v[164:167]\n\t"
    "ds_read_b64_tr_b16 a[40:41], v186 offset:24576\n\t"
    "v_cvt_pk_bf16_f32 v154, v128, v129\n\t"
    "v_mfma_f32_16x16x32_bf16 v[116:119], a[4:7], v[232:235], v[116:119]\n\t"
    "v_exp_f32 v130, v130\n\t"
    "v_mfma_f32_16x16x32_bf16 v[116:119], a[8:11], v[236:239], v[116:119]\n\t"
    "ds_read_b64_tr_b16 a[150:151], v193 offset:18432\n\t"
    "ds_read_b64_tr_b16 a[42:43], v186 offset:28672\n\t"
    "v_mfma_f32_16x16x32_bf16 v[116:119], a[12:15], v[240:243], v[116:119]\n\t"
    "buffer_load_dwordx4 v105, s[52:55], s32 offen offset:1024 lds\n\t"
    "v_mfma_f32_16x16x32_bf16 v[120:123], a[16:19], v[196:199], v[156:159]\n\t"
    "v_exp_f32 v131, v131\n\t"
    "v_mfma_f32_16x16x32_bf16 v[120:123], a[20:23], v[200:203], v[120:123]\n\t"
    "ds_read_b64_tr_b16 a[152:153], v194 offset:16384\n\t"
    "v_cvt_pk_bf16_f32 v155, v130, v131\n\t"
    "v_mfma_f32_16x16x32_bf16 v[120:123], a[24:27], v[204:207], v[120:123]\n\t"
    "ds_read_b64_tr_b16 a[44:45], v187 offset:24576\n\t"
    "ds_read_b64_tr_b16 a[46:47], v187 offset:28672\n\t"
    "v_mfma_f32_16x16x32_bf16 v[120:123], a[28:31], v[208:211], v[120:123]\n\t"
    "ds_read_b64_tr_b16 a[154:155], v194 offset:18432\n\t"
    "ds_read_b64_tr_b16 a[156:157], v195 offset:16384\n\t"
    "v_mfma_f32_16x16x32_bf16 v[124:127], a[16:19], v[212:215], v[160:163]\n\t"
    "v_exp_f32 v108, v108\n\t"
    "v_mfma_f32_16x16x32_bf16 v[124:127], a[20:23], v[216:219], v[124:127]\n\t"
    "ds_read_b64_tr_b16 a[48:49], v188 offset:24576\n\t"
    "ds_read_b64_tr_b16 a[50:51], v188 offset:28672\n\t"
    "v_mfma_f32_16x16x32_bf16 v[124:127], a[24:27], v[220:223], v[124:127]\n\t"
    "s_waitcnt vmcnt(14)\n\t"
    "ds_read_b64_tr_b16 a[52:53], v189 offset:24576\n\t"
    "ds_read_b64_tr_b16 a[158:159], v195 offset:18432\n\t"
    "v_mfma_f32_16x16x32_bf16 v[124:127], a[28:31], v[224:227], v[124:127]\n\t"
    "v_exp_f32 v109, v109\n\t"
    "v_mfma_f32_16x16x32_bf16 v[128:131], a[16:19], v[228:231], v[164:167]\n\t"
    "ds_read_b64_tr_b16 a[54:55], v189 offset:28672\n\t"
    "ds_read_b64_tr_b16 a[56:57], v190 offset:24576\n\t"
    "v_mfma_f32_16x16x32_bf16 v[128:131], a[20:23], v[232:235], v[128:131]\n\t"
    "buffer_load_dwordx4 v106, s[52:55], s32 offen offset:2048 lds\n\t"
    "v_mfma_f32_16x16x32_bf16 v[128:131], a[24:27], v[236:239], v[128:131]\n\t"
    "v_exp_f32 v110, v110\n\t"
    "v_mfma_f32_16x16x32_bf16 v[128:131], a[28:31], v[240:243], v[128:131]\n\t"
    "ds_read_b64_tr_b16 a[160:161], v192\n\t"
    "v_cvt_pk_bf16_f32 v132, v108, v109\n\t"
    "v_mfma_f32_16x16x32_bf16 v[96:99], a[240:243], v[144:147], v[96:99]\n\t"
    "v_exp_f32 v111, v111\n\t"
    "v_mfma_f32_16x16x32_bf16 v[96:99], a[244:247], v[148:151], v[96:99]\n\t"
    "ds_read_b64_tr_b16 a[162:163], v192 offset:2048\n\t"
    "ds_read_b64_tr_b16 a[58:59], v190 offset:28672\n\t"
    "v_mfma_f32_16x16x32_bf16 v[96:99], a[248:251], v[152:155], v[96:99]\n\t"
    "s_waitcnt lgkmcnt(15)\n\t"
    "buffer_load_dwordx4 v107, s[52:55], s32 offen offset:3072 lds\n\t"
    "v_exp_f32 v112, v112\n\t"
    "v_mfma_f32_16x16x32_bf16 v[64:67], a[32:35], v[152:155], v[64:67]\n\t"
    "s_add_u32 s32, s34, 16\n\t"
    "ds_read_b64_tr_b16 a[164:165], v193\n\t"
    "ds_read_b64_tr_b16 a[60:61], v191 offset:24576\n\t"
    "v_cvt_pk_bf16_f32 v133, v110, v111\n\t"
    "v_mfma_f32_16x16x32_bf16 v[32:35], a[32:35], v[148:151], v[32:35]\n\t"
    "s_and_b32 s32, s32, 31\n\t"
    "v_exp_f32 v113, v113\n\t"
    "v_mfma_f32_16x16x32_bf16 v[0:3], a[32:35], v[144:147], v[0:3]\n\t"
    "s_mul_i32 s32, s32, 0xa000\n\t"
    "ds_read_b64_tr_b16 a[166:167], v193 offset:2048\n\t"
    "v_mfma_f32_16x16x32_bf16 v[4:7], a[36:39], v[144:147], v[4:7]\n\t"
    "s_add_u32 m0, s72, 0x4000\n\t"
    "v_exp_f32 v114, v114\n\t"
    "v_cvt_pk_bf16_f32 v136, v112, v113\n\t"
    "v_mfma_f32_16x16x32_bf16 v[36:39], a[36:39], v[148:151], v[36:39]\n\t"
    "s_waitcnt lgkmcnt(8)\n\t"
    "ds_read_b64_tr_b16 a[62:63], v191 offset:28672\n\t"
    "ds_read_b64_tr_b16 a[168:169], v194\n\t"
    "v_mfma_f32_16x16x32_bf16 v[68:71], a[36:39], v[152:155], v[68:71]\n\t"
    "v_exp_f32 v115, v115\n\t"
    "v_mfma_f32_16x16x32_bf16 v[72:75], a[40:43], v[152:155], v[72:75]\n\t"
    "ds_read_b64_tr_b16 a[170:171], v194 offset:2048\n\t"
    "ds_read_b64_tr_b16 a[172:173], v195\n\t"
    "ds_read_b64_tr_b16 a[174:175], v195 offset:2048\n\t"
    "buffer_load_dwordx4 v104, s[52:55], s32 offen lds\n\t"
    "v_mfma_f32_16x16x32_bf16 v[40:43], a[40:43], v[148:151], v[40:43]\n\t"
    "buffer_load_dwordx4 v105, s[52:55], s32 offen offset:1024 lds\n\t"
    "v_exp_f32 v116, v116\n\t"
    "v_mfma_f32_16x16x32_bf16 v[8:11], a[40:43], v[144:147], v[8:11]\n\t"
    "ds_read_b128 a[0:3], v172 offset:16384\n\t"
    "v_cvt_pk_bf16_f32 v137, v114, v115\n\t"
    "v_mfma_f32_16x16x32_bf16 v[12:15], a[44:47], v[144:147], v[12:15]\n\t"
    "v_exp_f32 v117, v117\n\t"
    "v_mfma_f32_16x16x32_bf16 v[44:47], a[44:47], v[148:151], v[44:47]\n\t"
    "s_waitcnt lgkmcnt(1)\n\t"
    "ds_read_b128 a[4:7], v173 offset:16384\n\t"
    "v_cvt_pk_bf16_f32 v140, v116, v117\n\t"
    "v_mfma_f32_16x16x32_bf16 v[76:79], a[44:47], v[152:155], v[76:79]\n\t"
    "buffer_load_dwordx4 v106, s[52:55], s32 offen offset:2048 lds\n\t"
    "buffer_load_dwordx4 v107, s[52:55], s32 offen offset:3072 lds\n\t"
    "v_exp_f32 v118, v118\n\t"
    "v_mfma_f32_16x16x32_bf16 v[80:83], a[48:51], v[152:155], v[80:83]\n\t"
    "s_add_u32 s32, s34, 17\n\t"
    "ds_read_b128 a[8:11], v174 offset:16384\n\t"
    "ds_read_b128 a[12:15], v175 offset:16384\n\t"
    "v_mfma_f32_16x16x32_bf16 v[48:51], a[48:51], v[148:151], v[48:51]\n\t"
    "s_and_b32 s32, s32, 31\n\t"
    "v_exp_f32 v119, v119\n\t"
    "v_mfma_f32_16x16x32_bf16 v[16:19], a[48:51], v[144:147], v[16:19]\n\t"
    "s_mul_i32 s32, s32, 0xa000\n\t"
    "s_add_u32 m0, s72, 0\n\t"
    "ds_read_b128 a[16:19], v172 offset:20480\n\t"
    "v_mfma_f32_16x16x32_bf16 v[20:23], a[52:55], v[144:147], v[20:23]\n\t"
    "buffer_load_dwordx4 v104, s[52:55], s32 offen lds\n\t"
    "v_exp_f32 v120, v120\n\t"
    "v_mfma_f32_16x16x32_bf16 v[52:55], a[52:55], v[148:151], v[52:55]\n\t"
    "ds_read_b128 a[20:23], v173 offset:20480\n\t"
    "v_cvt_pk_bf16_f32 v141, v118, v119\n\t"
    "v_mfma_f32_16x16x32_bf16 v[84:87], a[52:55], v[152:155], v[84:87]\n\t"
    "ds_read_b128 a[24:27], v174 offset:20480\n\t"
    "buffer_load_dwordx4 v105, s[52:55], s32 offen offset:1024 lds\n\t"
    "v_exp_f32 v121, v121\n\t"
    "v_mfma_f32_16x16x32_bf16 v[88:91], a[56:59], v[152:155], v[88:91]\n\t"
    "ds_read_b128 a[28:31], v175 offset:20480\n\t"
    "v_cvt_pk_bf16_f32 v134, v120, v121\n\t"
    "v_mfma_f32_16x16x32_bf16 v[56:59], a[56:59], v[148:151], v[56:59]\n\t"
    "buffer_load_dwordx4 v106, s[52:55], s32 offen offset:2048 lds\n\t"
    "v_exp_f32 v122, v122\n\t"
    "v_mfma_f32_16x16x32_bf16 v[24:27], a[56:59], v[144:147], v[24:27]\n\t"
    "v_exp_f32 v123, v123\n\t"
    "v_mfma_f32_16x16x32_bf16 v[28:31], a[60:63], v[144:147], v[28:31]\n\t"
    "v_exp_f32 v124, v124\n\t"
    "v_mfma_f32_16x16x32_bf16 v[60:63], a[60:63], v[148:151], v[60:63]\n\t"
    "buffer_load_dwordx4 v107, s[52:55], s32 offen offset:3072 lds\n\t"
    "s_add_u32 s32, s34, 8\n\t"
    "s_and_b32 s32, s32, 31\n\t"
    "s_mul_i32 s32, s32, 0xa000\n\t"
    "v_cvt_pk_bf16_f32 v135, v122, v123\n\t"
    "v_exp_f32 v125, v125\n\t"
    "v_exp_f32 v126, v126\n\t"
    "v_cvt_pk_bf16_f32 v138, v124, v125\n\t"
    "v_mfma_f32_16x16x32_bf16 v[92:95], a[60:63], v[152:155], v[92:95]\n\t"
    "s_waitcnt lgkmcnt(0)\n\t"
    "s_barrier\n\t"
    "v_mfma_f32_16x16x32_bf16 v[108:111], a[0:3], v[196:199], v[156:159]\n\t"
    "ds_read_b64_tr_b16 a[32:33], v184 offset:40960\n\t"
    "ds_read_b64_tr_b16 a[34:35], v184 offset:45056\n\t"
    "v_mfma_f32_16x16x32_bf16 v[108:111], a[4:7], v[200:203], v[108:111]\n\t"
    "v_exp_f32 v127, v127\n\t"
    "v_mfma_f32_16x16x32_bf16 v[108:111], a[8:11], v[204:207], v[108:111]\n\t"
    "ds_read_b64_tr_b16 a[176:177], v192 offset:32768\n\t"
    "ds_read_b64_tr_b16 a[36:37], v185 offset:40960\n\t"
    "v_mfma_f32_16x16x32_bf16 v[108:111], a[12:15], v[208:211], v[108:111]\n\t"
    "s_add_u32 m0, s72, 0x14000\n\t"
    "v_exp_f32 v128, v128\n\t"
    "v_mfma_f32_16x16x32_bf16 v[112:115], a[0:3], v[212:215], v[160:163]\n\t"
    "ds_read_b64_tr_b16 a[38:39], v185 offset:45056\n\t"
    "v_cvt_pk_bf16_f32 v139, v126, v127\n\t"
    "v_mfma_f32_16x16x32_bf16 v[112:115], a[4:7], v[216:219], v[112:115]\n\t"
    "buffer_load_dwordx4 v104, s[52:55], s32 offen lds\n\t"
    "v_mfma_f32_16x16x32_bf16 v[112:115], a[8:11], v[220:223], v[112:115]\n\t"
    "v_exp_f32 v129, v129\n\t"
    "v_mfma_f32_16x16x32_bf16 v[112:115], a[12:15], v[224:227], v[112:115]\n\t"
    "ds_read_b64_tr_b16 a[178:179], v192 offset:34816\n\t"
    "ds_read_b64_tr_b16 a[180:181], v193 offset:32768\n\t"
    "v_mfma_f32_16x16x32_bf16 v[116:119], a[0:3], v[228:231], v[164:167]\n\t"
    "v_exp_f32 v130, v130\n\t"
    "v_mfma_f32_16x16x32_bf16 v[116:119], a[4:7], v[232:235], v[116:119]\n\t"
    "ds_read_b64_tr_b16 a[40:41], v186 offset:40960\n\t"
    "v_cvt_pk_bf16_f32 v142, v128, v129\n\t"
    "v_mfma_f32_16x16x32_bf16 v[116:119], a[8:11], v[236:239], v[116:119]\n\t"
    "buffer_load_dwordx4 v105, s[52:55], s32 offen offset:1024 lds\n\t"
    "v_mfma_f32_16x16x32_bf16 v[116:119], a[12:15], v[240:243], v[116:119]\n\t"
    "v_exp_f32 v131, v131\n\t"
    "v_mfma_f32_16x16x32_bf16 v[120:123], a[16:19], v[196:199], v[156:159]\n\t"
    "ds_read_b64_tr_b16 a[182:183], v193 offset:34816\n\t"
    "v_cvt_pk_bf16_f32 v143, v130, v131\n\t"
    "v_mfma_f32_16x16x32_bf16 v[120:123], a[20:23], v[200:203], v[120:123]\n\t"
    "ds_read_b64_tr_b16 a[42:43], v186 offset:45056\n\t"
    "ds_read_b64_tr_b16 a[184:185], v194 offset:32768\n\t"
    "v_mfma_f32_16x16x32_bf16 v[120:123], a[24:27], v[204:207], v[120:123]\n\t"
    "ds_read_b64_tr_b16 a[44:45], v187 offset:40960\n\t"
    "ds_read_b64_tr_b16 a[46:47], v187 offset:45056\n\t"
    "v_mfma_f32_16x16x32_bf16 v[120:123], a[28:31], v[208:211], v[120:123]\n\t"
    "v_exp_f32 v108, v108\n\t"
    "v_mfma_f32_16x16x32_bf16 v[124:127], a[16:19], v[212:215], v[160:163]\n\t"
    "ds_read_b64_tr_b16 a[186:187], v194 offset:34816\n\t"
    "ds_read_b64_tr_b16 a[188:189], v195 offset:32768\n\t"
    "v_mfma_f32_16x16x32_bf16 v[124:127], a[20:23], v[216:219], v[124:127]\n\t"
    "v_exp_f32 v109, v109\n\t"
    "v_mfma_f32_16x16x32_bf16 v[124:127], a[24:27], v[220:223], v[124:127]\n\t"
    "ds_read_b64_tr_b16 a[48:49], v188 offset:40960\n\t"
    "ds_read_b64_tr_b16 a[50:51], v188 offset:45056\n\t"
    "v_mfma_f32_16x16x32_bf16 v[124:127], a[28:31], v[224:227], v[124:127]\n\t"
    "ds_read_b64_tr_b16 a[52:53], v189 offset:40960\n\t"
    "v_cvt_pk_bf16_f32 v144, v108, v109\n\t"
    "v_mfma_f32_16x16x32_bf16 v[128:131], a[16:19], v[228:231], v[164:167]\n\t"
    "s_waitcnt vmcnt(18)\n\t"
    "v_exp_f32 v110, v110\n\t"
    "v_mfma_f32_16x16x32_bf16 v[128:131], a[20:23], v[232:235], v[128:131]\n\t"
    "ds_read_b64_tr_b16 a[190:191], v195 offset:34816\n\t"
    "ds_read_b64_tr_b16 a[54:55], v189 offset:45056\n\t"
    "v_mfma_f32_16x16x32_bf16 v[128:131], a[24:27], v[236:239], v[128:131]\n\t"
    "v_exp_f32 v111, v111\n\t"
    "v_mfma_f32_16x16x32_bf16 v[128:131], a[28:31], v[240:243], v[128:131]\n\t"
    "buffer_load_dwordx4 v106, s[52:55], s32 offen offset:2048 lds\n\t"
    "v_mfma_f32_16x16x32_bf16 v[96:99], a[240:243], v[132:135], v[96:99]\n\t"
    "ds_read_b64_tr_b16 a[56:57], v190 offset:40960\n\t"
    "ds_read_b64_tr_b16 a[192:193], v100 offset:16384\n\t"
    "v_mfma_f32_16x16x32_bf16 v[96:99], a[244:247], v[136:139], v[96:99]\n\t"
    "v_exp_f32 v112, v112\n\t"
    "v_mfma_f32_16x16x32_bf16 v[96:99], a[248:251], v[140:143], v[96:99]\n\t"
    "s_waitcnt lgkmcnt(15)\n\t"
    "ds_read_b64_tr_b16 a[194:195], v100 offset:18432\n\t"
    "v_cvt_pk_bf16_f32 v145, v110, v111\n\t"
    "v_mfma_f32_16x16x32_bf16 v[64:67], a[32:35], v[140:143], v[64:67]\n\t"
    "v_exp_f32 v113, v113\n\t"
    "v_mfma_f32_16x16x32_bf16 v[32:35], a[32:35], v[136:139], v[32:35]\n\t"
    "ds_read_b64_tr_b16 a[58:59], v190 offset:45056\n\t"
    "v_cvt_pk_bf16_f32 v148, v112, v113\n\t"
    "v_mfma_f32_16x16x32_bf16 v[0:3], a[32:35], v[132:135], v[0:3]\n\t"
    "v_exp_f32 v114, v114\n\t"
    "v_mfma_f32_16x16x32_bf16 v[4:7], a[36:39], v[132:135], v[4:7]\n\t"
    "buffer_load_dwordx4 v107, s[52:55], s32 offen offset:3072 lds\n\t"
    "v_mfma_f32_16x16x32_bf16 v[36:39], a[36:39], v[136:139], v[36:39]\n\t"
    "s_add_u32 s32, s34, 9\n\t"
    "ds_read_b64_tr_b16 a[196:197], v101 offset:16384\n\t"
    "ds_read_b64_tr_b16 a[60:61], v191 offset:40960\n\t"
    "v_mfma_f32_16x16x32_bf16 v[68:71], a[36:39], v[140:143], v[68:71]\n\t"
    "s_waitcnt lgkmcnt(15)\n\t"
    "v_exp_f32 v115, v115\n\t"
    "v_mfma_f32_16x16x32_bf16 v[72:75], a[40:43], v[140:143], v[72:75]\n\t"
    "s_and_b32 s32, s32, 31\n\t"
    "s_mul_i32 s32, s32, 0xa000\n\t"
    "ds_read_b64_tr_b16 a[198:199], v101 offset:18432\n\t"
    "v_mfma_f32_16x16x32_bf16 v[40:43], a[40:43], v[136:139], v[40:43]\n\t"
    "s_add_u32 m0, s72, 0x8000\n\t"
    "v_exp_f32 v116, v116\n\t"
    "v_mfma_f32_16x16x32_bf16 v[8:11], a[40:43], v[132:135], v[8:11]\n\t"
    "s_waitcnt lgkmcnt(14)\n\t"
    "ds_read_b64_tr_b16 a[62:63], v191 offset:45056\n\t"
    "ds_read_b64_tr_b16 a[200:201], v102 offset:16384\n\t"
    "v_cvt_pk_bf16_f32 v149, v114, v115\n\t"
    "v_mfma_f32_16x16x32_bf16 v[12:15], a[44:47], v[132:135], v[12:15]\n\t"
    "s_waitcnt lgkmcnt(10)\n\t"
    "v_exp_f32 v117, v117\n\t"
    "v_mfma_f32_16x16x32_bf16 v[44:47], a[44:47], v[136:139], v[44:47]\n\t"
    "ds_read_b64_tr_b16 a[202:203], v102 offset:18432\n\t"
    "ds_read_b64_tr_b16 a[204:205], v103 offset:16384\n\t"
    "buffer_load_dwordx4 v104, s[52:55], s32 offen lds\n\t"
    "v_mfma_f32_16x16x32_bf16 v[76:79], a[44:47], v[140:143], v[76:79]\n\t"
    "ds_read_b64_tr_b16 a[206:207], v103 offset:18432\n\t"
    "buffer_load_dwordx4 v105, s[52:55], s32 offen offset:1024 lds\n\t"
    "buffer_load_dwordx4 v106, s[52:55], s32 offen offset:2048 lds\n\t"
    "v_exp_f32 v118, v118\n\t"
    "v_mfma_f32_16x16x32_bf16 v[80:83], a[48:51], v[140:143], v[80:83]\n\t"
    "v_cvt_pk_bf16_f32 v152, v116, v117\n\t"
    "v_mfma_f32_16x16x32_bf16 v[48:51], a[48:51], v[136:139], v[48:51]\n\t"
    "buffer_load_dwordx4 v107, s[52:55], s32 offen offset:3072 lds\n\t"
    "v_exp_f32 v119, v119\n\t"
    "v_mfma_f32_16x16x32_bf16 v[16:19], a[48:51], v[132:135], v[16:19]\n\t"
    "s_waitcnt lgkmcnt(12)\n\t"
    "s_add_u32 s32, s34, 13\n\t"
    "s_and_b32 s32, s32, 31\n\t"
    "s_add_u32 m0, s72, 0x24000\n\t"
    "v_mfma_f32_16x16x32_bf16 v[20:23], a[52:55], v[132:135], v[20:23]\n\t"
    "s_mul_i32 s32, s32, 0xa000\n\t"
    "s_waitcnt lgkmcnt(0)\n\t"
    "buffer_load_dwordx4 v104, s[52:55], s32 offen lds\n\t"
    "v_exp_f32 v120, v120\n\t"
    "v_mfma_f32_16x16x32_bf16 v[52:55], a[52:55], v[136:139], v[52:55]\n\t"
    "buffer_load_dwordx4 v105, s[52:55], s32 offen offset:1024 lds\n\t"
    "v_cvt_pk_bf16_f32 v153, v118, v119\n\t"
    "v_mfma_f32_16x16x32_bf16 v[84:87], a[52:55], v[140:143], v[84:87]\n\t"
    "v_exp_f32 v121, v121\n\t"
    "v_mfma_f32_16x16x32_bf16 v[88:91], a[56:59], v[140:143], v[88:91]\n\t"
    "v_exp_f32 v122, v122\n\t"
    "v_mfma_f32_16x16x32_bf16 v[56:59], a[56:59], v[136:139], v[56:59]\n\t"
    "buffer_load_dwordx4 v106, s[52:55], s32 offen offset:2048 lds\n\t"
    "v_cvt_pk_bf16_f32 v146, v120, v121\n\t"
    "v_mfma_f32_16x16x32_bf16 v[24:27], a[56:59], v[132:135], v[24:27]\n\t"
    "v_exp_f32 v123, v123\n\t"
    "v_mfma_f32_16x16x32_bf16 v[28:31], a[60:63], v[132:135], v[28:31]\n\t"
    "v_exp_f32 v124, v124\n\t"
    "v_mfma_f32_16x16x32_bf16 v[60:63], a[60:63], v[136:139], v[60:63]\n\t"
    "buffer_load_dwordx4 v107, s[52:55], s32 offen offset:3072 lds\n\t"
    "s_add_u32 s32, s34, 14\n\t"
    "s_and_b32 s32, s32, 31\n\t"
    "s_mul_i32 s32, s32, 0xa000\n\t"
    "v_cvt_pk_bf16_f32 v147, v122, v123\n\t"
    "v_exp_f32 v125, v125\n\t"
    "v_exp_f32 v126, v126\n\t"
    "v_cvt_pk_bf16_f32 v150, v124, v125\n\t"
    "v_mfma_f32_16x16x32_bf16 v[92:95], a[60:63], v[140:143], v[92:95]\n\t"
    "s_barrier\n\t"
    "ds_read_b64_tr_b16 a[32:33], v184 offset:57344\n\t"
    "ds_read_b64_tr_b16 a[34:35], v184 offset:61440\n\t"
    "ds_read_b64_tr_b16 a[36:37], v185 offset:57344\n\t"
    "ds_read_b64_tr_b16 a[38:39], v185 offset:61440\n\t"
    "ds_read_b64_tr_b16 a[40:41], v186 offset:57344\n\t"
    "ds_read_b64_tr_b16 a[42:43], v186 offset:61440\n\t"
    "ds_read_b64_tr_b16 a[44:45], v187 offset:57344\n\t"
    "ds_read_b64_tr_b16 a[46:47], v187 offset:61440\n\t"
    "ds_read_b64_tr_b16 a[48:49], v188 offset:57344\n\t"
    "ds_read_b64_tr_b16 a[50:51], v188 offset:61440\n\t"
    "ds_read_b64_tr_b16 a[52:53], v189 offset:57344\n\t"
    "ds_read_b64_tr_b16 a[54:55], v189 offset:61440\n\t"
    "ds_read_b64_tr_b16 a[56:57], v190 offset:57344\n\t"
    "ds_read_b64_tr_b16 a[58:59], v190 offset:61440\n\t"
    "ds_read_b64_tr_b16 a[60:61], v191 offset:57344\n\t"
    "ds_read_b64_tr_b16 a[62:63], v191 offset:61440\n\t"
    "v_exp_f32 v127, v127\n\t"
    "v_exp_f32 v128, v128\n\t"
    "v_cvt_pk_bf16_f32 v151, v126, v127\n\t"
    "v_exp_f32 v129, v129\n\t"
    "v_exp_f32 v130, v130\n\t"
    "v_cvt_pk_bf16_f32 v154, v128, v129\n\t"
    "v_exp_f32 v131, v131\n\t"
    "s_nop 0\n\t"
    "v_cvt_pk_bf16_f32 v155, v130, v131\n\t"
    "s_add_u32 m0, s72, 0x18000\n\t"
    "s_nop 0\n\t"
    "buffer_load_dwordx4 v104, s[52:55], s32 offen lds\n\t"
    "buffer_load_dwordx4 v105, s[52:55], s32 offen offset:1024 lds\n\t"
    "buffer_load_dwordx4 v106, s[52:55], s32 offen offset:2048 lds\n\t"
    "s_branch Lcs7%=\n\t"
    ".p2align 12\n\t"
    "Lc7%=:\n\t"
    "s_setpc_b64 s[22:23]\n\t"
    "Lcs7%=:\n\t"
    "buffer_load_dwordx4 v107, s[52:55], s32 offen offset:3072 lds\n\t"
    "s_add_u32 s32, s34, 18\n\t"
    "s_and_b32 s32, s32, 31\n\t"
    "s_mul_i32 s32, s32, 0xa000\n\t"
    "v_mfma_f32_16x16x32_bf16 v[96:99], a[240:243], v[144:147], v[96:99]\n\t"
    "s_waitcnt vmcnt(28)\n\t"
    "ds_read_b64_tr_b16 a[208:209], v100\n\t"
    "ds_read_b64_tr_b16 a[210:211], v100 offset:2048\n\t"
    "v_mfma_f32_16x16x32_bf16 v[96:99], a[244:247], v[148:151], v[96:99]\n\t"
    "ds_read_b64_tr_b16 a[212:213], v101\n\t"
    "ds_read_b64_tr_b16 a[214:215], v101 offset:2048\n\t"
    "v_mfma_f32_16x16x32_bf16 v[96:99], a[248:251], v[152:155], v[96:99]\n\t"
    "s_waitcnt lgkmcnt(15)\n\t"
    "ds_read_b64_tr_b16 a[216:217], v102\n\t"
    "ds_read_b64_tr_b16 a[218:219], v102 offset:2048\n\t"
    "v_mfma_f32_16x16x32_bf16 v[64:67], a[32:35], v[152:155], v[64:67]\n\t"
    "ds_read_b64_tr_b16 a[220:221], v103\n\t"
    "ds_read_b64_tr_b16 a[222:223], v103 offset:2048\n\t"
    "v_mfma_f32_16x16x32_bf16 v[32:35], a[32:35], v[148:151], v[32:35]\n\t"
    "v_mfma_f32_16x16x32_bf16 v[0:3], a[32:35], v[144:147], v[0:3]\n\t"
    "v_mfma_f32_16x16x32_bf16 v[4:7], a[36:39], v[144:147], v[4:7]\n\t"
    "ds_read_b64_tr_b16 a[224:225], v192 offset:49152\n\t"
    "ds_read_b64_tr_b16 a[226:227], v192 offset:51200\n\t"
    "v_mfma_f32_16x16x32_bf16 v[36:39], a[36:39], v[148:151], v[36:39]\n\t"
    "ds_read_b64_tr_b16 a[228:229], v193 offset:49152\n\t"
    "ds_read_b64_tr_b16 a[230:231], v193 offset:51200\n\t"
    "v_mfma_f32_16x16x32_bf16 v[68:71], a[36:39], v[152:155], v[68:71]\n\t"
    "s_waitcnt lgkmcnt(15)\n\t"
    "ds_read_b64_tr_b16 a[232:233], v194 offset:49152\n\t"
    "ds_read_b64_tr_b16 a[234:235], v194 offset:51200\n\t"
    "v_mfma_f32_16x16x32_bf16 v[72:75], a[40:43], v[152:155], v[72:75]\n\t"
    "s_add_u32 m0, s72, 0x20000\n\t"
    "ds_read_b64_tr_b16 a[236:237], v195 offset:49152\n\t"
    "ds_read_b64_tr_b16 a[238:239], v195 offset:51200\n\t"
    "v_mfma_f32_16x16x32_bf16 v[40:43], a[40:43], v[148:151], v[40:43]\n\t"
    "s_waitcnt lgkmcnt(8)\n\t"
    "v_mfma_f32_16x16x32_bf16 v[8:11], a[40:43], v[144:147], v[8:11]\n\t"
    "buffer_load_dwordx4 v104, s[52:55], s32 offen lds\n\t"
    "v_mfma_f32_16x16x32_bf16 v[12:15], a[44:47], v[144:147], v[12:15]\n\t"
    "buffer_load_dwordx4 v105, s[52:55], s32 offen offset:1024 lds\n\t"
    "v_mfma_f32_16x16x32_bf16 v[44:47], a[44:47], v[148:151], v[44:47]\n\t"
    "v_mfma_f32_16x16x32_bf16 v[76:79], a[44:47], v[152:155], v[76:79]\n\t"
    "buffer_load_dwordx4 v106, s[52:55], s32 offen offset:2048 lds\n\t"
    "v_mfma_f32_16x16x32_bf16 v[80:83], a[48:51], v[152:155], v[80:83]\n\t"
    "v_mfma_f32_16x16x32_bf16 v[48:51], a[48:51], v[148:151], v[48:51]\n\t"
    "s_waitcnt lgkmcnt(0)\n\t"
    "buffer_load_dwordx4 v107, s[52:55], s32 offen offset:3072 lds\n\t"
    "v_mfma_f32_16x16x32_bf16 v[16:19], a[48:51], v[144:147], v[16:19]\n\t"
    "v_mfma_f32_16x16x32_bf16 v[20:23], a[52:55], v[144:147], v[20:23]\n\t"
    "s_add_u32 s32, s34, 19\n\t"
    "s_and_b32 s32, s32, 31\n\t"
    "s_add_u32 m0, s72, 0xc000\n\t"
    "v_mfma_f32_16x16x32_bf16 v[52:55], a[52:55], v[148:151], v[52:55]\n\t"
    "s_mul_i32 s32, s32, 0xa000\n\t"
    "buffer_load_dwordx4 v104, s[52:55], s32 offen lds\n\t"
    "v_mfma_f32_16x16x32_bf16 v[84:87], a[52:55], v[152:155], v[84:87]\n\t"
    "buffer_load_dwordx4 v105, s[52:55], s32 offen offset:1024 lds\n\t"
    "v_mfma_f32_16x16x32_bf16 v[88:91], a[56:59], v[152:155], v[88:91]\n\t"
    "v_mfma_f32_16x16x32_bf16 v[56:59], a[56:59], v[148:151], v[56:59]\n\t"
    "buffer_load_dwordx4 v106, s[52:55], s32 offen offset:2048 lds\n\t"
    "v_mfma_f32_16x16x32_bf16 v[24:27], a[56:59], v[144:147], v[24:27]\n\t"
    "v_mfma_f32_16x16x32_bf16 v[28:31], a[60:63], v[144:147], v[28:31]\n\t"
    "buffer_load_dwordx4 v107, s[52:55], s32 offen offset:3072 lds\n\t"
    "v_mfma_f32_16x16x32_bf16 v[60:63], a[60:63], v[148:151], v[60:63]\n\t"
    "s_add_u32 s32, s34, 20\n\t"
    "s_and_b32 s32, s32, 31\n\t"
    "s_mul_i32 s32, s32, 0xa000\n\t"
    "v_mfma_f32_16x16x32_bf16 v[92:95], a[60:63], v[152:155], v[92:95]\n\t"
    "Lepi%=:\n\t"
    "v_lshlrev_b32 v116, 4, v244\n\t"
    "s_mov_b32 s75, 0x20000\n\t"
    "s_mov_b32 s76, 0x40000\n\t"
    "v_mov_b64 v[108:109], 0\n\t"
    "v_mov_b64 v[110:111], 0\n\t"
    "v_max3_f32 v112, v96, v97, v98\n\t"
    "v_rcp_f32 v96, v96\n\t"
    "v_rcp_f32 v97, v97\n\t"
    "v_rcp_f32 v98, v98\n\t"
    "v_mul_f32 v112, 0x5f800000, v112\n\t"
    "v_mul_f32 v112, 0, v112\n\t"
    "v_pk_mul_f32 v[0:1], v[0:1], v[96:97] op_sel:[0,0] op_sel_hi:[1,0]\n\t"
    "v_pk_mul_f32 v[2:3], v[2:3], v[96:97] op_sel:[0,0] op_sel_hi:[1,0]\n\t"
    "v_pk_mul_f32 v[4:5], v[4:5], v[96:97] op_sel:[0,0] op_sel_hi:[1,0]\n\t"
    "v_pk_mul_f32 v[6:7], v[6:7], v[96:97] op_sel:[0,0] op_sel_hi:[1,0]\n\t"
    "v_cvt_pk_bf16_f32 v132, v0, v1\n\t"
    "v_cvt_pk_bf16_f32 v133, v2, v3\n\t"
    "v_cvt_pk_bf16_f32 v134, v4, v5\n\t"
    "v_cvt_pk_bf16_f32 v135, v6, v7\n\t"
    "buffer_store_dwordx4 v[132:135], v116, s[60:63], 0 offen sc1\n\t"
    "s_nop 0\n\t"
    "v_mfma_f32_16x16x32_bf16 v[112:115], v[108:111], v[132:135], v[112:115]\n\t"
    "v_pk_mul_f32 v[8:9], v[8:9], v[96:97] op_sel:[0,0] op_sel_hi:[1,0]\n\t"
    "v_pk_mul_f32 v[10:11], v[10:11], v[96:97] op_sel:[0,0] op_sel_hi:[1,0]\n\t"
    "v_pk_mul_f32 v[12:13], v[12:13], v[96:97] op_sel:[0,0] op_sel_hi:[1,0]\n\t"
    "v_pk_mul_f32 v[14:15], v[14:15], v[96:97] op_sel:[0,0] op_sel_hi:[1,0]\n\t"
    "v_cvt_pk_bf16_f32 v144, v8, v9\n\t"
    "v_cvt_pk_bf16_f32 v145, v10, v11\n\t"
    "v_cvt_pk_bf16_f32 v146, v12, v13\n\t"
    "v_cvt_pk_bf16_f32 v147, v14, v15\n\t"
    "buffer_store_dwordx4 v[144:147], v116, s[60:63], 0 offen offset:1024 sc1\n\t"
    "s_nop 0\n\t"
    "v_mfma_f32_16x16x32_bf16 v[112:115], v[108:111], v[144:147], v[112:115]\n\t"
    "v_pk_mul_f32 v[16:17], v[16:17], v[96:97] op_sel:[0,0] op_sel_hi:[1,0]\n\t"
    "v_pk_mul_f32 v[18:19], v[18:19], v[96:97] op_sel:[0,0] op_sel_hi:[1,0]\n\t"
    "v_pk_mul_f32 v[20:21], v[20:21], v[96:97] op_sel:[0,0] op_sel_hi:[1,0]\n\t"
    "v_pk_mul_f32 v[22:23], v[22:23], v[96:97] op_sel:[0,0] op_sel_hi:[1,0]\n\t"
    "v_cvt_pk_bf16_f32 v136, v16, v17\n\t"
    "v_cvt_pk_bf16_f32 v137, v18, v19\n\t"
    "v_cvt_pk_bf16_f32 v138, v20, v21\n\t"
    "v_cvt_pk_bf16_f32 v139, v22, v23\n\t"
    "buffer_store_dwordx4 v[136:139], v116, s[60:63], 0 offen offset:2048 sc1\n\t"
    "s_nop 0\n\t"
    "v_mfma_f32_16x16x32_bf16 v[112:115], v[108:111], v[136:139], v[112:115]\n\t"
    "v_pk_mul_f32 v[24:25], v[24:25], v[96:97] op_sel:[0,0] op_sel_hi:[1,0]\n\t"
    "v_pk_mul_f32 v[26:27], v[26:27], v[96:97] op_sel:[0,0] op_sel_hi:[1,0]\n\t"
    "v_pk_mul_f32 v[28:29], v[28:29], v[96:97] op_sel:[0,0] op_sel_hi:[1,0]\n\t"
    "v_pk_mul_f32 v[30:31], v[30:31], v[96:97] op_sel:[0,0] op_sel_hi:[1,0]\n\t"
    "v_cvt_pk_bf16_f32 v148, v24, v25\n\t"
    "v_cvt_pk_bf16_f32 v149, v26, v27\n\t"
    "v_cvt_pk_bf16_f32 v150, v28, v29\n\t"
    "v_cvt_pk_bf16_f32 v151, v30, v31\n\t"
    "buffer_store_dwordx4 v[148:151], v116, s[60:63], 0 offen offset:3072 sc1\n\t"
    "s_nop 0\n\t"
    "v_mfma_f32_16x16x32_bf16 v[112:115], v[108:111], v[148:151], v[112:115]\n\t"
    "v_pk_mul_f32 v[32:33], v[32:33], v[96:97] op_sel:[0,1] op_sel_hi:[1,1]\n\t"
    "v_pk_mul_f32 v[34:35], v[34:35], v[96:97] op_sel:[0,1] op_sel_hi:[1,1]\n\t"
    "v_pk_mul_f32 v[36:37], v[36:37], v[96:97] op_sel:[0,1] op_sel_hi:[1,1]\n\t"
    "v_pk_mul_f32 v[38:39], v[38:39], v[96:97] op_sel:[0,1] op_sel_hi:[1,1]\n\t"
    "v_cvt_pk_bf16_f32 v140, v32, v33\n\t"
    "v_cvt_pk_bf16_f32 v141, v34, v35\n\t"
    "v_cvt_pk_bf16_f32 v142, v36, v37\n\t"
    "v_cvt_pk_bf16_f32 v143, v38, v39\n\t"
    "buffer_store_dwordx4 v[140:143], v116, s[60:63], s75 offen sc1\n\t"
    "s_nop 0\n\t"
    "v_mfma_f32_16x16x32_bf16 v[112:115], v[108:111], v[140:143], v[112:115]\n\t"
    "v_pk_mul_f32 v[40:41], v[40:41], v[96:97] op_sel:[0,1] op_sel_hi:[1,1]\n\t"
    "v_pk_mul_f32 v[42:43], v[42:43], v[96:97] op_sel:[0,1] op_sel_hi:[1,1]\n\t"
    "v_pk_mul_f32 v[44:45], v[44:45], v[96:97] op_sel:[0,1] op_sel_hi:[1,1]\n\t"
    "v_pk_mul_f32 v[46:47], v[46:47], v[96:97] op_sel:[0,1] op_sel_hi:[1,1]\n\t"
    "v_cvt_pk_bf16_f32 v152, v40, v41\n\t"
    "v_cvt_pk_bf16_f32 v153, v42, v43\n\t"
    "v_cvt_pk_bf16_f32 v154, v44, v45\n\t"
    "v_cvt_pk_bf16_f32 v155, v46, v47\n\t"
    "buffer_store_dwordx4 v[152:155], v116, s[60:63], s75 offen offset:1024 sc1\n\t"
    "s_nop 0\n\t"
    "v_mfma_f32_16x16x32_bf16 v[112:115], v[108:111], v[152:155], v[112:115]\n\t"
    "v_pk_mul_f32 v[48:49], v[48:49], v[96:97] op_sel:[0,1] op_sel_hi:[1,1]\n\t"
    "v_pk_mul_f32 v[50:51], v[50:51], v[96:97] op_sel:[0,1] op_sel_hi:[1,1]\n\t"
    "v_pk_mul_f32 v[52:53], v[52:53], v[96:97] op_sel:[0,1] op_sel_hi:[1,1]\n\t"
    "v_pk_mul_f32 v[54:55], v[54:55], v[96:97] op_sel:[0,1] op_sel_hi:[1,1]\n\t"
    "v_cvt_pk_bf16_f32 v132, v48, v49\n\t"
    "v_cvt_pk_bf16_f32 v133, v50, v51\n\t"
    "v_cvt_pk_bf16_f32 v134, v52, v53\n\t"
    "v_cvt_pk_bf16_f32 v135, v54, v55\n\t"
    "buffer_store_dwordx4 v[132:135], v116, s[60:63], s75 offen offset:2048 sc1\n\t"
    "s_nop 0\n\t"
    "v_mfma_f32_16x16x32_bf16 v[112:115], v[108:111], v[132:135], v[112:115]\n\t"
    "v_pk_mul_f32 v[56:57], v[56:57], v[96:97] op_sel:[0,1] op_sel_hi:[1,1]\n\t"
    "v_pk_mul_f32 v[58:59], v[58:59], v[96:97] op_sel:[0,1] op_sel_hi:[1,1]\n\t"
    "v_pk_mul_f32 v[60:61], v[60:61], v[96:97] op_sel:[0,1] op_sel_hi:[1,1]\n\t"
    "v_pk_mul_f32 v[62:63], v[62:63], v[96:97] op_sel:[0,1] op_sel_hi:[1,1]\n\t"
    "v_cvt_pk_bf16_f32 v144, v56, v57\n\t"
    "v_cvt_pk_bf16_f32 v145, v58, v59\n\t"
    "v_cvt_pk_bf16_f32 v146, v60, v61\n\t"
    "v_cvt_pk_bf16_f32 v147, v62, v63\n\t"
    "buffer_store_dwordx4 v[144:147], v116, s[60:63], s75 offen offset:3072 sc1\n\t"
    "s_nop 0\n\t"
    "v_mfma_f32_16x16x32_bf16 v[112:115], v[108:111], v[144:147], v[112:115]\n\t"
    "v_pk_mul_f32 v[64:65], v[64:65], v[98:99] op_sel:[0,0] op_sel_hi:[1,0]\n\t"
    "v_pk_mul_f32 v[66:67], v[66:67], v[98:99] op_sel:[0,0] op_sel_hi:[1,0]\n\t"
    "v_pk_mul_f32 v[68:69], v[68:69], v[98:99] op_sel:[0,0] op_sel_hi:[1,0]\n\t"
    "v_pk_mul_f32 v[70:71], v[70:71], v[98:99] op_sel:[0,0] op_sel_hi:[1,0]\n\t"
    "v_cvt_pk_bf16_f32 v136, v64, v65\n\t"
    "v_cvt_pk_bf16_f32 v137, v66, v67\n\t"
    "v_cvt_pk_bf16_f32 v138, v68, v69\n\t"
    "v_cvt_pk_bf16_f32 v139, v70, v71\n\t"
    "buffer_store_dwordx4 v[136:139], v116, s[60:63], s76 offen sc1\n\t"
    "s_nop 0\n\t"
    "v_mfma_f32_16x16x32_bf16 v[112:115], v[108:111], v[136:139], v[112:115]\n\t"
    "v_pk_mul_f32 v[72:73], v[72:73], v[98:99] op_sel:[0,0] op_sel_hi:[1,0]\n\t"
    "v_pk_mul_f32 v[74:75], v[74:75], v[98:99] op_sel:[0,0] op_sel_hi:[1,0]\n\t"
    "v_pk_mul_f32 v[76:77], v[76:77], v[98:99] op_sel:[0,0] op_sel_hi:[1,0]\n\t"
    "v_pk_mul_f32 v[78:79], v[78:79], v[98:99] op_sel:[0,0] op_sel_hi:[1,0]\n\t"
    "v_cvt_pk_bf16_f32 v148, v72, v73\n\t"
    "v_cvt_pk_bf16_f32 v149, v74, v75\n\t"
    "v_cvt_pk_bf16_f32 v150, v76, v77\n\t"
    "v_cvt_pk_bf16_f32 v151, v78, v79\n\t"
    "buffer_store_dwordx4 v[148:151], v116, s[60:63], s76 offen offset:1024 sc1\n\t"
    "s_nop 0\n\t"
    "v_mfma_f32_16x16x32_bf16 v[112:115], v[108:111], v[148:151], v[112:115]\n\t"
    "v_pk_mul_f32 v[80:81], v[80:81], v[98:99] op_sel:[0,0] op_sel_hi:[1,0]\n\t"
    "v_pk_mul_f32 v[82:83], v[82:83], v[98:99] op_sel:[0,0] op_sel_hi:[1,0]\n\t"
    "v_pk_mul_f32 v[84:85], v[84:85], v[98:99] op_sel:[0,0] op_sel_hi:[1,0]\n\t"
    "v_pk_mul_f32 v[86:87], v[86:87], v[98:99] op_sel:[0,0] op_sel_hi:[1,0]\n\t"
    "v_cvt_pk_bf16_f32 v140, v80, v81\n\t"
    "v_cvt_pk_bf16_f32 v141, v82, v83\n\t"
    "v_cvt_pk_bf16_f32 v142, v84, v85\n\t"
    "v_cvt_pk_bf16_f32 v143, v86, v87\n\t"
    "buffer_store_dwordx4 v[140:143], v116, s[60:63], s76 offen offset:2048 sc1\n\t"
    "s_nop 0\n\t"
    "v_mfma_f32_16x16x32_bf16 v[112:115], v[108:111], v[140:143], v[112:115]\n\t"
    "v_pk_mul_f32 v[88:89], v[88:89], v[98:99] op_sel:[0,0] op_sel_hi:[1,0]\n\t"
    "v_pk_mul_f32 v[90:91], v[90:91], v[98:99] op_sel:[0,0] op_sel_hi:[1,0]\n\t"
    "v_pk_mul_f32 v[92:93], v[92:93], v[98:99] op_sel:[0,0] op_sel_hi:[1,0]\n\t"
    "v_pk_mul_f32 v[94:95], v[94:95], v[98:99] op_sel:[0,0] op_sel_hi:[1,0]\n\t"
    "v_cvt_pk_bf16_f32 v152, v88, v89\n\t"
    "v_cvt_pk_bf16_f32 v153, v90, v91\n\t"
    "v_cvt_pk_bf16_f32 v154, v92, v93\n\t"
    "v_cvt_pk_bf16_f32 v155, v94, v95\n\t"
    "buffer_store_dwordx4 v[152:155], v116, s[60:63], s76 offen offset:3072 sc1\n\t"
    "s_nop 0\n\t"
    "v_mfma_f32_16x16x32_bf16 v[112:115], v[108:111], v[152:155], v[112:115]\n\t"
    "s_nop 7\n\t"
    "v_cmp_u_f32 vcc, v112, v112\n\t"
    "s_nop 3\n\t"
    "s_cmp_lg_u64 vcc, 0\n\t"
    "s_cselect_b32 s74, s35, 0\n\t"
    "s_cmp_lg_u32 s74, 0\n\t"
    "s_cbranch_scc1 Lfb%=\n\t"
    "v_subrev_u32 v236, 0x20000, v100\n\t"
    "v_subrev_u32 v237, 0x20000, v101\n\t"
    "v_subrev_u32 v238, 0x20000, v102\n\t"
    "v_subrev_u32 v239, 0x20000, v103\n\t"
    "v_subrev_u32 v240, 0x10000, v100\n\t"
    "v_subrev_u32 v241, 0x10000, v101\n\t"
    "v_subrev_u32 v242, 0x10000, v102\n\t"
    "v_subrev_u32 v243, 0x10000, v103\n\t"
    "s_waitcnt vmcnt(44)\n\t"
    "ds_read_b64_tr_b16 a[0:1], v240\n\t"
    "ds_read_b64_tr_b16 a[2:3], v240 offset:2048\n\t"
    "ds_read_b64_tr_b16 a[4:5], v241\n\t"
    "ds_read_b64_tr_b16 a[6:7], v241 offset:2048\n\t"
    "ds_read_b64_tr_b16 a[8:9], v242\n\t"
    "ds_read_b64_tr_b16 a[10:11], v242 offset:2048\n\t"
    "ds_read_b64_tr_b16 a[12:13], v243\n\t"
    "ds_read_b64_tr_b16 a[14:15], v243 offset:2048\n\t"
    "s_waitcnt vmcnt(40)\n\t"
    "ds_read_b64_tr_b16 a[16:17], v236 offset:16384\n\t"
    "ds_read_b64_tr_b16 a[18:19], v236 offset:18432\n\t"
    "ds_read_b64_tr_b16 a[20:21], v237 offset:16384\n\t"
    "ds_read_b64_tr_b16 a[22:23], v237 offset:18432\n\t"
    "ds_read_b64_tr_b16 a[24:25], v238 offset:16384\n\t"
    "ds_read_b64_tr_b16 a[26:27], v238 offset:18432\n\t"
    "ds_read_b64_tr_b16 a[28:29], v239 offset:16384\n\t"
    "ds_read_b64_tr_b16 a[30:31], v239 offset:18432\n\t"
    "s_waitcnt lgkmcnt(8)\n\t"
    "s_add_u32 m0, s72, 0x10000\n\t"
    "s_nop 0\n\t"
    "buffer_load_dwordx4 v104, s[52:55], s32 offen lds\n\t"
    "buffer_load_dwordx4 v105, s[52:55], s32 offen offset:1024 lds\n\t"
    "buffer_load_dwordx4 v106, s[52:55], s32 offen offset:2048 lds\n\t"
    "buffer_load_dwordx4 v107, s[52:55], s32 offen offset:3072 lds\n\t"
    "s_add_u32 s32, s34, 21\n\t"
    "s_and_b32 s32, s32, 31\n\t"
    "s_mul_i32 s32, s32, 0xa000\n\t"
    "s_waitcnt vmcnt(40)\n\t"
    "ds_read_b64_tr_b16 a[32:33], v236\n\t"
    "ds_read_b64_tr_b16 a[34:35], v236 offset:2048\n\t"
    "ds_read_b64_tr_b16 a[36:37], v237\n\t"
    "ds_read_b64_tr_b16 a[38:39], v237 offset:2048\n\t"
    "ds_read_b64_tr_b16 a[40:41], v238\n\t"
    "ds_read_b64_tr_b16 a[42:43], v238 offset:2048\n\t"
    "ds_read_b64_tr_b16 a[44:45], v239\n\t"
    "ds_read_b64_tr_b16 a[46:47], v239 offset:2048\n\t"
    "s_waitcnt lgkmcnt(8)\n\t"
    "s_add_u32 m0, s72, 0x4000\n\t"
    "s_nop 0\n\t"
    "buffer_load_dwordx4 v104, s[52:55], s32 offen lds\n\t"
    "buffer_load_dwordx4 v105, s[52:55], s32 offen offset:1024 lds\n\t"
    "buffer_load_dwordx4 v106, s[52:55], s32 offen offset:2048 lds\n\t"
    "buffer_load_dwordx4 v107, s[52:55], s32 offen offset:3072 lds\n\t"
    "s_add_u32 s32, s34, 22\n\t"
    "s_and_b32 s32, s32, 31\n\t"
    "s_mul_i32 s32, s32, 0xa000\n\t"
    "s_waitcnt vmcnt(40)\n\t"
    "ds_read_b64_tr_b16 a[48:49], v240 offset:16384\n\t"
    "ds_read_b64_tr_b16 a[50:51], v240 offset:18432\n\t"
    "ds_read_b64_tr_b16 a[52:53], v241 offset:16384\n\t"
    "ds_read_b64_tr_b16 a[54:55], v241 offset:18432\n\t"
    "ds_read_b64_tr_b16 a[56:57], v242 offset:16384\n\t"
    "ds_read_b64_tr_b16 a[58:59], v242 offset:18432\n\t"
    "ds_read_b64_tr_b16 a[60:61], v243 offset:16384\n\t"
    "ds_read_b64_tr_b16 a[62:63], v243 offset:18432\n\t"
    "s_waitcnt lgkmcnt(8)\n\t"
    "s_add_u32 m0, s72, 0\n\t"
    "s_nop 0\n\t"
    "buffer_load_dwordx4 v104, s[52:55], s32 offen lds\n\t"
    "buffer_load_dwordx4 v105, s[52:55], s32 offen offset:1024 lds\n\t"
    "buffer_load_dwordx4 v106, s[52:55], s32 offen offset:2048 lds\n\t"
    "buffer_load_dwordx4 v107, s[52:55], s32 offen offset:3072 lds\n\t"
    "s_add_u32 s32, s34, 23\n\t"
    "s_and_b32 s32, s32, 31\n\t"
    "s_mul_i32 s32, s32, 0xa000\n\t"
    "s_waitcnt vmcnt(40)\n\t"
    "ds_read_b64_tr_b16 a[240:241], v236 offset:32768\n\t"
    "ds_read_b64_tr_b16 a[242:243], v236 offset:34816\n\t"
    "ds_read_b64_tr_b16 a[244:245], v237 offset:32768\n\t"
    "ds_read_b64_tr_b16 a[246:247], v237 offset:34816\n\t"
    "ds_read_b64_tr_b16 a[248:249], v238 offset:32768\n\t"
    "ds_read_b64_tr_b16 a[250:251], v238 offset:34816\n\t"
    "ds_read_b64_tr_b16 a[252:253], v239 offset:32768\n\t"
    "ds_read_b64_tr_b16 a[254:255], v239 offset:34816\n\t"
    "s_waitcnt vmcnt(24)\n\t"
    "ds_read_b64_tr_b16 v[64:65], v100 offset:16384\n\t"
    "ds_read_b64_tr_b16 v[66:67], v100 offset:18432\n\t"
    "ds_read_b64_tr_b16 v[68:69], v101 offset:16384\n\t"
    "ds_read_b64_tr_b16 v[70:71], v101 offset:18432\n\t"
    "ds_read_b64_tr_b16 v[72:73], v102 offset:16384\n\t"
    "ds_read_b64_tr_b16 v[74:75], v102 offset:18432\n\t"
    "ds_read_b64_tr_b16 v[76:77], v103 offset:16384\n\t"
    "ds_read_b64_tr_b16 v[78:79], v103 offset:18432\n\t"
    "s_waitcnt vmcnt(32)\n\t"
    "ds_read_b64_tr_b16 v[80:81], v240 offset:32768\n\t"
    "ds_read_b64_tr_b16 v[82:83], v240 offset:34816\n\t"
    "ds_read_b64_tr_b16 v[84:85], v241 offset:32768\n\t"
    "ds_read_b64_tr_b16 v[86:87], v241 offset:34816\n\t"
    "ds_read_b64_tr_b16 v[88:89], v242 offset:32768\n\t"
    "ds_read_b64_tr_b16 v[90:91], v242 offset:34816\n\t"
    "ds_read_b64_tr_b16 v[92:93], v243 offset:32768\n\t"
    "ds_read_b64_tr_b16 v[94:95], v243 offset:34816\n\t"
    "s_waitcnt vmcnt(28)\n\t"
    "ds_read_b64_tr_b16 v[108:109], v100\n\t"
    "ds_read_b64_tr_b16 v[110:111], v100 offset:2048\n\t"
    "ds_read_b64_tr_b16 v[112:113], v101\n\t"
    "ds_read_b64_tr_b16 v[114:115], v101 offset:2048\n\t"
    "ds_read_b64_tr_b16 v[116:117], v102\n\t"
    "ds_read_b64_tr_b16 v[118:119], v102 offset:2048\n\t"
    "ds_read_b64_tr_b16 v[120:121], v103\n\t"
    "ds_read_b64_tr_b16 v[122:123], v103 offset:2048\n\t"
    "s_waitcnt vmcnt(24)\n\t"
    "ds_read_b64_tr_b16 v[124:125], v236 offset:49152\n\t"
    "ds_read_b64_tr_b16 v[126:127], v236 offset:51200\n\t"
    "ds_read_b64_tr_b16 v[128:129], v237 offset:49152\n\t"
    "ds_read_b64_tr_b16 v[130:131], v237 offset:51200\n\t"
    "ds_read_b64_tr_b16 v[132:133], v238 offset:49152\n\t"
    "ds_read_b64_tr_b16 v[134:135], v238 offset:51200\n\t"
    "ds_read_b64_tr_b16 v[136:137], v239 offset:49152\n\t"
    "ds_read_b64_tr_b16 v[138:139], v239 offset:51200\n\t"
    "v_add_co_u32 v248, vcc, 0x7f4a7c15, v248\n\t"
    "v_mov_b32 v96, 0x9e3779b9\n\t"
    "s_nop 0\n\t"
    "v_addc_co_u32 v249, vcc, v249, v96, vcc\n\t"
    "s_nop 1\n\t"
    "v_add_co_u32 v250, vcc, 0x7f4a7c15, v250\n\t"
    "s_nop 1\n\t"
    "v_addc_co_u32 v251, vcc, v251, v96, vcc\n\t"
    "s_lshl_b32 s74, s66, 5\n\t"
    "s_add_u32 s74, s74, s65\n\t"
    "s_lshl_b32 s74, s74, 8\n\t"
    "s_add_u32 s74, s74, 0x1e000\n\t"
    "v_lshlrev_b32 v247, 3, v244\n\t"
    "v_cmp_gt_u32 vcc, 30, v244\n\t"
    "s_nop 1\n\t"
    "v_cndmask_b32 v247, 2.0, v247, vcc\n\t"
    "s_cmp_lg_u32 s64, 0\n\t"
    "s_waitcnt vmcnt(12)\n\t"
    "s_barrier\n\t"
    "s_cbranch_scc1 Lnf%=\n\t"
    "buffer_store_dwordx2 v[248:249], v247, s[24:27], s74 offen sc1\n\t"
    "Lnf%=:\n\t"
    "s_cmp_lt_i32 s33, 0\n\t"
    "s_cbranch_scc1 Lend%=\n\t"
    "s_waitcnt lgkmcnt(15)\n\t"
    "s_add_u32 m0, s72, 0x14000\n\t"
    "s_nop 0\n\t"
    "buffer_load_dwordx4 v104, s[52:55], s32 offen lds\n\t"
    "buffer_load_dwordx4 v105, s[52:55], s32 offen offset:1024 lds\n\t"
    "buffer_load_dwordx4 v106, s[52:55], s32 offen offset:2048 lds\n\t"
    "buffer_load_dwordx4 v107, s[52:55], s32 offen offset:3072 lds\n\t"
    "s_add_u32 s32, s34, 24\n\t"
    "s_and_b32 s32, s32, 31\n\t"
    "s_mul_i32 s32, s32, 0xa000\n\t"
    "v_and_b32 v247, 7, v244\n\t"
    "v_lshlrev_b32 v247, 8, v247\n\t"
    "s_mul_i32 s75, s33, 0x89\n\t"
    "s_lshr_b32 s75, s75, 12\n\t"
    "s_mul_i32 s74, s75, 0x1f10\n\t"
    "s_lshl3_add_u32 s74, s33, s74\n\t"
    "s_add_u32 s74, s74, s68\n\t"
    "s_add_u32 s74, s74, 0x1e000\n\t"
    "s_lshl_b32 s77, s34, 10\n\t"
    "s_lshl_b32 s76, s64, 15\n\t"
    "s_add_u32 s75, s64, 1\n\t"
    "s_and_b32 s75, s75, 3\n\t"
    "s_lshl_b32 s75, s75, 17\n\t"
    "s_add_u32 s75, s75, s76\n\t"
    "v_lshl_add_u32 v96, v244, 4, s75\n\t"
    "s_add_u32 s75, s64, 2\n\t"
    "s_and_b32 s75, s75, 3\n\t"
    "s_lshl_b32 s75, s75, 17\n\t"
    "s_add_u32 s75, s75, s76\n\t"
    "v_lshl_add_u32 v97, v244, 4, s75\n\t"
    "s_add_u32 s75, s64, 3\n\t"
    "s_and_b32 s75, s75, 3\n\t"
    "s_lshl_b32 s75, s75, 17\n\t"
    "s_add_u32 s75, s75, s76\n\t"
    "v_lshl_add_u32 v98, v244, 4, s75\n\t"
    "s_add_u32 s75, s64, 4\n\t"
    "s_and_b32 s75, s75, 3\n\t"
    "s_lshl_b32 s75, s75, 17\n\t"
    "s_add_u32 s75, s75, s76\n\t"
    "v_lshl_add_u32 v99, v244, 4, s75\n\t"
    "s_add_u32 m0, s72, 0x8000\n\t"
    "s_nop 0\n\t"
    "buffer_load_dwordx4 v104, s[52:55], s32 offen lds\n\t"
    "buffer_load_dwordx4 v105, s[52:55], s32 offen offset:1024 lds\n\t"
    "buffer_load_dwordx4 v106, s[52:55], s32 offen offset:2048 lds\n\t"
    "buffer_load_dwordx4 v107, s[52:55], s32 offen offset:3072 lds\n\t"
    "s_add_u32 s32, s34, 25\n\t"
    "s_and_b32 s32, s32, 31\n\t"
    "s_mul_i32 s32, s32, 0xa000\n\t"
    "s_add_u32 m0, s72, 0x24000\n\t"
    "s_nop 0\n\t"
    "buffer_load_dwordx4 v104, s[52:55], s32 offen lds\n\t"
    "buffer_load_dwordx4 v105, s[52:55], s32 offen offset:1024 lds\n\t"
    "buffer_load_dwordx4 v106, s[52:55], s32 offen offset:2048 lds\n\t"
    "buffer_load_dwordx4 v107, s[52:55], s32 offen offset:3072 lds\n\t"
    "s_add_u32 s32, s34, 26\n\t"
    "s_and_b32 s32, s32, 31\n\t"
    "s_mul_i32 s32, s32, 0xa000\n\t"
    "s_add_u32 m0, s72, 0x18000\n\t"
    "s_nop 0\n\t"
    "buffer_load_dwordx4 v104, s[52:55], s32 offen lds\n\t"
    "buffer_load_dwordx4 v105, s[52:55], s32 offen offset:1024 lds\n\t"
    "buffer_load_dwordx4 v106, s[52:55], s32 offen offset:2048 lds\n\t"
    "buffer_load_dwordx4 v107, s[52:55], s32 offen offset:3072 lds\n\t"
    "s_add_u32 s32, s34, 27\n\t"
    "s_and_b32 s32, s32, 31\n\t"
    "s_mul_i32 s32, s32, 0xa000\n\t"
    "s_waitcnt lgkmcnt(8)\n\t"
    "s_add_u32 m0, s72, 0x20000\n\t"
    "s_nop 0\n\t"
    "buffer_load_dwordx4 v104, s[52:55], s32 offen lds\n\t"
    "buffer_load_dwordx4 v105, s[52:55], s32 offen offset:1024 lds\n\t"
    "buffer_load_dwordx4 v106, s[52:55], s32 offen offset:2048 lds\n\t"
    "buffer_load_dwordx4 v107, s[52:55], s32 offen offset:3072 lds\n\t"
    "s_add_u32 s32, s34, 28\n\t"
    "s_and_b32 s32, s32, 31\n\t"
    "s_mul_i32 s32, s32, 0xa000\n\t"
    "s_waitcnt lgkmcnt(0)\n\t"
    "s_add_u32 m0, s72, 0xc000\n\t"
    "s_nop 0\n\t"
    "buffer_load_dwordx4 v104, s[52:55], s32 offen lds\n\t"
    "buffer_load_dwordx4 v105, s[52:55], s32 offen offset:1024 lds\n\t"
    "buffer_load_dwordx4 v106, s[52:55], s32 offen offset:2048 lds\n\t"
    "buffer_load_dwordx4 v107, s[52:55], s32 offen offset:3072 lds\n\t"
    "s_add_u32 s32, s34, 29\n\t"
    "s_and_b32 s32, s32, 31\n\t"
    "s_mul_i32 s32, s32, 0xa000\n\t"
    "buffer_load_dwordx2 v[248:249], v247, s[24:27], s74 offen sc1\n\t"
    "s_add_u32 m0, s72, 0x1c000\n\t"
    "s_nop 0\n\t"
    "buffer_load_dwordx4 v104, s[52:55], s32 offen lds\n\t"
    "buffer_load_dwordx4 v105, s[52:55], s32 offen offset:1024 lds\n\t"
    "buffer_load_dwordx4 v106, s[52:55], s32 offen offset:2048 lds\n\t"
    "buffer_load_dwordx4 v107, s[52:55], s32 offen offset:3072 lds\n\t"
    "s_add_u32 s32, s34, 30\n\t"
    "s_and_b32 s32, s32, 31\n\t"
    "s_mul_i32 s32, s32, 0xa000\n\t"
    "s_waitcnt vmcnt(37)\n\t"
    "ds_read_b64_tr_b16 v[140:141], v240\n\t"
    "ds_read_b64_tr_b16 v[142:143], v240 offset:2048\n\t"
    "ds_read_b64_tr_b16 v[144:145], v241\n\t"
    "ds_read_b64_tr_b16 v[146:147], v241 offset:2048\n\t"
    "ds_read_b64_tr_b16 v[148:149], v242\n\t"
    "ds_read_b64_tr_b16 v[150:151], v242 offset:2048\n\t"
    "ds_read_b64_tr_b16 v[152:153], v243\n\t"
    "ds_read_b64_tr_b16 v[154:155], v243 offset:2048\n\t"
    "s_waitcnt vmcnt(33)\n\t"
    "ds_read_b64_tr_b16 v[156:157], v236 offset:16384\n\t"
    "ds_read_b64_tr_b16 v[158:159], v236 offset:18432\n\t"
    "ds_read_b64_tr_b16 v[160:161], v237 offset:16384\n\t"
    "ds_read_b64_tr_b16 v[162:163], v237 offset:18432\n\t"
    "ds_read_b64_tr_b16 v[164:165], v238 offset:16384\n\t"
    "ds_read_b64_tr_b16 v[166:167], v238 offset:18432\n\t"
    "ds_read_b64_tr_b16 v[168:169], v239 offset:16384\n\t"
    "ds_read_b64_tr_b16 v[170:171], v239 offset:18432\n\t"
    "s_waitcnt lgkmcnt(8)\n\t"
    "s_add_u32 m0, s72, 0x10000\n\t"
    "s_nop 0\n\t"
    "buffer_load_dwordx4 v104, s[52:55], s32 offen lds\n\t"
    "buffer_load_dwordx4 v105, s[52:55], s32 offen offset:1024 lds\n\t"
    "buffer_load_dwordx4 v106, s[52:55], s32 offen offset:2048 lds\n\t"
    "buffer_load_dwordx4 v107, s[52:55], s32 offen offset:3072 lds\n\t"
    "s_add_u32 s32, s34, 31\n\t"
    "s_and_b32 s32, s32, 31\n\t"
    "s_mul_i32 s32, s32, 0xa000\n\t"
    "s_waitcnt lgkmcnt(0)\n\t"
    "s_add_u32 m0, s72, 0x4000\n\t"
    "s_nop 0\n\t"
    "buffer_load_dwordx4 v104, s[52:55], s32 offen lds\n\t"
    "buffer_load_dwordx4 v105, s[52:55], s32 offen offset:1024 lds\n\t"
    "buffer_load_dwordx4 v106, s[52:55], s32 offen offset:2048 lds\n\t"
    "buffer_load_dwordx4 v107, s[52:55], s32 offen offset:3072 lds\n\t"
    "s_mov_b32 s69, 0x200000\n\t"
    "s_waitcnt vmcnt(12)\n\t"
    "v_cmp_ne_u64 vcc, v[248:249], v[250:251]\n\t"
    "s_nop 3\n\t"
    "s_cbranch_vccz Lgo%=\n\t"
    "Lpoll%=:\n\t"
    "buffer_load_dwordx2 v[248:249], v247, s[24:27], s74 offen sc1\n\t"
    "s_waitcnt vmcnt(0)\n\t"
    "v_cmp_ne_u64 vcc, v[248:249], v[250:251]\n\t"
    "s_nop 3\n\t"
    "s_cbranch_vccz Lgo%=\n\t"
    "s_sleep 1\n\t"
    "s_sub_u32 s69, s69, 1\n\t"
    "s_cbranch_scc0 Lpoll%=\n\t"
    "Lgo%=:\n\t"
    "buffer_load_dwordx4 v[172:175], v96, s[28:31], s77 offen sc1\n\t"
    "buffer_load_dwordx4 v[176:179], v97, s[28:31], s77 offen sc1\n\t"
    "buffer_load_dwordx4 v[180:183], v98, s[28:31], s77 offen sc1\n\t"
    "buffer_load_dwordx4 v[184:187], v99, s[28:31], s77 offen sc1\n\t"
    "s_add_u32 s77, s77, 0x400\n\t"
    "s_and_b32 s77, s77, 0x7fff\n\t"
    "buffer_load_dwordx4 v[188:191], v96, s[28:31], s77 offen sc1\n\t"
    "buffer_load_dwordx4 v[192:195], v97, s[28:31], s77 offen sc1\n\t"
    "buffer_load_dwordx4 v[196:199], v98, s[28:31], s77 offen sc1\n\t"
    "buffer_load_dwordx4 v[200:203], v99, s[28:31], s77 offen sc1\n\t"
    "s_add_u32 s77, s77, 0x400\n\t"
    "s_and_b32 s77, s77, 0x7fff\n\t"
    "buffer_load_dwordx4 v[204:207], v96, s[28:31], s77 offen sc1\n\t"
    "buffer_load_dwordx4 v[208:211], v97, s[28:31], s77 offen sc1\n\t"
    "buffer_load_dwordx4 v[212:215], v98, s[28:31], s77 offen sc1\n\t"
    "buffer_load_dwordx4 v[216:219], v99, s[28:31], s77 offen sc1\n\t"
    "s_add_u32 s77, s77, 0xc00\n\t"
    "s_and_b32 s77, s77, 0x7fff\n\t"
    "buffer_load_dwordx4 v[220:223], v96, s[28:31], s77 offen sc1\n\t"
    "buffer_load_dwordx4 v[224:227], v97, s[28:31], s77 offen sc1\n\t"
    "buffer_load_dwordx4 v[228:231], v98, s[28:31], s77 offen sc1\n\t"
    "buffer_load_dwordx4 v[232:235], v99, s[28:31], s77 offen sc1\n\t"
    "s_add_u32 s77, s77, 0x400\n\t"
    "s_and_b32 s77, s77, 0x7fff\n\t"
    "v_lshl_add_u32 v248, v244, 4, s72\n\t"
    "s_sub_u32 s75, s64, 1\n\t"
    "s_and_b32 s75, s75, 3\n\t"
    "s_lshl_b32 s75, s75, 12\n\t"
    "v_lshl_add_u32 v249, v244, 4, s75\n\t"
    "s_sub_u32 s75, s64, 2\n\t"
    "s_and_b32 s75, s75, 3\n\t"
    "s_lshl_b32 s75, s75, 12\n\t"
    "v_lshl_add_u32 v253, v244, 4, s75\n\t"
    "s_sub_u32 s75, s64, 3\n\t"
    "s_and_b32 s75, s75, 3\n\t"
    "s_lshl_b32 s75, s75, 12\n\t"
    "v_lshl_add_u32 v254, v244, 4, s75\n\t"
    "v_mul_u32_u24 v245, 0x500, v245\n\t"
    "v_and_b32 v247, 1, v246\n\t"
    "v_lshl_add_u32 v245, v247, 5, v245\n\t"
    "v_lshrrev_b32 v247, 1, v246\n\t"
    "v_lshl_add_u32 v245, v247, 4, v245\n\t"
    "v_and_b32 v247, 7, v244\n\t"
    "v_lshl_add_u32 v252, v247, 3, v252\n\t"
    "s_lshl_b32 s75, s64, 28\n\t"
    "v_add_u32 v252, s75, v252\n\t"
    "s_waitcnt vmcnt(15)\n\t"
    "v_mfma_f32_16x16x32_bf16 v[0:3], a[64:67], v[172:175], 0\n\t"
    "v_mfma_f32_16x16x32_bf16 v[4:7], a[68:71], v[172:175], 0\n\t"
    "v_mfma_f32_16x16x32_bf16 v[8:11], a[72:75], v[172:175], 0\n\t"
    "v_mfma_f32_16x16x32_bf16 v[12:15], a[76:79], v[172:175], 0\n\t"
    "s_waitcnt vmcnt(14)\n\t"
    "buffer_load_dwordx4 v[172:175], v96, s[28:31], s77 offen sc1\n\t"
    "v_mfma_f32_16x16x32_bf16 v[28:31], a[76:79], v[176:179], 0\n\t"
    "v_mfma_f32_16x16x32_bf16 v[24:27], a[72:75], v[176:179], 0\n\t"
    "v_mfma_f32_16x16x32_bf16 v[20:23], a[68:71], v[176:179], 0\n\t"
    "v_mfma_f32_16x16x32_bf16 v[16:19], a[64:67], v[176:179], 0\n\t"
    "s_waitcnt vmcnt(14)\n\t"
    "buffer_load_dwordx4 v[176:179], v97, s[28:31], s77 offen sc1\n\t"
    "v_mfma_f32_16x16x32_bf16 v[32:35], a[64:67], v[180:183], 0\n\t"
    "v_mfma_f32_16x16x32_bf16 v[36:39], a[68:71], v[180:183], 0\n\t"
    "v_mfma_f32_16x16x32_bf16 v[40:43], a[72:75], v[180:183], 0\n\t"
    "v_mfma_f32_16x16x32_bf16 v[44:47], a[76:79], v[180:183], 0\n\t"
    "s_waitcnt vmcnt(14)\n\t"
    "buffer_load_dwordx4 v[180:183], v98, s[28:31], s77 offen sc1\n\t"
    "v_mfma_f32_16x16x32_bf16 v[60:63], a[76:79], v[184:187], 0\n\t"
    "v_mfma_f32_16x16x32_bf16 v[56:59], a[72:75], v[184:187], 0\n\t"
    "v_mfma_f32_16x16x32_bf16 v[52:55], a[68:71], v[184:187], 0\n\t"
    "v_mfma_f32_16x16x32_bf16 v[48:51], a[64:67], v[184:187], 0\n\t"
    "s_waitcnt vmcnt(14)\n\t"
    "buffer_load_dwordx4 v[184:187], v99, s[28:31], s77 offen sc1\n\t"
    "v_mfma_f32_16x16x32_bf16 v[0:3], a[80:83], v[188:191], v[0:3]\n\t"
    "s_add_u32 s77, s77, 0x7400\n\t"
    "s_and_b32 s77, s77, 0x7fff\n\t"
    "v_mfma_f32_16x16x32_bf16 v[4:7], a[84:87], v[188:191], v[4:7]\n\t"
    "ds_read_b64_tr_b16 a[64:65], v236\n\t"
    "ds_read_b64_tr_b16 a[66:67], v236 offset:2048\n\t"
    "v_mfma_f32_16x16x32_bf16 v[8:11], a[88:91], v[188:191], v[8:11]\n\t"
    "ds_read_b64_tr_b16 a[68:69], v237\n\t"
    "ds_read_b64_tr_b16 a[70:71], v237 offset:2048\n\t"
    "v_mfma_f32_16x16x32_bf16 v[12:15], a[92:95], v[188:191], v[12:15]\n\t"
    "s_waitcnt vmcnt(14)\n\t"
    "buffer_load_dwordx4 v[188:191], v96, s[28:31], s77 offen sc1\n\t"
    "v_mfma_f32_16x16x32_bf16 v[28:31], a[92:95], v[192:195], v[28:31]\n\t"
    "ds_read_b64_tr_b16 a[72:73], v238\n\t"
    "ds_read_b64_tr_b16 a[74:75], v238 offset:2048\n\t"
    "v_mfma_f32_16x16x32_bf16 v[24:27], a[88:91], v[192:195], v[24:27]\n\t"
    "s_branch Lcs8%=\n\t"
    ".p2align 12\n\t"
    "Lc8%=:\n\t"
    "s_setpc_b64 s[22:23]\n\t"
    "Lcs8%=:\n\t"
    "ds_read_b64_tr_b16 a[76:77], v239\n\t"
    "ds_read_b64_tr_b16 a[78:79], v239 offset:2048\n\t"
    "v_mfma_f32_16x16x32_bf16 v[20:23], a[84:87], v[192:195], v[20:23]\n\t"
    "v_mfma_f32_16x16x32_bf16 v[16:19], a[80:83], v[192:195], v[16:19]\n\t"
    "s_waitcnt vmcnt(14)\n\t"
    "buffer_load_dwordx4 v[192:195], v97, s[28:31], s77 offen sc1\n\t"
    "v_mfma_f32_16x16x32_bf16 v[32:35], a[80:83], v[196:199], v[32:35]\n\t"
    "v_mfma_f32_16x16x32_bf16 v[36:39], a[84:87], v[196:199], v[36:39]\n\t"
    "v_mfma_f32_16x16x32_bf16 v[40:43], a[88:91], v[196:199], v[40:43]\n\t"
    "v_mfma_f32_16x16x32_bf16 v[44:47], a[92:95], v[196:199], v[44:47]\n\t"
    "s_waitcnt vmcnt(14)\n\t"
    "buffer_load_dwordx4 v[196:199], v98, s[28:31], s77 offen sc1\n\t"
    "v_mfma_f32_16x16x32_bf16 v[60:63], a[92:95], v[200:203], v[60:63]\n\t"
    "v_mfma_f32_16x16x32_bf16 v[56:59], a[88:91], v[200:203], v[56:59]\n\t"
    "v_mfma_f32_16x16x32_bf16 v[52:55], a[84:87], v[200:203], v[52:55]\n\t"
    "v_mfma_f32_16x16x32_bf16 v[48:51], a[80:83], v[200:203], v[48:51]\n\t"
    "s_waitcnt vmcnt(14)\n\t"
    "buffer_load_dwordx4 v[200:203], v99, s[28:31], s77 offen sc1\n\t"
    "v_mfma_f32_16x16x32_bf16 v[0:3], a[96:99], v[204:207], v[0:3]\n\t"
    "s_add_u32 s77, s77, 0x1c00\n\t"
    "s_and_b32 s77, s77, 0x7fff\n\t"
    "v_mfma_f32_16x16x32_bf16 v[4:7], a[100:103], v[204:207], v[4:7]\n\t"
    "ds_read_b64_tr_b16 a[80:81], v240 offset:16384\n\t"
    "ds_read_b64_tr_b16 a[82:83], v240 offset:18432\n\t"
    "v_mfma_f32_16x16x32_bf16 v[8:11], a[104:107], v[204:207], v[8:11]\n\t"
    "ds_read_b64_tr_b16 a[84:85], v241 offset:16384\n\t"
    "ds_read_b64_tr_b16 a[86:87], v241 offset:18432\n\t"
    "v_mfma_f32_16x16x32_bf16 v[12:15], a[108:111], v[204:207], v[12:15]\n\t"
    "s_waitcnt vmcnt(14)\n\t"
    "buffer_load_dwordx4 v[204:207], v96, s[28:31], s77 offen sc1\n\t"
    "v_mfma_f32_16x16x32_bf16 v[28:31], a[108:111], v[208:211], v[28:31]\n\t"
    "ds_read_b64_tr_b16 a[88:89], v242 offset:16384\n\t"
    "ds_read_b64_tr_b16 a[90:91], v242 offset:18432\n\t"
    "v_mfma_f32_16x16x32_bf16 v[24:27], a[104:107], v[208:211], v[24:27]\n\t"
    "ds_read_b64_tr_b16 a[92:93], v243 offset:16384\n\t"
    "ds_read_b64_tr_b16 a[94:95], v243 offset:18432\n\t"
    "v_mfma_f32_16x16x32_bf16 v[20:23], a[100:103], v[208:211], v[20:23]\n\t"
    "v_mfma_f32_16x16x32_bf16 v[16:19], a[96:99], v[208:211], v[16:19]\n\t"
    "s_waitcnt vmcnt(14)\n\t"
    "buffer_load_dwordx4 v[208:211], v97, s[28:31], s77 offen sc1\n\t"
    "v_mfma_f32_16x16x32_bf16 v[32:35], a[96:99], v[212:215], v[32:35]\n\t"
    "v_mfma_f32_16x16x32_bf16 v[36:39], a[100:103], v[212:215], v[36:39]\n\t"
    "v_mfma_f32_16x16x32_bf16 v[40:43], a[104:107], v[212:215], v[40:43]\n\t"
    "v_mfma_f32_16x16x32_bf16 v[44:47], a[108:111], v[212:215], v[44:47]\n\t"
    "s_waitcnt vmcnt(14)\n\t"
    "buffer_load_dwordx4 v[212:215], v98, s[28:31], s77 offen sc1\n\t"
    "v_mfma_f32_16x16x32_bf16 v[60:63], a[108:111], v[216:219], v[60:63]\n\t"
    "v_mfma_f32_16x16x32_bf16 v[56:59], a[104:107], v[216:219], v[56:59]\n\t"
    "v_mfma_f32_16x16x32_bf16 v[52:55], a[100:103], v[216:219], v[52:55]\n\t"
    "v_mfma_f32_16x16x32_bf16 v[48:51], a[96:99], v[216:219], v[48:51]\n\t"
    "s_waitcnt vmcnt(14)\n\t"
    "buffer_load_dwordx4 v[216:219], v99, s[28:31], s77 offen sc1\n\t"
    "v_mfma_f32_16x16x32_bf16 v[0:3], a[144:147], v[220:223], v[0:3]\n\t"
    "s_add_u32 s77, s77, 0x6800\n\t"
    "s_and_b32 s77, s77, 0x7fff\n\t"
    "v_mfma_f32_16x16x32_bf16 v[4:7], a[148:151], v[220:223], v[4:7]\n\t"
    "ds_read_b64_tr_b16 a[96:97], v236 offset:32768\n\t"
    "ds_read_b64_tr_b16 a[98:99], v236 offset:34816\n\t"
    "v_mfma_f32_16x16x32_bf16 v[8:11], a[152:155], v[220:223], v[8:11]\n\t"
    "ds_read_b64_tr_b16 a[100:101], v237 offset:32768\n\t"
    "ds_read_b64_tr_b16 a[102:103], v237 offset:34816\n\t"
    "v_mfma_f32_16x16x32_bf16 v[12:15], a[156:159], v[220:223], v[12:15]\n\t"
    "s_waitcnt vmcnt(14)\n\t"
    "buffer_load_dwordx4 v[220:223], v96, s[28:31], s77 offen sc1\n\t"
    "v_mfma_f32_16x16x32_bf16 v[28:31], a[156:159], v[224:227], v[28:31]\n\t"
    "ds_read_b64_tr_b16 a[104:105], v238 offset:32768\n\t"
    "ds_read_b64_tr_b16 a[106:107], v238 offset:34816\n\t"
    "v_mfma_f32_16x16x32_bf16 v[24:27], a[152:155], v[224:227], v[24:27]\n\t"
    "ds_read_b64_tr_b16 a[108:109], v239 offset:32768\n\t"
    "ds_read_b64_tr_b16 a[110:111], v239 offset:34816\n\t"
    "v_mfma_f32_16x16x32_bf16 v[20:23], a[148:151], v[224:227], v[20:23]\n\t"
    "v_mfma_f32_16x16x32_bf16 v[16:19], a[144:147], v[224:227], v[16:19]\n\t"
    "s_waitcnt vmcnt(14)\n\t"
    "buffer_load_dwordx4 v[224:227], v97, s[28:31], s77 offen sc1\n\t"
    "v_mfma_f32_16x16x32_bf16 v[32:35], a[144:147], v[228:231], v[32:35]\n\t"
    "v_mfma_f32_16x16x32_bf16 v[36:39], a[148:151], v[228:231], v[36:39]\n\t"
    "v_mfma_f32_16x16x32_bf16 v[40:43], a[152:155], v[228:231], v[40:43]\n\t"
    "v_mfma_f32_16x16x32_bf16 v[44:47], a[156:159], v[228:231], v[44:47]\n\t"
    "s_waitcnt vmcnt(14)\n\t"
    "buffer_load_dwordx4 v[228:231], v98, s[28:31], s77 offen sc1\n\t"
    "v_mfma_f32_16x16x32_bf16 v[60:63], a[156:159], v[232:235], v[60:63]\n\t"
    "v_mfma_f32_16x16x32_bf16 v[56:59], a[152:155], v[232:235], v[56:59]\n\t"
    "v_mfma_f32_16x16x32_bf16 v[52:55], a[148:151], v[232:235], v[52:55]\n\t"
    "v_mfma_f32_16x16x32_bf16 v[48:51], a[144:147], v[232:235], v[48:51]\n\t"
    "s_waitcnt vmcnt(14)\n\t"
    "buffer_load_dwordx4 v[232:235], v99, s[28:31], s77 offen sc1\n\t"
    "v_mfma_f32_16x16x32_bf16 v[0:3], a[112:115], v[172:175], v[0:3]\n\t"
    "s_add_u32 s77, s77, 0x1c00\n\t"
    "s_and_b32 s77, s77, 0x7fff\n\t"
    "v_mfma_f32_16x16x32_bf16 v[4:7], a[116:119], v[172:175], v[4:7]\n\t"
    "ds_read_b64_tr_b16 a[144:145], v100 offset:16384\n\t"
    "ds_read_b64_tr_b16 a[146:147], v100 offset:18432\n\t"
    "v_mfma_f32_16x16x32_bf16 v[8:11], a[120:123], v[172:175], v[8:11]\n\t"
    "ds_read_b64_tr_b16 a[148:149], v101 offset:16384\n\t"
    "ds_read_b64_tr_b16 a[150:151], v101 offset:18432\n\t"
    "v_mfma_f32_16x16x32_bf16 v[12:15], a[124:127], v[172:175], v[12:15]\n\t"
    "s_waitcnt vmcnt(14)\n\t"
    "buffer_load_dwordx4 v[172:175], v96, s[28:31], s77 offen sc1\n\t"
    "v_mfma_f32_16x16x32_bf16 v[28:31], a[124:127], v[176:179], v[28:31]\n\t"
    "ds_read_b64_tr_b16 a[152:153], v102 offset:16384\n\t"
    "ds_read_b64_tr_b16 a[154:155], v102 offset:18432\n\t"
    "v_mfma_f32_16x16x32_bf16 v[24:27], a[120:123], v[176:179], v[24:27]\n\t"
    "ds_read_b64_tr_b16 a[156:157], v103 offset:16384\n\t"
    "ds_read_b64_tr_b16 a[158:159], v103 offset:18432\n\t"
    "v_mfma_f32_16x16x32_bf16 v[20:23], a[116:119], v[176:179], v[20:23]\n\t"
    "v_mfma_f32_16x16x32_bf16 v[16:19], a[112:115], v[176:179], v[16:19]\n\t"
    "s_waitcnt vmcnt(14)\n\t"
    "buffer_load_dwordx4 v[176:179], v97, s[28:31], s77 offen sc1\n\t"
    "v_mfma_f32_16x16x32_bf16 v[32:35], a[112:115], v[180:183], v[32:35]\n\t"
    "v_mfma_f32_16x16x32_bf16 v[36:39], a[116:119], v[180:183], v[36:39]\n\t"
    "v_mfma_f32_16x16x32_bf16 v[40:43], a[120:123], v[180:183], v[40:43]\n\t"
    "v_mfma_f32_16x16x32_bf16 v[44:47], a[124:127], v[180:183], v[44:47]\n\t"
    "s_waitcnt vmcnt(14)\n\t"
    "buffer_load_dwordx4 v[180:183], v98, s[28:31], s77 offen sc1\n\t"
    "v_mfma_f32_16x16x32_bf16 v[60:63], a[124:127], v[184:187], v[60:63]\n\t"
    "v_mfma_f32_16x16x32_bf16 v[56:59], a[120:123], v[184:187], v[56:59]\n\t"
    "v_mfma_f32_16x16x32_bf16 v[52:55], a[116:119], v[184:187], v[52:55]\n\t"
    "v_mfma_f32_16x16x32_bf16 v[48:51], a[112:115], v[184:187], v[48:51]\n\t"
    "s_waitcnt vmcnt(14)\n\t"
    "buffer_load_dwordx4 v[184:187], v99, s[28:31], s77 offen sc1\n\t"
    "v_mfma_f32_16x16x32_bf16 v[0:3], a[128:131], v[188:191], v[0:3]\n\t"
    "s_add_u32 s77, s77, 0x7000\n\t"
    "s_and_b32 s77, s77, 0x7fff\n\t"
    "v_mfma_f32_16x16x32_bf16 v[4:7], a[132:135], v[188:191], v[4:7]\n\t"
    "ds_read_b64_tr_b16 a[112:113], v240 offset:32768\n\t"
    "ds_read_b64_tr_b16 a[114:115], v240 offset:34816\n\t"
    "v_mfma_f32_16x16x32_bf16 v[8:11], a[136:139], v[188:191], v[8:11]\n\t"
    "ds_read_b64_tr_b16 a[116:117], v241 offset:32768\n\t"
    "ds_read_b64_tr_b16 a[118:119], v241 offset:34816\n\t"
    "v_mfma_f32_16x16x32_bf16 v[12:15], a[140:143], v[188:191], v[12:15]\n\t"
    "s_waitcnt vmcnt(14)\n\t"
    "buffer_load_dwordx4 v[188:191], v96, s[28:31], s77 offen sc1\n\t"
    "v_mfma_f32_16x16x32_bf16 v[28:31], a[140:143], v[192:195], v[28:31]\n\t"
    "ds_read_b64_tr_b16 a[120:121], v242 offset:32768\n\t"
    "ds_read_b64_tr_b16 a[122:123], v242 offset:34816\n\t"
    "v_mfma_f32_16x16x32_bf16 v[24:27], a[136:139], v[192:195], v[24:27]\n\t"
    "ds_read_b64_tr_b16 a[124:125], v243 offset:32768\n\t"
    "ds_read_b64_tr_b16 a[126:127], v243 offset:34816\n\t"
    "v_mfma_f32_16x16x32_bf16 v[20:23], a[132:135], v[192:195], v[20:23]\n\t"
    "v_mfma_f32_16x16x32_bf16 v[16:19], a[128:131], v[192:195], v[16:19]\n\t"
    "s_waitcnt vmcnt(14)\n\t"
    "buffer_load_dwordx4 v[192:195], v97, s[28:31], s77 offen sc1\n\t"
    "v_mfma_f32_16x16x32_bf16 v[32:35], a[128:131], v[196:199], v[32:35]\n\t"
    "v_mfma_f32_16x16x32_bf16 v[36:39], a[132:135], v[196:199], v[36:39]\n\t"
    "v_mfma_f32_16x16x32_bf16 v[40:43], a[136:139], v[196:199], v[40:43]\n\t"
    "v_mfma_f32_16x16x32_bf16 v[44:47], a[140:143], v[196:199], v[44:47]\n\t"
    "s_waitcnt vmcnt(14)\n\t"
    "buffer_load_dwordx4 v[196:199], v98, s[28:31], s77 offen sc1\n\t"
    "v_mfma_f32_16x16x32_bf16 v[60:63], a[140:143], v[200:203], v[60:63]\n\t"
    "v_mfma_f32_16x16x32_bf16 v[56:59], a[136:139], v[200:203], v[56:59]\n\t"
    "v_mfma_f32_16x16x32_bf16 v[52:55], a[132:135], v[200:203], v[52:55]\n\t"
    "v_mfma_f32_16x16x32_bf16 v[48:51], a[128:131], v[200:203], v[48:51]\n\t"
    "s_waitcnt vmcnt(14)\n\t"
    "buffer_load_dwordx4 v[200:203], v99, s[28:31], s77 offen sc1\n\t"
    "v_mfma_f32_16x16x32_bf16 v[0:3], a[176:179], v[204:207], v[0:3]\n\t"
    "s_add_u32 s77, s77, 0x1400\n\t"
    "s_and_b32 s77, s77, 0x7fff\n\t"
    "v_mfma_f32_16x16x32_bf16 v[4:7], a[180:183], v[204:207], v[4:7]\n\t"
    "ds_read_b64_tr_b16 a[128:129], v100\n\t"
    "ds_read_b64_tr_b16 a[130:131], v100 offset:2048\n\t"
    "v_mfma_f32_16x16x32_bf16 v[8:11], a[184:187], v[204:207], v[8:11]\n\t"
    "ds_read_b64_tr_b16 a[132:133], v101\n\t"
    "ds_read_b64_tr_b16 a[134:135], v101 offset:2048\n\t"
    "v_mfma_f32_16x16x32_bf16 v[12:15], a[188:191], v[204:207], v[12:15]\n\t"
    "s_waitcnt vmcnt(14)\n\t"
    "buffer_load_dwordx4 v[204:207], v96, s[28:31], s77 offen sc1\n\t"
    "v_mfma_f32_16x16x32_bf16 v[28:31], a[188:191], v[208:211], v[28:31]\n\t"
    "ds_read_b64_tr_b16 a[136:137], v102\n\t"
    "ds_read_b64_tr_b16 a[138:139], v102 offset:2048\n\t"
    "v_mfma_f32_16x16x32_bf16 v[24:27], a[184:187], v[208:211], v[24:27]\n\t"
    "ds_read_b64_tr_b16 a[140:141], v103\n\t"
    "ds_read_b64_tr_b16 a[142:143], v103 offset:2048\n\t"
    "v_mfma_f32_16x16x32_bf16 v[20:23], a[180:183], v[208:211], v[20:23]\n\t"
    "v_mfma_f32_16x16x32_bf16 v[16:19], a[176:179], v[208:211], v[16:19]\n\t"
    "s_waitcnt vmcnt(14)\n\t"
    "buffer_load_dwordx4 v[208:211], v97, s[28:31], s77 offen sc1\n\t"
    "v_mfma_f32_16x16x32_bf16 v[32:35], a[176:179], v[212:215], v[32:35]\n\t"
    "v_mfma_f32_16x16x32_bf16 v[36:39], a[180:183], v[212:215], v[36:39]\n\t"
    "v_mfma_f32_16x16x32_bf16 v[40:43], a[184:187], v[212:215], v[40:43]\n\t"
    "v_mfma_f32_16x16x32_bf16 v[44:47], a[188:191], v[212:215], v[44:47]\n\t"
    "s_waitcnt vmcnt(14)\n\t"
    "buffer_load_dwordx4 v[212:215], v98, s[28:31], s77 offen sc1\n\t"
    "v_mfma_f32_16x16x32_bf16 v[60:63], a[188:191], v[216:219], v[60:63]\n\t"
    "v_mfma_f32_16x16x32_bf16 v[56:59], a[184:187], v[216:219], v[56:59]\n\t"
    "v_mfma_f32_16x16x32_bf16 v[52:55], a[180:183], v[216:219], v[52:55]\n\t"
    "v_mfma_f32_16x16x32_bf16 v[48:51], a[176:179], v[216:219], v[48:51]\n\t"
    "s_waitcnt vmcnt(14)\n\t"
    "buffer_load_dwordx4 v[216:219], v99, s[28:31], s77 offen sc1\n\t"
    "v_mfma_f32_16x16x32_bf16 v[0:3], a[160:163], v[220:223], v[0:3]\n\t"
    "s_add_u32 s77, s77, 0xc00\n\t"
    "s_and_b32 s77, s77, 0x7fff\n\t"
    "v_mfma_f32_16x16x32_bf16 v[4:7], a[164:167], v[220:223], v[4:7]\n\t"
    "ds_read_b64_tr_b16 a[176:177], v236 offset:49152\n\t"
    "ds_read_b64_tr_b16 a[178:179], v236 offset:51200\n\t"
    "v_mfma_f32_16x16x32_bf16 v[8:11], a[168:171], v[220:223], v[8:11]\n\t"
    "ds_read_b64_tr_b16 a[180:181], v237 offset:49152\n\t"
    "ds_read_b64_tr_b16 a[182:183], v237 offset:51200\n\t"
    "v_mfma_f32_16x16x32_bf16 v[12:15], a[172:175], v[220:223], v[12:15]\n\t"
    "s_waitcnt vmcnt(14)\n\t"
    "buffer_load_dwordx4 v[220:223], v96, s[28:31], s77 offen sc1\n\t"
    "v_mfma_f32_16x16x32_bf16 v[28:31], a[172:175], v[224:227], v[28:31]\n\t"
    "ds_read_b64_tr_b16 a[184:185], v238 offset:49152\n\t"
    "ds_read_b64_tr_b16 a[186:187], v238 offset:51200\n\t"
    "v_mfma_f32_16x16x32_bf16 v[24:27], a[168:171], v[224:227], v[24:27]\n\t"
    "ds_read_b64_tr_b16 a[188:189], v239 offset:49152\n\t"
    "ds_read_b64_tr_b16 a[190:191], v239 offset:51200\n\t"
    "v_mfma_f32_16x16x32_bf16 v[20:23], a[164:167], v[224:227], v[20:23]\n\t"
    "v_mfma_f32_16x16x32_bf16 v[16:19], a[160:163], v[224:227], v[16:19]\n\t"
    "s_waitcnt vmcnt(14)\n\t"
    "buffer_load_dwordx4 v[224:227], v97, s[28:31], s77 offen sc1\n\t"
    "v_mfma_f32_16x16x32_bf16 v[32:35], a[160:163], v[228:231], v[32:35]\n\t"
    "v_mfma_f32_16x16x32_bf16 v[36:39], a[164:167], v[228:231], v[36:39]\n\t"
    "v_mfma_f32_16x16x32_bf16 v[40:43], a[168:171], v[228:231], v[40:43]\n\t"
    "v_mfma_f32_16x16x32_bf16 v[44:47], a[172:175], v[228:231], v[44:47]\n\t"
    "s_waitcnt vmcnt(14)\n\t"
    "buffer_load_dwordx4 v[228:231], v98, s[28:31], s77 offen sc1\n\t"
    "v_mfma_f32_16x16x32_bf16 v[60:63], a[172:175], v[232:235], v[60:63]\n\t"
    "v_mfma_f32_16x16x32_bf16 v[56:59], a[168:171], v[232:235], v[56:59]\n\t"
    "v_mfma_f32_16x16x32_bf16 v[52:55], a[164:167], v[232:235], v[52:55]\n\t"
    "v_mfma_f32_16x16x32_bf16 v[48:51], a[160:163], v[232:235], v[48:51]\n\t"
    "s_waitcnt vmcnt(14)\n\t"
    "buffer_load_dwordx4 v[232:235], v99, s[28:31], s77 offen sc1\n\t"
    "v_mfma_f32_16x16x32_bf16 v[0:3], a[224:227], v[172:175], v[0:3]\n\t"
    "s_add_u32 s77, s77, 0x400\n\t"
    "s_and_b32 s77, s77, 0x7fff\n\t"
    "v_mfma_f32_16x16x32_bf16 v[4:7], a[228:231], v[172:175], v[4:7]\n\t"
    "ds_read_b64_tr_b16 a[160:161], v240 offset:49152\n\t"
    "ds_read_b64_tr_b16 a[162:163], v240 offset:51200\n\t"
    "v_mfma_f32_16x16x32_bf16 v[8:11], a[232:235], v[172:175], v[8:11]\n\t"
    "ds_read_b64_tr_b16 a[164:165], v241 offset:49152\n\t"
    "ds_read_b64_tr_b16 a[166:167], v241 offset:51200\n\t"
    "v_mfma_f32_16x16x32_bf16 v[12:15], a[236:239], v[172:175], v[12:15]\n\t"
    "s_waitcnt vmcnt(14)\n\t"
    "buffer_load_dwordx4 v[172:175], v96, s[28:31], s77 offen sc1\n\t"
    "v_mfma_f32_16x16x32_bf16 v[28:31], a[236:239], v[176:179], v[28:31]\n\t"
    "ds_read_b64_tr_b16 a[168:169], v242 offset:49152\n\t"
    "ds_read_b64_tr_b16 a[170:171], v242 offset:51200\n\t"
    "v_mfma_f32_16x16x32_bf16 v[24:27], a[232:235], v[176:179], v[24:27]\n\t"
    "ds_read_b64_tr_b16 a[172:173], v243 offset:49152\n\t"
    "ds_read_b64_tr_b16 a[174:175], v243 offset:51200\n\t"
    "v_mfma_f32_16x16x32_bf16 v[20:23], a[228:231], v[176:179], v[20:23]\n\t"
    "v_mfma_f32_16x16x32_bf16 v[16:19], a[224:227], v[176:179], v[16:19]\n\t"
    "s_waitcnt vmcnt(14)\n\t"
    "buffer_load_dwordx4 v[176:179], v97, s[28:31], s77 offen sc1\n\t"
    "v_mfma_f32_16x16x32_bf16 v[32:35], a[224:227], v[180:183], v[32:35]\n\t"
    "v_mfma_f32_16x16x32_bf16 v[36:39], a[228:231], v[180:183], v[36:39]\n\t"
    "v_mfma_f32_16x16x32_bf16 v[40:43], a[232:235], v[180:183], v[40:43]\n\t"
    "v_mfma_f32_16x16x32_bf16 v[44:47], a[236:239], v[180:183], v[44:47]\n\t"
    "s_waitcnt vmcnt(14)\n\t"
    "buffer_load_dwordx4 v[180:183], v98, s[28:31], s77 offen sc1\n\t"
    "v_mfma_f32_16x16x32_bf16 v[60:63], a[236:239], v[184:187], v[60:63]\n\t"
    "v_mfma_f32_16x16x32_bf16 v[56:59], a[232:235], v[184:187], v[56:59]\n\t"
    "v_mfma_f32_16x16x32_bf16 v[52:55], a[228:231], v[184:187], v[52:55]\n\t"
    "v_mfma_f32_16x16x32_bf16 v[48:51], a[224:227], v[184:187], v[48:51]\n\t"
    "s_waitcnt vmcnt(14)\n\t"
    "buffer_load_dwordx4 v[184:187], v99, s[28:31], s77 offen sc1\n\t"
    "v_mfma_f32_16x16x32_bf16 v[0:3], a[192:195], v[188:191], v[0:3]\n\t"
    "s_add_u32 s77, s77, 0x400\n\t"
    "s_and_b32 s77, s77, 0x7fff\n\t"
    "v_mfma_f32_16x16x32_bf16 v[4:7], a[196:199], v[188:191], v[4:7]\n\t"
    "ds_read_b64_tr_b16 a[224:225], v240\n\t"
    "ds_read_b64_tr_b16 a[226:227], v240 offset:2048\n\t"
    "v_mfma_f32_16x16x32_bf16 v[8:11], a[200:203], v[188:191], v[8:11]\n\t"
    "ds_read_b64_tr_b16 a[228:229], v241\n\t"
    "ds_read_b64_tr_b16 a[230:231], v241 offset:2048\n\t"
    "v_mfma_f32_16x16x32_bf16 v[12:15], a[204:207], v[188:191], v[12:15]\n\t"
    "s_waitcnt vmcnt(14)\n\t"
    "buffer_load_dwordx4 v[188:191], v96, s[28:31], s77 offen sc1\n\t"
    "v_mfma_f32_16x16x32_bf16 v[28:31], a[204:207], v[192:195], v[28:31]\n\t"
    "ds_read_b64_tr_b16 a[232:233], v242\n\t"
    "ds_read_b64_tr_b16 a[234:235], v242 offset:2048\n\t"
    "v_mfma_f32_16x16x32_bf16 v[24:27], a[200:203], v[192:195], v[24:27]\n\t"
    "ds_read_b64_tr_b16 a[236:237], v243\n\t"
    "ds_read_b64_tr_b16 a[238:239], v243 offset:2048\n\t"
    "v_mfma_f32_16x16x32_bf16 v[20:23], a[196:199], v[192:195], v[20:23]\n\t"
    "v_mfma_f32_16x16x32_bf16 v[16:19], a[192:195], v[192:195], v[16:19]\n\t"
    "s_waitcnt vmcnt(14)\n\t"
    "buffer_load_dwordx4 v[192:195], v97, s[28:31], s77 offen sc1\n\t"
    "v_mfma_f32_16x16x32_bf16 v[32:35], a[192:195], v[196:199], v[32:35]\n\t"
    "v_mfma_f32_16x16x32_bf16 v[36:39], a[196:199], v[196:199], v[36:39]\n\t"
    "v_mfma_f32_16x16x32_bf16 v[40:43], a[200:203], v[196:199], v[40:43]\n\t"
    "v_mfma_f32_16x16x32_bf16 v[44:47], a[204:207], v[196:199], v[44:47]\n\t"
    "s_waitcnt vmcnt(14)\n\t"
    "buffer_load_dwordx4 v[196:199], v98, s[28:31], s77 offen sc1\n\t"
    "v_mfma_f32_16x16x32_bf16 v[60:63], a[204:207], v[200:203], v[60:63]\n\t"
    "v_mfma_f32_16x16x32_bf16 v[56:59], a[200:203], v[200:203], v[56:59]\n\t"
    "v_mfma_f32_16x16x32_bf16 v[52:55], a[196:199], v[200:203], v[52:55]\n\t"
    "v_mfma_f32_16x16x32_bf16 v[48:51], a[192:195], v[200:203], v[48:51]\n\t"
    "s_waitcnt vmcnt(14)\n\t"
    "buffer_load_dwordx4 v[200:203], v99, s[28:31], s77 offen sc1\n\t"
    "v_mfma_f32_16x16x32_bf16 v[0:3], a[208:211], v[204:207], v[0:3]\n\t"
    "s_add_u32 s77, s77, 0x5c00\n\t"
    "s_and_b32 s77, s77, 0x7fff\n\t"
    "v_mfma_f32_16x16x32_bf16 v[4:7], a[212:215], v[204:207], v[4:7]\n\t"
    "ds_read_b64_tr_b16 a[192:193], v236 offset:16384\n\t"
    "ds_read_b64_tr_b16 a[194:195], v236 offset:18432\n\t"
    "v_mfma_f32_16x16x32_bf16 v[8:11], a[216:219], v[204:207], v[8:11]\n\t"
    "ds_read_b64_tr_b16 a[196:197], v237 offset:16384\n\t"
    "ds_read_b64_tr_b16 a[198:199], v237 offset:18432\n\t"
    "v_mfma_f32_16x16x32_bf16 v[12:15], a[220:223], v[204:207], v[12:15]\n\t"
    "s_waitcnt vmcnt(14)\n\t"
    "buffer_load_dwordx4 v[204:207], v96, s[28:31], s77 offen sc1\n\t"
    "v_mfma_f32_16x16x32_bf16 v[28:31], a[220:223], v[208:211], v[28:31]\n\t"
    "ds_read_b64_tr_b16 a[200:201], v238 offset:16384\n\t"
    "ds_read_b64_tr_b16 a[202:203], v238 offset:18432\n\t"
    "v_mfma_f32_16x16x32_bf16 v[24:27], a[216:219], v[208:211], v[24:27]\n\t"
    "ds_read_b64_tr_b16 a[204:205], v239 offset:16384\n\t"
    "ds_read_b64_tr_b16 a[206:207], v239 offset:18432\n\t"
    "v_mfma_f32_16x16x32_bf16 v[20:23], a[212:215], v[208:211], v[20:23]\n\t"
    "v_mfma_f32_16x16x32_bf16 v[16:19], a[208:211], v[208:211], v[16:19]\n\t"
    "s_waitcnt vmcnt(14)\n\t"
    "buffer_load_dwordx4 v[208:211], v97, s[28:31], s77 offen sc1\n\t"
    "v_mfma_f32_16x16x32_bf16 v[32:35], a[208:211], v[212:215], v[32:35]\n\t"
    "v_mfma_f32_16x16x32_bf16 v[36:39], a[212:215], v[212:215], v[36:39]\n\t"
    "v_mfma_f32_16x16x32_bf16 v[40:43], a[216:219], v[212:215], v[40:43]\n\t"
    "v_mfma_f32_16x16x32_bf16 v[44:47], a[220:223], v[212:215], v[44:47]\n\t"
    "s_waitcnt vmcnt(14)\n\t"
    "buffer_load_dwordx4 v[212:215], v98, s[28:31], s77 offen sc1\n\t"
    "v_mfma_f32_16x16x32_bf16 v[60:63], a[220:223], v[216:219], v[60:63]\n\t"
    "v_mfma_f32_16x16x32_bf16 v[56:59], a[216:219], v[216:219], v[56:59]\n\t"
    "v_mfma_f32_16x16x32_bf16 v[52:55], a[212:215], v[216:219], v[52:55]\n\t"
    "v_mfma_f32_16x16x32_bf16 v[48:51], a[208:211], v[216:219], v[48:51]\n\t"
    "s_waitcnt vmcnt(14)\n\t"
    "buffer_load_dwordx4 v[216:219], v99, s[28:31], s77 offen sc1\n\t"
    "v_mfma_f32_16x16x32_bf16 v[0:3], a[0:3], v[220:223], v[0:3]\n\t"
    "s_add_u32 s77, s77, 0x400\n\t"
    "s_and_b32 s77, s77, 0x7fff\n\t"
    "v_mfma_f32_16x16x32_bf16 v[4:7], a[4:7], v[220:223], v[4:7]\n\t"
    "v_mfma_f32_16x16x32_bf16 v[8:11], a[8:11], v[220:223], v[8:11]\n\t"
    "v_mfma_f32_16x16x32_bf16 v[12:15], a[12:15], v[220:223], v[12:15]\n\t"
    "s_waitcnt vmcnt(14)\n\t"
    "buffer_load_dwordx4 v[220:223], v96, s[28:31], s77 offen sc1\n\t"
    "v_mfma_f32_16x16x32_bf16 v[28:31], a[12:15], v[224:227], v[28:31]\n\t"
    "v_mfma_f32_16x16x32_bf16 v[24:27], a[8:11], v[224:227], v[24:27]\n\t"
    "v_mfma_f32_16x16x32_bf16 v[20:23], a[4:7], v[224:227], v[20:23]\n\t"
    "v_mfma_f32_16x16x32_bf16 v[16:19], a[0:3], v[224:227], v[16:19]\n\t"
    "s_waitcnt vmcnt(14)\n\t"
    "buffer_load_dwordx4 v[224:227], v97, s[28:31], s77 offen sc1\n\t"
    "v_mfma_f32_16x16x32_bf16 v[32:35], a[0:3], v[228:231], v[32:35]\n\t"
    "v_mfma_f32_16x16x32_bf16 v[36:39], a[4:7], v[228:231], v[36:39]\n\t"
    "v_mfma_f32_16x16x32_bf16 v[40:43], a[8:11], v[228:231], v[40:43]\n\t"
    "v_mfma_f32_16x16x32_bf16 v[44:47], a[12:15], v[228:231], v[44:47]\n\t"
    "s_waitcnt vmcnt(14)\n\t"
    "buffer_load_dwordx4 v[228:231], v98, s[28:31], s77 offen sc1\n\t"
    "v_mfma_f32_16x16x32_bf16 v[60:63], a[12:15], v[232:235], v[60:63]\n\t"
    "v_mfma_f32_16x16x32_bf16 v[56:59], a[8:11], v[232:235], v[56:59]\n\t"
    "v_mfma_f32_16x16x32_bf16 v[52:55], a[4:7], v[232:235], v[52:55]\n\t"
    "v_mfma_f32_16x16x32_bf16 v[48:51], a[0:3], v[232:235], v[48:51]\n\t"
    "s_waitcnt vmcnt(14)\n\t"
    "buffer_load_dwordx4 v[232:235], v99, s[28:31], s77 offen sc1\n\t"
    "v_mfma_f32_16x16x32_bf16 v[0:3], a[16:19], v[172:175], v[0:3]\n\t"
    "s_add_u32 s77, s77, 0x1000\n\t"
    "s_and_b32 s77, s77, 0x7fff\n\t"
    "v_mfma_f32_16x16x32_bf16 v[4:7], a[20:23], v[172:175], v[4:7]\n\t"
    "v_mfma_f32_16x16x32_bf16 v[8:11], a[24:27], v[172:175], v[8:11]\n\t"
    "v_mfma_f32_16x16x32_bf16 v[12:15], a[28:31], v[172:175], v[12:15]\n\t"
    "s_waitcnt vmcnt(14)\n\t"
    "buffer_load_dwordx4 v[172:175], v96, s[28:31], s77 offen sc1\n\t"
    "v_mfma_f32_16x16x32_bf16 v[28:31], a[28:31], v[176:179], v[28:31]\n\t"
    "v_mfma_f32_16x16x32_bf16 v[24:27], a[24:27], v[176:179], v[24:27]\n\t"
    "v_mfma_f32_16x16x32_bf16 v[20:23], a[20:23], v[176:179], v[20:23]\n\t"
    "v_mfma_f32_16x16x32_bf16 v[16:19], a[16:19], v[176:179], v[16:19]\n\t"
    "s_waitcnt vmcnt(14)\n\t"
    "buffer_load_dwordx4 v[176:179], v97, s[28:31], s77 offen sc1\n\t"
    "v_mfma_f32_16x16x32_bf16 v[32:35], a[16:19], v[180:183], v[32:35]\n\t"
    "v_mfma_f32_16x16x32_bf16 v[36:39], a[20:23], v[180:183], v[36:39]\n\t"
    "v_mfma_f32_16x16x32_bf16 v[40:43], a[24:27], v[180:183], v[40:43]\n\t"
    "v_mfma_f32_16x16x32_bf16 v[44:47], a[28:31], v[180:183], v[44:47]\n\t"
    "s_waitcnt vmcnt(14)\n\t"
    "buffer_load_dwordx4 v[180:183], v98, s[28:31], s77 offen sc1\n\t"
    "v_mfma_f32_16x16x32_bf16 v[60:63], a[28:31], v[184:187], v[60:63]\n\t"
    "v_mfma_f32_16x16x32_bf16 v[56:59], a[24:27], v[184:187], v[56:59]\n\t"
    "v_mfma_f32_16x16x32_bf16 v[52:55], a[20:23], v[184:187], v[52:55]\n\t"
    "v_mfma_f32_16x16x32_bf16 v[48:51], a[16:19], v[184:187], v[48:51]\n\t"
    "s_waitcnt vmcnt(14)\n\t"
    "buffer_load_dwordx4 v[184:187], v99, s[28:31], s77 offen sc1\n\t"
    "v_mfma_f32_16x16x32_bf16 v[0:3], a[32:35], v[188:191], v[0:3]\n\t"
    "s_add_u32 s77, s77, 0x400\n\t"
    "s_and_b32 s77, s77, 0x7fff\n\t"
    "v_mfma_f32_16x16x32_bf16 v[4:7], a[36:39], v[188:191], v[4:7]\n\t"
    "v_mfma_f32_16x16x32_bf16 v[8:11], a[40:43], v[188:191], v[8:11]\n\t"
    "v_mfma_f32_16x16x32_bf16 v[12:15], a[44:47], v[188:191], v[12:15]\n\t"
    "s_waitcnt vmcnt(14)\n\t"
    "buffer_load_dwordx4 v[188:191], v96, s[28:31], s77 offen sc1\n\t"
    "v_mfma_f32_16x16x32_bf16 v[28:31], a[44:47], v[192:195], v[28:31]\n\t"
    "v_mfma_f32_16x16x32_bf16 v[24:27], a[40:43], v[192:195], v[24:27]\n\t"
    "v_mfma_f32_16x16x32_bf16 v[20:23], a[36:39], v[192:195], v[20:23]\n\t"
    "v_mfma_f32_16x16x32_bf16 v[16:19], a[32:35], v[192:195], v[16:19]\n\t"
    "s_waitcnt vmcnt(14)\n\t"
    "buffer_load_dwordx4 v[192:195], v97, s[28:31], s77 offen sc1\n\t"
    "v_mfma_f32_16x16x32_bf16 v[32:35], a[32:35], v[196:199], v[32:35]\n\t"
    "v_mfma_f32_16x16x32_bf16 v[36:39], a[36:39], v[196:199], v[36:39]\n\t"
    "v_mfma_f32_16x16x32_bf16 v[40:43], a[40:43], v[196:199], v[40:43]\n\t"
    "v_mfma_f32_16x16x32_bf16 v[44:47], a[44:47], v[196:199], v[44:47]\n\t"
    "s_waitcnt vmcnt(14)\n\t"
    "buffer_load_dwordx4 v[196:199], v98, s[28:31], s77 offen sc1\n\t"
    "v_mfma_f32_16x16x32_bf16 v[60:63], a[44:47], v[200:203], v[60:63]\n\t"
    "v_mfma_f32_16x16x32_bf16 v[56:59], a[40:43], v[200:203], v[56:59]\n\t"
    "v_mfma_f32_16x16x32_bf16 v[52:55], a[36:39], v[200:203], v[52:55]\n\t"
    "v_mfma_f32_16x16x32_bf16 v[48:51], a[32:35], v[200:203], v[48:51]\n\t"
    "s_waitcnt vmcnt(14)\n\t"
    "buffer_load_dwordx4 v[200:203], v99, s[28:31], s77 offen sc1\n\t"
    "v_mfma_f32_16x16x32_bf16 v[0:3], a[48:51], v[204:207], v[0:3]\n\t"
    "s_add_u32 s77, s77, 0x1000\n\t"
    "s_and_b32 s77, s77, 0x7fff\n\t"
    "v_mfma_f32_16x16x32_bf16 v[4:7], a[52:55], v[204:207], v[4:7]\n\t"
    "v_mfma_f32_16x16x32_bf16 v[8:11], a[56:59], v[204:207], v[8:11]\n\t"
    "v_mfma_f32_16x16x32_bf16 v[12:15], a[60:63], v[204:207], v[12:15]\n\t"
    "s_waitcnt vmcnt(14)\n\t"
    "buffer_load_dwordx4 v[204:207], v96, s[28:31], s77 offen sc1\n\t"
    "v_mfma_f32_16x16x32_bf16 v[28:31], a[60:63], v[208:211], v[28:31]\n\t"
    "v_mfma_f32_16x16x32_bf16 v[24:27], a[56:59], v[208:211], v[24:27]\n\t"
    "v_mfma_f32_16x16x32_bf16 v[20:23], a[52:55], v[208:211], v[20:23]\n\t"
    "v_mfma_f32_16x16x32_bf16 v[16:19], a[48:51], v[208:211], v[16:19]\n\t"
    "s_waitcnt vmcnt(14)\n\t"
    "buffer_load_dwordx4 v[208:211], v97, s[28:31], s77 offen sc1\n\t"
    "v_mfma_f32_16x16x32_bf16 v[32:35], a[48:51], v[212:215], v[32:35]\n\t"
    "v_mfma_f32_16x16x32_bf16 v[36:39], a[52:55], v[212:215], v[36:39]\n\t"
    "v_mfma_f32_16x16x32_bf16 v[40:43], a[56:59], v[212:215], v[40:43]\n\t"
    "v_mfma_f32_16x16x32_bf16 v[44:47], a[60:63], v[212:215], v[44:47]\n\t"
    "s_waitcnt vmcnt(14)\n\t"
    "buffer_load_dwordx4 v[212:215], v98, s[28:31], s77 offen sc1\n\t"
    "v_mfma_f32_16x16x32_bf16 v[60:63], a[60:63], v[216:219], v[60:63]\n\t"
    "v_mfma_f32_16x16x32_bf16 v[56:59], a[56:59], v[216:219], v[56:59]\n\t"
    "v_mfma_f32_16x16x32_bf16 v[52:55], a[52:55], v[216:219], v[52:55]\n\t"
    "v_mfma_f32_16x16x32_bf16 v[48:51], a[48:51], v[216:219], v[48:51]\n\t"
    "s_waitcnt vmcnt(14)\n\t"
    "buffer_load_dwordx4 v[216:219], v99, s[28:31], s77 offen sc1\n\t"
    "v_mfma_f32_16x16x32_bf16 v[0:3], a[240:243], v[220:223], v[0:3]\n\t"
    "s_add_u32 s77, s77, 0x400\n\t"
    "s_and_b32 s77, s77, 0x7fff\n\t"
    "v_mfma_f32_16x16x32_bf16 v[4:7], a[244:247], v[220:223], v[4:7]\n\t"
    "v_mfma_f32_16x16x32_bf16 v[8:11], a[248:251], v[220:223], v[8:11]\n\t"
    "v_mfma_f32_16x16x32_bf16 v[12:15], a[252:255], v[220:223], v[12:15]\n\t"
    "s_waitcnt vmcnt(14)\n\t"
    "buffer_load_dwordx4 v[220:223], v96, s[28:31], s77 offen sc1\n\t"
    "v_mfma_f32_16x16x32_bf16 v[28:31], a[252:255], v[224:227], v[28:31]\n\t"
    "v_mfma_f32_16x16x32_bf16 v[24:27], a[248:251], v[224:227], v[24:27]\n\t"
    "v_mfma_f32_16x16x32_bf16 v[20:23], a[244:247], v[224:227], v[20:23]\n\t"
    "v_mfma_f32_16x16x32_bf16 v[16:19], a[240:243], v[224:227], v[16:19]\n\t"
    "s_waitcnt vmcnt(14)\n\t"
    "buffer_load_dwordx4 v[224:227], v97, s[28:31], s77 offen sc1\n\t"
    "v_mfma_f32_16x16x32_bf16 v[32:35], a[240:243], v[228:231], v[32:35]\n\t"
    "v_mfma_f32_16x16x32_bf16 v[36:39], a[244:247], v[228:231], v[36:39]\n\t"
    "v_mfma_f32_16x16x32_bf16 v[40:43], a[248:251], v[228:231], v[40:43]\n\t"
    "v_mfma_f32_16x16x32_bf16 v[44:47], a[252:255], v[228:231], v[44:47]\n\t"
    "s_waitcnt vmcnt(14)\n\t"
    "buffer_load_dwordx4 v[228:231], v98, s[28:31], s77 offen sc1\n\t"
    "v_mfma_f32_16x16x32_bf16 v[60:63], a[252:255], v[232:235], v[60:63]\n\t"
    "v_mfma_f32_16x16x32_bf16 v[56:59], a[248:251], v[232:235], v[56:59]\n\t"
    "v_mfma_f32_16x16x32_bf16 v[52:55], a[244:247], v[232:235], v[52:55]\n\t"
    "v_mfma_f32_16x16x32_bf16 v[48:51], a[240:243], v[232:235], v[48:51]\n\t"
    "s_waitcnt vmcnt(14)\n\t"
    "buffer_load_dwordx4 v[232:235], v99, s[28:31], s77 offen sc1\n\t"
    "v_mfma_f32_16x16x32_bf16 v[0:3], v[64:67], v[172:175], v[0:3]\n\t"
    "s_add_u32 s77, s77, 0x400\n\t"
    "s_and_b32 s77, s77, 0x7fff\n\t"
    "v_mfma_f32_16x16x32_bf16 v[4:7], v[68:71], v[172:175], v[4:7]\n\t"
    "v_mfma_f32_16x16x32_bf16 v[8:11], v[72:75], v[172:175], v[8:11]\n\t"
    "v_mfma_f32_16x16x32_bf16 v[12:15], v[76:79], v[172:175], v[12:15]\n\t"
    "s_waitcnt vmcnt(14)\n\t"
    "buffer_load_dwordx4 v[172:175], v96, s[28:31], s77 offen sc1\n\t"
    "v_mfma_f32_16x16x32_bf16 v[28:31], v[76:79], v[176:179], v[28:31]\n\t"
    "v_mfma_f32_16x16x32_bf16 v[24:27], v[72:75], v[176:179], v[24:27]\n\t"
    "v_mfma_f32_16x16x32_bf16 v[20:23], v[68:71], v[176:179], v[20:23]\n\t"
    "v_mfma_f32_16x16x32_bf16 v[16:19], v[64:67], v[176:179], v[16:19]\n\t"
    "s_waitcnt vmcnt(14)\n\t"
    "buffer_load_dwordx4 v[176:179], v97, s[28:31], s77 offen sc1\n\t"
    "v_mfma_f32_16x16x32_bf16 v[32:35], v[64:67], v[180:183], v[32:35]\n\t"
    "v_mfma_f32_16x16x32_bf16 v[36:39], v[68:71], v[180:183], v[36:39]\n\t"
    "v_mfma_f32_16x16x32_bf16 v[40:43], v[72:75], v[180:183], v[40:43]\n\t"
    "v_mfma_f32_16x16x32_bf16 v[44:47], v[76:79], v[180:183], v[44:47]\n\t"
    "s_waitcnt vmcnt(14)\n\t"
    "buffer_load_dwordx4 v[180:183], v98, s[28:31], s77 offen sc1\n\t"
    "v_mfma_f32_16x16x32_bf16 v[60:63], v[76:79], v[184:187], v[60:63]\n\t"
    "v_mfma_f32_16x16x32_bf16 v[56:59], v[72:75], v[184:187], v[56:59]\n\t"
    "v_mfma_f32_16x16x32_bf16 v[52:55], v[68:71], v[184:187], v[52:55]\n\t"
    "v_mfma_f32_16x16x32_bf16 v[48:51], v[64:67], v[184:187], v[48:51]\n\t"
    "s_waitcnt vmcnt(14)\n\t"
    "buffer_load_dwordx4 v[184:187], v99, s[28:31], s77 offen sc1\n\t"
    "v_mfma_f32_16x16x32_bf16 v[0:3], v[80:83], v[188:191], v[0:3]\n\t"
    "s_add_u32 s77, s77, 0x400\n\t"
    "s_and_b32 s77, s77, 0x7fff\n\t"
    "v_mfma_f32_16x16x32_bf16 v[4:7], v[84:87], v[188:191], v[4:7]\n\t"
    "v_mfma_f32_16x16x32_bf16 v[8:11], v[88:91], v[188:191], v[8:11]\n\t"
    "v_mfma_f32_16x16x32_bf16 v[12:15], v[92:95], v[188:191], v[12:15]\n\t"
    "s_waitcnt vmcnt(14)\n\t"
    "buffer_load_dwordx4 v[188:191], v96, s[28:31], s77 offen sc1\n\t"
    "v_mfma_f32_16x16x32_bf16 v[28:31], v[92:95], v[192:195], v[28:31]\n\t"
    "v_mfma_f32_16x16x32_bf16 v[24:27], v[88:91], v[192:195], v[24:27]\n\t"
    "v_mfma_f32_16x16x32_bf16 v[20:23], v[84:87], v[192:195], v[20:23]\n\t"
    "v_mfma_f32_16x16x32_bf16 v[16:19], v[80:83], v[192:195], v[16:19]\n\t"
    "s_waitcnt vmcnt(14)\n\t"
    "buffer_load_dwordx4 v[192:195], v97, s[28:31], s77 offen sc1\n\t"
    "v_mfma_f32_16x16x32_bf16 v[32:35], v[80:83], v[196:199], v[32:35]\n\t"
    "v_mfma_f32_16x16x32_bf16 v[36:39], v[84:87], v[196:199], v[36:39]\n\t"
    "v_mfma_f32_16x16x32_bf16 v[40:43], v[88:91], v[196:199], v[40:43]\n\t"
    "v_mfma_f32_16x16x32_bf16 v[44:47], v[92:95], v[196:199], v[44:47]\n\t"
    "s_waitcnt vmcnt(14)\n\t"
    "buffer_load_dwordx4 v[196:199], v98, s[28:31], s77 offen sc1\n\t"
    "v_mfma_f32_16x16x32_bf16 v[60:63], v[92:95], v[200:203], v[60:63]\n\t"
    "v_mfma_f32_16x16x32_bf16 v[56:59], v[88:91], v[200:203], v[56:59]\n\t"
    "v_mfma_f32_16x16x32_bf16 v[52:55], v[84:87], v[200:203], v[52:55]\n\t"
    "v_mfma_f32_16x16x32_bf16 v[48:51], v[80:83], v[200:203], v[48:51]\n\t"
    "s_waitcnt vmcnt(14)\n\t"
    "buffer_load_dwordx4 v[200:203], v99, s[28:31], s77 offen sc1\n\t"
    "v_mfma_f32_16x16x32_bf16 v[0:3], v[108:111], v[204:207], v[0:3]\n\t"
    "s_add_u32 s77, s77, 0x400\n\t"
    "s_and_b32 s77, s77, 0x7fff\n\t"
    "v_mfma_f32_16x16x32_bf16 v[4:7], v[112:115], v[204:207], v[4:7]\n\t"
    "v_mfma_f32_16x16x32_bf16 v[8:11], v[116:119], v[204:207], v[8:11]\n\t"
    "v_mfma_f32_16x16x32_bf16 v[12:15], v[120:123], v[204:207], v[12:15]\n\t"
    "s_waitcnt vmcnt(14)\n\t"
    "buffer_load_dwordx4 v[204:207], v96, s[28:31], s77 offen sc1\n\t"
    "v_mfma_f32_16x16x32_bf16 v[28:31], v[120:123], v[208:211], v[28:31]\n\t"
    "v_mfma_f32_16x16x32_bf16 v[24:27], v[116:119], v[208:211], v[24:27]\n\t"
    "v_mfma_f32_16x16x32_bf16 v[20:23], v[112:115], v[208:211], v[20:23]\n\t"
    "v_mfma_f32_16x16x32_bf16 v[16:19], v[108:111], v[208:211], v[16:19]\n\t"
    "s_waitcnt vmcnt(14)\n\t"
    "buffer_load_dwordx4 v[208:211], v97, s[28:31], s77 offen sc1\n\t"
    "v_mfma_f32_16x16x32_bf16 v[32:35], v[108:111], v[212:215], v[32:35]\n\t"
    "v_mfma_f32_16x16x32_bf16 v[36:39], v[112:115], v[212:215], v[36:39]\n\t"
    "v_mfma_f32_16x16x32_bf16 v[40:43], v[116:119], v[212:215], v[40:43]\n\t"
    "v_mfma_f32_16x16x32_bf16 v[44:47], v[120:123], v[212:215], v[44:47]\n\t"
    "s_waitcnt vmcnt(14)\n\t"
    "buffer_load_dwordx4 v[212:215], v98, s[28:31], s77 offen sc1\n\t"
    "v_mfma_f32_16x16x32_bf16 v[60:63], v[120:123], v[216:219], v[60:63]\n\t"
    "v_mfma_f32_16x16x32_bf16 v[56:59], v[116:119], v[216:219], v[56:59]\n\t"
    "v_mfma_f32_16x16x32_bf16 v[52:55], v[112:115], v[216:219], v[52:55]\n\t"
    "v_mfma_f32_16x16x32_bf16 v[48:51], v[108:111], v[216:219], v[48:51]\n\t"
    "s_waitcnt vmcnt(14)\n\t"
    "buffer_load_dwordx4 v[216:219], v99, s[28:31], s77 offen sc1\n\t"
    "v_mfma_f32_16x16x32_bf16 v[0:3], v[124:127], v[220:223], v[0:3]\n\t"
    "s_add_u32 s77, s77, 0x400\n\t"
    "s_and_b32 s77, s77, 0x7fff\n\t"
    "v_mfma_f32_16x16x32_bf16 v[4:7], v[128:131], v[220:223], v[4:7]\n\t"
    "v_mfma_f32_16x16x32_bf16 v[8:11], v[132:135], v[220:223], v[8:11]\n\t"
    "v_mfma_f32_16x16x32_bf16 v[12:15], v[136:139], v[220:223], v[12:15]\n\t"
    "s_waitcnt vmcnt(14)\n\t"
    "buffer_load_dwordx4 v[220:223], v96, s[28:31], s77 offen sc1\n\t"
    "v_mfma_f32_16x16x32_bf16 v[28:31], v[136:139], v[224:227], v[28:31]\n\t"
    "v_mfma_f32_16x16x32_bf16 v[24:27], v[132:135], v[224:227], v[24:27]\n\t"
    "v_mfma_f32_16x16x32_bf16 v[20:23], v[128:131], v[224:227], v[20:23]\n\t"
    "v_mfma_f32_16x16x32_bf16 v[16:19], v[124:127], v[224:227], v[16:19]\n\t"
    "s_waitcnt vmcnt(14)\n\t"
    "buffer_load_dwordx4 v[224:227], v97, s[28:31], s77 offen sc1\n\t"
    "v_mfma_f32_16x16x32_bf16 v[32:35], v[124:127], v[228:231], v[32:35]\n\t"
    "s_branch Lcs9%=\n\t"
    ".p2align 12\n\t"
    "Lc9%=:\n\t"
    "s_setpc_b64 s[22:23]\n\t"
    "Lcs9%=:\n\t"
    "v_mfma_f32_16x16x32_bf16 v[36:39], v[128:131], v[228:231], v[36:39]\n\t"
    "v_mfma_f32_16x16x32_bf16 v[40:43], v[132:135], v[228:231], v[40:43]\n\t"
    "v_mfma_f32_16x16x32_bf16 v[44:47], v[136:139], v[228:231], v[44:47]\n\t"
    "s_waitcnt vmcnt(14)\n\t"
    "buffer_load_dwordx4 v[228:231], v98, s[28:31], s77 offen sc1\n\t"
    "v_mfma_f32_16x16x32_bf16 v[60:63], v[136:139], v[232:235], v[60:63]\n\t"
    "v_mfma_f32_16x16x32_bf16 v[56:59], v[132:135], v[232:235], v[56:59]\n\t"
    "v_mfma_f32_16x16x32_bf16 v[52:55], v[128:131], v[232:235], v[52:55]\n\t"
    "v_mfma_f32_16x16x32_bf16 v[48:51], v[124:127], v[232:235], v[48:51]\n\t"
    "s_waitcnt vmcnt(14)\n\t"
    "buffer_load_dwordx4 v[232:235], v99, s[28:31], s77 offen sc1\n\t"
    "v_mfma_f32_16x16x32_bf16 v[0:3], v[140:143], v[172:175], v[0:3]\n\t"
    "s_add_u32 s77, s77, 0x400\n\t"
    "s_and_b32 s77, s77, 0x7fff\n\t"
    "v_mfma_f32_16x16x32_bf16 v[4:7], v[144:147], v[172:175], v[4:7]\n\t"
    "v_mfma_f32_16x16x32_bf16 v[8:11], v[148:151], v[172:175], v[8:11]\n\t"
    "v_mfma_f32_16x16x32_bf16 v[12:15], v[152:155], v[172:175], v[12:15]\n\t"
    "s_waitcnt vmcnt(14)\n\t"
    "buffer_load_dwordx4 v[172:175], v96, s[28:31], s77 offen sc1\n\t"
    "v_mfma_f32_16x16x32_bf16 v[28:31], v[152:155], v[176:179], v[28:31]\n\t"
    "v_mfma_f32_16x16x32_bf16 v[24:27], v[148:151], v[176:179], v[24:27]\n\t"
    "v_mfma_f32_16x16x32_bf16 v[20:23], v[144:147], v[176:179], v[20:23]\n\t"
    "v_mfma_f32_16x16x32_bf16 v[16:19], v[140:143], v[176:179], v[16:19]\n\t"
    "s_waitcnt vmcnt(14)\n\t"
    "buffer_load_dwordx4 v[176:179], v97, s[28:31], s77 offen sc1\n\t"
    "v_mfma_f32_16x16x32_bf16 v[32:35], v[140:143], v[180:183], v[32:35]\n\t"
    "v_mfma_f32_16x16x32_bf16 v[36:39], v[144:147], v[180:183], v[36:39]\n\t"
    "v_mfma_f32_16x16x32_bf16 v[40:43], v[148:151], v[180:183], v[40:43]\n\t"
    "v_mfma_f32_16x16x32_bf16 v[44:47], v[152:155], v[180:183], v[44:47]\n\t"
    "s_waitcnt vmcnt(14)\n\t"
    "buffer_load_dwordx4 v[180:183], v98, s[28:31], s77 offen sc1\n\t"
    "v_mfma_f32_16x16x32_bf16 v[60:63], v[152:155], v[184:187], v[60:63]\n\t"
    "v_mfma_f32_16x16x32_bf16 v[56:59], v[148:151], v[184:187], v[56:59]\n\t"
    "v_mfma_f32_16x16x32_bf16 v[52:55], v[144:147], v[184:187], v[52:55]\n\t"
    "v_mfma_f32_16x16x32_bf16 v[48:51], v[140:143], v[184:187], v[48:51]\n\t"
    "s_waitcnt vmcnt(14)\n\t"
    "buffer_load_dwordx4 v[184:187], v99, s[28:31], s77 offen sc1\n\t"
    "v_mfma_f32_16x16x32_bf16 v[0:3], v[156:159], v[188:191], v[0:3]\n\t"
    "s_add_u32 s77, s77, 0x400\n\t"
    "s_and_b32 s77, s77, 0x7fff\n\t"
    "v_mfma_f32_16x16x32_bf16 v[4:7], v[160:163], v[188:191], v[4:7]\n\t"
    "v_mfma_f32_16x16x32_bf16 v[8:11], v[164:167], v[188:191], v[8:11]\n\t"
    "v_mfma_f32_16x16x32_bf16 v[12:15], v[168:171], v[188:191], v[12:15]\n\t"
    "s_waitcnt vmcnt(14)\n\t"
    "buffer_load_dwordx4 v[188:191], v96, s[28:31], s77 offen sc1\n\t"
    "v_mfma_f32_16x16x32_bf16 v[28:31], v[168:171], v[192:195], v[28:31]\n\t"
    "v_mfma_f32_16x16x32_bf16 v[24:27], v[164:167], v[192:195], v[24:27]\n\t"
    "v_mfma_f32_16x16x32_bf16 v[20:23], v[160:163], v[192:195], v[20:23]\n\t"
    "v_mfma_f32_16x16x32_bf16 v[16:19], v[156:159], v[192:195], v[16:19]\n\t"
    "s_waitcnt vmcnt(14)\n\t"
    "buffer_load_dwordx4 v[192:195], v97, s[28:31], s77 offen sc1\n\t"
    "v_mfma_f32_16x16x32_bf16 v[32:35], v[156:159], v[196:199], v[32:35]\n\t"
    "v_mfma_f32_16x16x32_bf16 v[36:39], v[160:163], v[196:199], v[36:39]\n\t"
    "v_mfma_f32_16x16x32_bf16 v[40:43], v[164:167], v[196:199], v[40:43]\n\t"
    "v_mfma_f32_16x16x32_bf16 v[44:47], v[168:171], v[196:199], v[44:47]\n\t"
    "s_waitcnt vmcnt(14)\n\t"
    "buffer_load_dwordx4 v[196:199], v98, s[28:31], s77 offen sc1\n\t"
    "v_mfma_f32_16x16x32_bf16 v[60:63], v[168:171], v[200:203], v[60:63]\n\t"
    "v_mfma_f32_16x16x32_bf16 v[56:59], v[164:167], v[200:203], v[56:59]\n\t"
    "v_mfma_f32_16x16x32_bf16 v[52:55], v[160:163], v[200:203], v[52:55]\n\t"
    "v_mfma_f32_16x16x32_bf16 v[48:51], v[156:159], v[200:203], v[48:51]\n\t"
    "s_waitcnt vmcnt(14) lgkmcnt(15)\n\t"
    "buffer_load_dwordx4 v[200:203], v99, s[28:31], s77 offen sc1\n\t"
    "v_mfma_f32_16x16x32_bf16 v[0:3], a[64:67], v[204:207], v[0:3]\n\t"
    "s_add_u32 s77, s77, 0x400\n\t"
    "s_and_b32 s77, s77, 0x7fff\n\t"
    "v_mfma_f32_16x16x32_bf16 v[4:7], a[68:71], v[204:207], v[4:7]\n\t"
    "v_mfma_f32_16x16x32_bf16 v[8:11], a[72:75], v[204:207], v[8:11]\n\t"
    "v_mfma_f32_16x16x32_bf16 v[12:15], a[76:79], v[204:207], v[12:15]\n\t"
    "s_waitcnt vmcnt(14)\n\t"
    "buffer_load_dwordx4 v[204:207], v96, s[28:31], s77 offen sc1\n\t"
    "v_mfma_f32_16x16x32_bf16 v[28:31], a[76:79], v[208:211], v[28:31]\n\t"
    "v_mfma_f32_16x16x32_bf16 v[24:27], a[72:75], v[208:211], v[24:27]\n\t"
    "v_mfma_f32_16x16x32_bf16 v[20:23], a[68:71], v[208:211], v[20:23]\n\t"
    "v_mfma_f32_16x16x32_bf16 v[16:19], a[64:67], v[208:211], v[16:19]\n\t"
    "s_waitcnt vmcnt(14)\n\t"
    "buffer_load_dwordx4 v[208:211], v97, s[28:31], s77 offen sc1\n\t"
    "v_mfma_f32_16x16x32_bf16 v[32:35], a[64:67], v[212:215], v[32:35]\n\t"
    "v_mfma_f32_16x16x32_bf16 v[36:39], a[68:71], v[212:215], v[36:39]\n\t"
    "v_mfma_f32_16x16x32_bf16 v[40:43], a[72:75], v[212:215], v[40:43]\n\t"
    "v_mfma_f32_16x16x32_bf16 v[44:47], a[76:79], v[212:215], v[44:47]\n\t"
    "s_waitcnt vmcnt(14)\n\t"
    "buffer_load_dwordx4 v[212:215], v98, s[28:31], s77 offen sc1\n\t"
    "v_mfma_f32_16x16x32_bf16 v[60:63], a[76:79], v[216:219], v[60:63]\n\t"
    "v_mfma_f32_16x16x32_bf16 v[56:59], a[72:75], v[216:219], v[56:59]\n\t"
    "v_mfma_f32_16x16x32_bf16 v[52:55], a[68:71], v[216:219], v[52:55]\n\t"
    "v_mfma_f32_16x16x32_bf16 v[48:51], a[64:67], v[216:219], v[48:51]\n\t"
    "s_waitcnt vmcnt(14)\n\t"
    "buffer_load_dwordx4 v[216:219], v99, s[28:31], s77 offen sc1\n\t"
    "v_mfma_f32_16x16x32_bf16 v[0:3], a[80:83], v[220:223], v[0:3]\n\t"
    "s_add_u32 s77, s77, 0x400\n\t"
    "s_and_b32 s77, s77, 0x7fff\n\t"
    "v_mfma_f32_16x16x32_bf16 v[4:7], a[84:87], v[220:223], v[4:7]\n\t"
    "v_mfma_f32_16x16x32_bf16 v[8:11], a[88:91], v[220:223], v[8:11]\n\t"
    "v_mfma_f32_16x16x32_bf16 v[12:15], a[92:95], v[220:223], v[12:15]\n\t"
    "s_waitcnt vmcnt(14)\n\t"
    "buffer_load_dwordx4 v[220:223], v96, s[28:31], s77 offen sc1\n\t"
    "v_mfma_f32_16x16x32_bf16 v[28:31], a[92:95], v[224:227], v[28:31]\n\t"
    "v_mfma_f32_16x16x32_bf16 v[24:27], a[88:91], v[224:227], v[24:27]\n\t"
    "v_mfma_f32_16x16x32_bf16 v[20:23], a[84:87], v[224:227], v[20:23]\n\t"
    "v_mfma_f32_16x16x32_bf16 v[16:19], a[80:83], v[224:227], v[16:19]\n\t"
    "s_waitcnt vmcnt(14)\n\t"
    "buffer_load_dwordx4 v[224:227], v97, s[28:31], s77 offen sc1\n\t"
    "v_mfma_f32_16x16x32_bf16 v[32:35], a[80:83], v[228:231], v[32:35]\n\t"
    "v_mfma_f32_16x16x32_bf16 v[36:39], a[84:87], v[228:231], v[36:39]\n\t"
    "v_mfma_f32_16x16x32_bf16 v[40:43], a[88:91], v[228:231], v[40:43]\n\t"
    "v_mfma_f32_16x16x32_bf16 v[44:47], a[92:95], v[228:231], v[44:47]\n\t"
    "s_waitcnt vmcnt(14)\n\t"
    "buffer_load_dwordx4 v[228:231], v98, s[28:31], s77 offen sc1\n\t"
    "v_mfma_f32_16x16x32_bf16 v[60:63], a[92:95], v[232:235], v[60:63]\n\t"
    "v_mfma_f32_16x16x32_bf16 v[56:59], a[88:91], v[232:235], v[56:59]\n\t"
    "v_mfma_f32_16x16x32_bf16 v[52:55], a[84:87], v[232:235], v[52:55]\n\t"
    "v_mfma_f32_16x16x32_bf16 v[48:51], a[80:83], v[232:235], v[48:51]\n\t"
    "s_waitcnt vmcnt(14)\n\t"
    "buffer_load_dwordx4 v[232:235], v99, s[28:31], s77 offen sc1\n\t"
    "v_mfma_f32_16x16x32_bf16 v[0:3], a[96:99], v[172:175], v[0:3]\n\t"
    "s_add_u32 s77, s77, 0x400\n\t"
    "s_and_b32 s77, s77, 0x7fff\n\t"
    "v_mfma_f32_16x16x32_bf16 v[4:7], a[100:103], v[172:175], v[4:7]\n\t"
    "v_mfma_f32_16x16x32_bf16 v[8:11], a[104:107], v[172:175], v[8:11]\n\t"
    "v_mfma_f32_16x16x32_bf16 v[12:15], a[108:111], v[172:175], v[12:15]\n\t"
    "s_waitcnt vmcnt(14)\n\t"
    "buffer_load_dwordx4 v[172:175], v96, s[28:31], s77 offen sc1\n\t"
    "v_mfma_f32_16x16x32_bf16 v[28:31], a[108:111], v[176:179], v[28:31]\n\t"
    "v_mfma_f32_16x16x32_bf16 v[24:27], a[104:107], v[176:179], v[24:27]\n\t"
    "v_mfma_f32_16x16x32_bf16 v[20:23], a[100:103], v[176:179], v[20:23]\n\t"
    "v_mfma_f32_16x16x32_bf16 v[16:19], a[96:99], v[176:179], v[16:19]\n\t"
    "s_waitcnt vmcnt(14)\n\t"
    "buffer_load_dwordx4 v[176:179], v97, s[28:31], s77 offen sc1\n\t"
    "v_mfma_f32_16x16x32_bf16 v[32:35], a[96:99], v[180:183], v[32:35]\n\t"
    "v_mfma_f32_16x16x32_bf16 v[36:39], a[100:103], v[180:183], v[36:39]\n\t"
    "v_mfma_f32_16x16x32_bf16 v[40:43], a[104:107], v[180:183], v[40:43]\n\t"
    "v_mfma_f32_16x16x32_bf16 v[44:47], a[108:111], v[180:183], v[44:47]\n\t"
    "s_waitcnt vmcnt(14)\n\t"
    "buffer_load_dwordx4 v[180:183], v98, s[28:31], s77 offen sc1\n\t"
    "v_mfma_f32_16x16x32_bf16 v[60:63], a[108:111], v[184:187], v[60:63]\n\t"
    "v_mfma_f32_16x16x32_bf16 v[56:59], a[104:107], v[184:187], v[56:59]\n\t"
    "v_mfma_f32_16x16x32_bf16 v[52:55], a[100:103], v[184:187], v[52:55]\n\t"
    "v_mfma_f32_16x16x32_bf16 v[48:51], a[96:99], v[184:187], v[48:51]\n\t"
    "s_waitcnt vmcnt(14)\n\t"
    "buffer_load_dwordx4 v[184:187], v99, s[28:31], s77 offen sc1\n\t"
    "v_mfma_f32_16x16x32_bf16 v[0:3], a[144:147], v[188:191], v[0:3]\n\t"
    "s_add_u32 s77, s77, 0x400\n\t"
    "s_and_b32 s77, s77, 0x7fff\n\t"
    "v_mfma_f32_16x16x32_bf16 v[4:7], a[148:151], v[188:191], v[4:7]\n\t"
    "v_mfma_f32_16x16x32_bf16 v[8:11], a[152:155], v[188:191], v[8:11]\n\t"
    "v_mfma_f32_16x16x32_bf16 v[12:15], a[156:159], v[188:191], v[12:15]\n\t"
    "s_waitcnt vmcnt(14)\n\t"
    "buffer_load_dwordx4 v[188:191], v96, s[28:31], s77 offen sc1\n\t"
    "v_mfma_f32_16x16x32_bf16 v[28:31], a[156:159], v[192:195], v[28:31]\n\t"
    "v_mfma_f32_16x16x32_bf16 v[24:27], a[152:155], v[192:195], v[24:27]\n\t"
    "v_mfma_f32_16x16x32_bf16 v[20:23], a[148:151], v[192:195], v[20:23]\n\t"
    "v_mfma_f32_16x16x32_bf16 v[16:19], a[144:147], v[192:195], v[16:19]\n\t"
    "s_waitcnt vmcnt(14)\n\t"
    "buffer_load_dwordx4 v[192:195], v97, s[28:31], s77 offen sc1\n\t"
    "v_mfma_f32_16x16x32_bf16 v[32:35], a[144:147], v[196:199], v[32:35]\n\t"
    "v_mfma_f32_16x16x32_bf16 v[36:39], a[148:151], v[196:199], v[36:39]\n\t"
    "v_mfma_f32_16x16x32_bf16 v[40:43], a[152:155], v[196:199], v[40:43]\n\t"
    "v_mfma_f32_16x16x32_bf16 v[44:47], a[156:159], v[196:199], v[44:47]\n\t"
    "s_waitcnt vmcnt(14)\n\t"
    "buffer_load_dwordx4 v[196:199], v98, s[28:31], s77 offen sc1\n\t"
    "v_mfma_f32_16x16x32_bf16 v[60:63], a[156:159], v[200:203], v[60:63]\n\t"
    "v_mfma_f32_16x16x32_bf16 v[56:59], a[152:155], v[200:203], v[56:59]\n\t"
    "v_mfma_f32_16x16x32_bf16 v[52:55], a[148:151], v[200:203], v[52:55]\n\t"
    "v_mfma_f32_16x16x32_bf16 v[48:51], a[144:147], v[200:203], v[48:51]\n\t"
    "s_waitcnt vmcnt(14)\n\t"
    "buffer_load_dwordx4 v[200:203], v99, s[28:31], s77 offen sc1\n\t"
    "v_mfma_f32_16x16x32_bf16 v[0:3], a[112:115], v[204:207], v[0:3]\n\t"
    "s_add_u32 s77, s77, 0x400\n\t"
    "s_and_b32 s77, s77, 0x7fff\n\t"
    "v_mfma_f32_16x16x32_bf16 v[4:7], a[116:119], v[204:207], v[4:7]\n\t"
    "v_mfma_f32_16x16x32_bf16 v[8:11], a[120:123], v[204:207], v[8:11]\n\t"
    "v_mfma_f32_16x16x32_bf16 v[12:15], a[124:127], v[204:207], v[12:15]\n\t"
    "s_waitcnt vmcnt(14)\n\t"
    "buffer_load_dwordx4 v[204:207], v96, s[28:31], s77 offen sc1\n\t"
    "v_mfma_f32_16x16x32_bf16 v[28:31], a[124:127], v[208:211], v[28:31]\n\t"
    "v_mfma_f32_16x16x32_bf16 v[24:27], a[120:123], v[208:211], v[24:27]\n\t"
    "v_mfma_f32_16x16x32_bf16 v[20:23], a[116:119], v[208:211], v[20:23]\n\t"
    "v_mfma_f32_16x16x32_bf16 v[16:19], a[112:115], v[208:211], v[16:19]\n\t"
    "s_waitcnt vmcnt(14)\n\t"
    "buffer_load_dwordx4 v[208:211], v97, s[28:31], s77 offen sc1\n\t"
    "v_mfma_f32_16x16x32_bf16 v[32:35], a[112:115], v[212:215], v[32:35]\n\t"
    "v_mfma_f32_16x16x32_bf16 v[36:39], a[116:119], v[212:215], v[36:39]\n\t"
    "v_mfma_f32_16x16x32_bf16 v[40:43], a[120:123], v[212:215], v[40:43]\n\t"
    "v_mfma_f32_16x16x32_bf16 v[44:47], a[124:127], v[212:215], v[44:47]\n\t"
    "s_waitcnt vmcnt(14)\n\t"
    "buffer_load_dwordx4 v[212:215], v98, s[28:31], s77 offen sc1\n\t"
    "v_mfma_f32_16x16x32_bf16 v[60:63], a[124:127], v[216:219], v[60:63]\n\t"
    "v_mfma_f32_16x16x32_bf16 v[56:59], a[120:123], v[216:219], v[56:59]\n\t"
    "v_mfma_f32_16x16x32_bf16 v[52:55], a[116:119], v[216:219], v[52:55]\n\t"
    "v_mfma_f32_16x16x32_bf16 v[48:51], a[112:115], v[216:219], v[48:51]\n\t"
    "s_waitcnt vmcnt(14)\n\t"
    "buffer_load_dwordx4 v[216:219], v99, s[28:31], s77 offen sc1\n\t"
    "v_mfma_f32_16x16x32_bf16 v[0:3], a[128:131], v[220:223], v[0:3]\n\t"
    "s_add_u32 s77, s77, 0x400\n\t"
    "s_and_b32 s77, s77, 0x7fff\n\t"
    "v_mfma_f32_16x16x32_bf16 v[4:7], a[132:135], v[220:223], v[4:7]\n\t"
    "v_mfma_f32_16x16x32_bf16 v[8:11], a[136:139], v[220:223], v[8:11]\n\t"
    "v_mfma_f32_16x16x32_bf16 v[12:15], a[140:143], v[220:223], v[12:15]\n\t"
    "s_waitcnt vmcnt(14)\n\t"
    "buffer_load_dwordx4 v[220:223], v96, s[28:31], s77 offen sc1\n\t"
    "v_mfma_f32_16x16x32_bf16 v[28:31], a[140:143], v[224:227], v[28:31]\n\t"
    "v_mfma_f32_16x16x32_bf16 v[24:27], a[136:139], v[224:227], v[24:27]\n\t"
    "v_mfma_f32_16x16x32_bf16 v[20:23], a[132:135], v[224:227], v[20:23]\n\t"
    "v_mfma_f32_16x16x32_bf16 v[16:19], a[128:131], v[224:227], v[16:19]\n\t"
    "s_waitcnt vmcnt(14)\n\t"
    "buffer_load_dwordx4 v[224:227], v97, s[28:31], s77 offen sc1\n\t"
    "v_mfma_f32_16x16x32_bf16 v[32:35], a[128:131], v[228:231], v[32:35]\n\t"
    "v_mfma_f32_16x16x32_bf16 v[36:39], a[132:135], v[228:231], v[36:39]\n\t"
    "v_mfma_f32_16x16x32_bf16 v[40:43], a[136:139], v[228:231], v[40:43]\n\t"
    "v_mfma_f32_16x16x32_bf16 v[44:47], a[140:143], v[228:231], v[44:47]\n\t"
    "s_waitcnt vmcnt(14)\n\t"
    "buffer_load_dwordx4 v[228:231], v98, s[28:31], s77 offen sc1\n\t"
    "v_mfma_f32_16x16x32_bf16 v[60:63], a[140:143], v[232:235], v[60:63]\n\t"
    "v_mfma_f32_16x16x32_bf16 v[56:59], a[136:139], v[232:235], v[56:59]\n\t"
    "v_mfma_f32_16x16x32_bf16 v[52:55], a[132:135], v[232:235], v[52:55]\n\t"
    "v_mfma_f32_16x16x32_bf16 v[48:51], a[128:131], v[232:235], v[48:51]\n\t"
    "s_waitcnt vmcnt(14)\n\t"
    "buffer_load_dwordx4 v[232:235], v99, s[28:31], s77 offen sc1\n\t"
    "v_mfma_f32_16x16x32_bf16 v[0:3], a[176:179], v[172:175], v[0:3]\n\t"
    "s_add_u32 s77, s77, 0x400\n\t"
    "s_and_b32 s77, s77, 0x7fff\n\t"
    "v_mfma_f32_16x16x32_bf16 v[4:7], a[180:183], v[172:175], v[4:7]\n\t"
    "v_mfma_f32_16x16x32_bf16 v[8:11], a[184:187], v[172:175], v[8:11]\n\t"
    "v_mfma_f32_16x16x32_bf16 v[12:15], a[188:191], v[172:175], v[12:15]\n\t"
    "s_waitcnt vmcnt(11)\n\t"
    "v_mfma_f32_16x16x32_bf16 v[0:3], a[160:163], v[188:191], v[0:3]\n\t"
    "v_mfma_f32_16x16x32_bf16 v[4:7], a[164:167], v[188:191], v[4:7]\n\t"
    "v_mfma_f32_16x16x32_bf16 v[8:11], a[168:171], v[188:191], v[8:11]\n\t"
    "v_mfma_f32_16x16x32_bf16 v[12:15], a[172:175], v[188:191], v[12:15]\n\t"
    "s_waitcnt vmcnt(7) lgkmcnt(14)\n\t"
    "v_mfma_f32_16x16x32_bf16 v[0:3], a[224:227], v[204:207], v[0:3]\n\t"
    "s_waitcnt lgkmcnt(12)\n\t"
    "v_mfma_f32_16x16x32_bf16 v[4:7], a[228:231], v[204:207], v[4:7]\n\t"
    "s_waitcnt lgkmcnt(10)\n\t"
    "v_mfma_f32_16x16x32_bf16 v[8:11], a[232:235], v[204:207], v[8:11]\n\t"
    "s_waitcnt lgkmcnt(8)\n\t"
    "v_mfma_f32_16x16x32_bf16 v[12:15], a[236:239], v[204:207], v[12:15]\n\t"
    "s_waitcnt vmcnt(3) lgkmcnt(6)\n\t"
    "v_mfma_f32_16x16x32_bf16 v[0:3], a[192:195], v[220:223], v[0:3]\n\t"
    "s_waitcnt lgkmcnt(4)\n\t"
    "v_mfma_f32_16x16x32_bf16 v[4:7], a[196:199], v[220:223], v[4:7]\n\t"
    "s_waitcnt lgkmcnt(2)\n\t"
    "v_mfma_f32_16x16x32_bf16 v[8:11], a[200:203], v[220:223], v[8:11]\n\t"
    "s_waitcnt lgkmcnt(0)\n\t"
    "v_mfma_f32_16x16x32_bf16 v[12:15], a[204:207], v[220:223], v[12:15]\n\t"
    "v_mfma_f32_16x16x32_bf16 v[16:19], a[176:179], v[176:179], v[16:19]\n\t"
    "v_mfma_f32_16x16x32_bf16 v[20:23], a[180:183], v[176:179], v[20:23]\n\t"
    "v_mfma_f32_16x16x32_bf16 v[24:27], a[184:187], v[176:179], v[24:27]\n\t"
    "v_mfma_f32_16x16x32_bf16 v[28:31], a[188:191], v[176:179], v[28:31]\n\t"
    "v_mfma_f32_16x16x32_bf16 v[16:19], a[160:163], v[192:195], v[16:19]\n\t"
    "v_mfma_f32_16x16x32_bf16 v[20:23], a[164:167], v[192:195], v[20:23]\n\t"
    "ds_write_b128 v248, v[0:3]\n\t"
    "v_mfma_f32_16x16x32_bf16 v[24:27], a[168:171], v[192:195], v[24:27]\n\t"
    "v_mfma_f32_16x16x32_bf16 v[28:31], a[172:175], v[192:195], v[28:31]\n\t"
    "v_mfma_f32_16x16x32_bf16 v[16:19], a[224:227], v[208:211], v[16:19]\n\t"
    "ds_write_b128 v248, v[4:7] offset:1024\n\t"
    "v_mfma_f32_16x16x32_bf16 v[20:23], a[228:231], v[208:211], v[20:23]\n\t"
    "v_mfma_f32_16x16x32_bf16 v[24:27], a[232:235], v[208:211], v[24:27]\n\t"
    "v_mfma_f32_16x16x32_bf16 v[28:31], a[236:239], v[208:211], v[28:31]\n\t"
    "s_waitcnt vmcnt(2)\n\t"
    "ds_write_b128 v248, v[8:11] offset:2048\n\t"
    "v_mfma_f32_16x16x32_bf16 v[16:19], a[192:195], v[224:227], v[16:19]\n\t"
    "v_mfma_f32_16x16x32_bf16 v[20:23], a[196:199], v[224:227], v[20:23]\n\t"
    "v_mfma_f32_16x16x32_bf16 v[24:27], a[200:203], v[224:227], v[24:27]\n\t"
    "ds_write_b128 v248, v[12:15] offset:3072\n\t"
    "v_mfma_f32_16x16x32_bf16 v[28:31], a[204:207], v[224:227], v[28:31]\n\t"
    "v_mfma_f32_16x16x32_bf16 v[32:35], a[176:179], v[180:183], v[32:35]\n\t"
    "v_mfma_f32_16x16x32_bf16 v[36:39], a[180:183], v[180:183], v[36:39]\n\t"
    "v_mfma_f32_16x16x32_bf16 v[40:43], a[184:187], v[180:183], v[40:43]\n\t"
    "v_mfma_f32_16x16x32_bf16 v[44:47], a[188:191], v[180:183], v[44:47]\n\t"
    "v_mfma_f32_16x16x32_bf16 v[32:35], a[160:163], v[196:199], v[32:35]\n\t"
    "v_mfma_f32_16x16x32_bf16 v[36:39], a[164:167], v[196:199], v[36:39]\n\t"
    "ds_write_b128 v248, v[16:19] offset:16384\n\t"
    "v_mfma_f32_16x16x32_bf16 v[40:43], a[168:171], v[196:199], v[40:43]\n\t"
    "v_mfma_f32_16x16x32_bf16 v[44:47], a[172:175], v[196:199], v[44:47]\n\t"
    "v_mfma_f32_16x16x32_bf16 v[32:35], a[224:227], v[212:215], v[32:35]\n\t"
    "ds_write_b128 v248, v[20:23] offset:17408\n\t"
    "v_mfma_f32_16x16x32_bf16 v[36:39], a[228:231], v[212:215], v[36:39]\n\t"
    "v_mfma_f32_16x16x32_bf16 v[40:43], a[232:235], v[212:215], v[40:43]\n\t"
    "v_mfma_f32_16x16x32_bf16 v[44:47], a[236:239], v[212:215], v[44:47]\n\t"
    "s_waitcnt vmcnt(1)\n\t"
    "ds_write_b128 v248, v[24:27] offset:18432\n\t"
    "v_mfma_f32_16x16x32_bf16 v[32:35], a[192:195], v[228:231], v[32:35]\n\t"
    "v_mfma_f32_16x16x32_bf16 v[36:39], a[196:199], v[228:231], v[36:39]\n\t"
    "v_mfma_f32_16x16x32_bf16 v[40:43], a[200:203], v[228:231], v[40:43]\n\t"
    "ds_write_b128 v248, v[28:31] offset:19456\n\t"
    "v_mfma_f32_16x16x32_bf16 v[44:47], a[204:207], v[228:231], v[44:47]\n\t"
    "v_mfma_f32_16x16x32_bf16 v[48:51], a[176:179], v[184:187], v[48:51]\n\t"
    "v_mfma_f32_16x16x32_bf16 v[52:55], a[180:183], v[184:187], v[52:55]\n\t"
    "v_mfma_f32_16x16x32_bf16 v[56:59], a[184:187], v[184:187], v[56:59]\n\t"
    "v_mfma_f32_16x16x32_bf16 v[60:63], a[188:191], v[184:187], v[60:63]\n\t"
    "v_mfma_f32_16x16x32_bf16 v[48:51], a[160:163], v[200:203], v[48:51]\n\t"
    "v_mfma_f32_16x16x32_bf16 v[52:55], a[164:167], v[200:203], v[52:55]\n\t"
    "ds_write_b128 v248, v[32:35] offset:32768\n\t"
    "v_mfma_f32_16x16x32_bf16 v[56:59], a[168:171], v[200:203], v[56:59]\n\t"
    "v_mfma_f32_16x16x32_bf16 v[60:63], a[172:175], v[200:203], v[60:63]\n\t"
    "v_mfma_f32_16x16x32_bf16 v[48:51], a[224:227], v[216:219], v[48:51]\n\t"
    "ds_write_b128 v248, v[36:39] offset:33792\n\t"
    "v_mfma_f32_16x16x32_bf16 v[52:55], a[228:231], v[216:219], v[52:55]\n\t"
    "v_mfma_f32_16x16x32_bf16 v[56:59], a[232:235], v[216:219], v[56:59]\n\t"
    "v_mfma_f32_16x16x32_bf16 v[60:63], a[236:239], v[216:219], v[60:63]\n\t"
    "s_waitcnt vmcnt(0)\n\t"
    "ds_write_b128 v248, v[40:43] offset:34816\n\t"
    "v_mfma_f32_16x16x32_bf16 v[48:51], a[192:195], v[232:235], v[48:51]\n\t"
    "v_mfma_f32_16x16x32_bf16 v[52:55], a[196:199], v[232:235], v[52:55]\n\t"
    "v_mfma_f32_16x16x32_bf16 v[56:59], a[200:203], v[232:235], v[56:59]\n\t"
    "ds_write_b128 v248, v[44:47] offset:35840\n\t"
    "v_mfma_f32_16x16x32_bf16 v[60:63], a[204:207], v[232:235], v[60:63]\n\t"
    "s_cmp_lg_u32 s64, 0\n\t"
    "s_waitcnt lgkmcnt(0)\n\t"
    "s_barrier\n\t"
    "buffer_store_dwordx2 v[250:251], v252, s[24:27], 0 offen offset:256 sc1\n\t"
    "ds_read_b128 v[108:111], v249\n\t"
    "ds_read_b128 v[124:127], v253 offset:16384\n\t"
    "ds_read_b128 v[140:143], v254 offset:32768\n\t"
    "ds_read_b128 v[112:115], v249 offset:1024\n\t"
    "ds_read_b128 v[128:131], v253 offset:17408\n\t"
    "ds_read_b128 v[144:147], v254 offset:33792\n\t"
    "ds_read_b128 v[116:119], v249 offset:2048\n\t"
    "ds_read_b128 v[132:135], v253 offset:18432\n\t"
    "ds_read_b128 v[148:151], v254 offset:34816\n\t"
    "ds_read_b128 v[120:123], v249 offset:3072\n\t"
    "ds_read_b128 v[136:139], v253 offset:19456\n\t"
    "ds_read_b128 v[152:155], v254 offset:35840\n\t"
    "s_waitcnt lgkmcnt(10)\n\t"
    "v_pk_add_f32 v[108:109], v[108:109], v[124:125]\n\t"
    "s_waitcnt lgkmcnt(9)\n\t"
    "v_pk_add_f32 v[48:49], v[48:49], v[140:141]\n\t"
    "v_pk_add_f32 v[110:111], v[110:111], v[126:127]\n\t"
    "v_pk_add_f32 v[50:51], v[50:51], v[142:143]\n\t"
    "v_pk_add_f32 v[48:49], v[48:49], v[108:109]\n\t"
    "v_pk_add_f32 v[50:51], v[50:51], v[110:111]\n\t"
    "v_cvt_pk_bf16_f32 v48, v48, v49\n\t"
    "v_cvt_pk_bf16_f32 v49, v50, v51\n\t"
    "s_waitcnt lgkmcnt(7)\n\t"
    "v_pk_add_f32 v[112:113], v[112:113], v[128:129]\n\t"
    "s_waitcnt lgkmcnt(6)\n\t"
    "v_pk_add_f32 v[52:53], v[52:53], v[144:145]\n\t"
    "v_pk_add_f32 v[114:115], v[114:115], v[130:131]\n\t"
    "v_pk_add_f32 v[54:55], v[54:55], v[146:147]\n\t"
    "v_pk_add_f32 v[52:53], v[52:53], v[112:113]\n\t"
    "v_pk_add_f32 v[54:55], v[54:55], v[114:115]\n\t"
    "v_cvt_pk_bf16_f32 v50, v52, v53\n\t"
    "v_cvt_pk_bf16_f32 v51, v54, v55\n\t"
    "s_nop 0\n\t"
    "v_permlane16_swap_b32 v48, v50\n\t"
    "v_permlane16_swap_b32 v49, v51\n\t"
    "buffer_store_dwordx4 v[48:51], v245, s[56:59], 0 offen sc1\n\t"
    "s_waitcnt lgkmcnt(4)\n\t"
    "v_pk_add_f32 v[116:117], v[116:117], v[132:133]\n\t"
    "s_waitcnt lgkmcnt(3)\n\t"
    "v_pk_add_f32 v[56:57], v[56:57], v[148:149]\n\t"
    "v_pk_add_f32 v[118:119], v[118:119], v[134:135]\n\t"
    "v_pk_add_f32 v[58:59], v[58:59], v[150:151]\n\t"
    "v_pk_add_f32 v[56:57], v[56:57], v[116:117]\n\t"
    "v_pk_add_f32 v[58:59], v[58:59], v[118:119]\n\t"
    "v_cvt_pk_bf16_f32 v56, v56, v57\n\t"
    "v_cvt_pk_bf16_f32 v57, v58, v59\n\t"
    "s_waitcnt lgkmcnt(1)\n\t"
    "v_pk_add_f32 v[120:121], v[120:121], v[136:137]\n\t"
    "s_waitcnt lgkmcnt(0)\n\t"
    "v_pk_add_f32 v[60:61], v[60:61], v[152:153]\n\t"
    "v_pk_add_f32 v[122:123], v[122:123], v[138:139]\n\t"
    "v_pk_add_f32 v[62:63], v[62:63], v[154:155]\n\t"
    "v_pk_add_f32 v[60:61], v[60:61], v[120:121]\n\t"
    "v_pk_add_f32 v[62:63], v[62:63], v[122:123]\n\t"
    "v_cvt_pk_bf16_f32 v58, v60, v61\n\t"
    "v_cvt_pk_bf16_f32 v59, v62, v63\n\t"
    "s_nop 0\n\t"
    "v_permlane16_swap_b32 v56, v58\n\t"
    "v_permlane16_swap_b32 v57, v59\n\t"
    "buffer_store_dwordx4 v[56:59], v245, s[56:59], 0 offen offset:64 sc1\n\t"
    "s_cbranch_scc1 Lend%=\n\t"
    "s_nop -1\n\t"
    "s_branch Lend%=\n\t"
    ".p2align 12\n\t"
    "Lc10%=:\n\t"
    "s_setpc_b64 s[22:23]\n\t"
    "Lcom%=:\n\t"
    "s_cbranch_scc1 Lalt%=\n\t"
    "s_waitcnt lgkmcnt(0)\n\t"
    "s_barrier\n\t"
    "v_mfma_f32_16x16x32_bf16 v[108:111], a[0:3], v[196:199], v[156:159]\n\t"
    "ds_read_b64_tr_b16 a[32:33], v176 offset:8192\n\t"
    "ds_read_b64_tr_b16 a[34:35], v176 offset:12288\n\t"
    "v_mfma_f32_16x16x32_bf16 v[108:111], a[4:7], v[200:203], v[108:111]\n\t"
    "s_sub_u32 s75, s39, s70\n\t"
    "v_exp_f32 v127, v127\n\t"
    "v_mfma_f32_16x16x32_bf16 v[108:111], a[8:11], v[204:207], v[108:111]\n\t"
    "s_max_i32 s39, s75, 0\n\t"
    "v_exp_f32 v128, v128\n\t"
    "v_mfma_f32_16x16x32_bf16 v[108:111], a[12:15], v[208:211], v[108:111]\n\t"
    "s_add_u32 m0, s68, 0x1c000\n\t"
    "ds_read_b64_tr_b16 a[36:37], v177 offset:8192\n\t"
    "v_cvt_pk_bf16_f32 v139, v126, v127\n\t"
    "v_mfma_f32_16x16x32_bf16 v[112:115], a[0:3], v[212:215], v[160:163]\n\t"
    "s_sub_u32 s21, s39, s75\n\t"
    "buffer_load_dwordx4 v192, s[40:43], s67 offen nt lds\n\t"
    "v_mfma_f32_16x16x32_bf16 v[112:115], a[4:7], v[216:219], v[112:115]\n\t"
    "s_add_u32 s39, s39, s70\n\t"
    "v_exp_f32 v129, v129\n\t"
    "v_mfma_f32_16x16x32_bf16 v[112:115], a[8:11], v[220:223], v[112:115]\n\t"
    "s_max_i32 s75, s75, 0\n\t"
    "v_exp_f32 v130, v130\n\t"
    "v_mfma_f32_16x16x32_bf16 v[112:115], a[12:15], v[224:227], v[112:115]\n\t"
    "s_min_i32 s21, s21, s77\n\t"
    "ds_read_b64_tr_b16 a[38:39], v177 offset:12288\n\t"
    "v_cvt_pk_bf16_f32 v142, v128, v129\n\t"
    "v_mfma_f32_16x16x32_bf16 v[116:119], a[0:3], v[228:231], v[164:167]\n\t"
    "s_add_u32 s39, s39, s19\n\t"
    "v_exp_f32 v131, v131\n\t"
    "v_mfma_f32_16x16x32_bf16 v[116:119], a[4:7], v[232:235], v[116:119]\n\t"
    "s_add_u32 s18, s18, s21\n\t"
    "ds_read_b64_tr_b16 a[40:41], v178 offset:8192\n\t"
    "v_cvt_pk_bf16_f32 v143, v130, v131\n\t"
    "v_mfma_f32_16x16x32_bf16 v[116:119], a[8:11], v[236:239], v[116:119]\n\t"
    "s_sub_u32 s18, s18, 6\n\t"
    "ds_read_b64_tr_b16 a[42:43], v178 offset:12288\n\t"
    "ds_read_b64_tr_b16 a[44:45], v179 offset:8192\n\t"
    "v_mfma_f32_16x16x32_bf16 v[116:119], a[12:15], v[240:243], v[116:119]\n\t"
    "s_max_i32 s18, s18, 0\n\t"
    "v_exp_f32 v108, v108\n\t"
    "v_mfma_f32_16x16x32_bf16 v[120:123], a[16:19], v[196:199], v[156:159]\n\t"
    "ds_read_b64_tr_b16 a[46:47], v179 offset:12288\n\t"
    "ds_read_b64_tr_b16 a[48:49], v180 offset:8192\n\t"
    "v_mfma_f32_16x16x32_bf16 v[120:123], a[20:23], v[200:203], v[120:123]\n\t"
    "s_min_i32 s18, s18, 60\n\t"
    "v_exp_f32 v109, v109\n\t"
    "v_mfma_f32_16x16x32_bf16 v[120:123], a[24:27], v[204:207], v[120:123]\n\t"
    "ds_read_b64_tr_b16 a[50:51], v180 offset:12288\n\t"
    "ds_read_b64_tr_b16 a[52:53], v181 offset:8192\n\t"
    "v_mfma_f32_16x16x32_bf16 v[120:123], a[28:31], v[208:211], v[120:123]\n\t"
    "s_lshr_b32 s21, s18, 1\n\t"
    "v_exp_f32 v110, v110\n\t"
    "v_mfma_f32_16x16x32_bf16 v[124:127], a[16:19], v[212:215], v[160:163]\n\t"
    "ds_read_b64_tr_b16 a[54:55], v181 offset:12288\n\t"
    "v_cvt_pk_bf16_f32 v144, v108, v109\n\t"
    "v_mfma_f32_16x16x32_bf16 v[124:127], a[20:23], v[216:219], v[124:127]\n\t"
    "s_add_u32 s39, s39, s21\n\t"
    "v_exp_f32 v111, v111\n\t"
    "v_mfma_f32_16x16x32_bf16 v[124:127], a[24:27], v[220:223], v[124:127]\n\t"
    "ds_read_b64_tr_b16 a[56:57], v182 offset:8192\n\t"
    "ds_read_b64_tr_b16 a[58:59], v182 offset:12288\n\t"
    "v_mfma_f32_16x16x32_bf16 v[124:127], a[28:31], v[224:227], v[124:127]\n\t"
    "v_exp_f32 v112, v112\n\t"
    "v_mfma_f32_16x16x32_bf16 v[128:131], a[16:19], v[228:231], v[164:167]\n\t"
    "ds_read_b64_tr_b16 a[60:61], v183 offset:8192\n\t"
    "v_cvt_pk_bf16_f32 v145, v110, v111\n\t"
    "v_mfma_f32_16x16x32_bf16 v[128:131], a[20:23], v[232:235], v[128:131]\n\t"
    "ds_read_b64_tr_b16 a[62:63], v183 offset:12288\n\t"
    "ds_read_b128 a[0:3], v168\n\t"
    "v_mfma_f32_16x16x32_bf16 v[128:131], a[24:27], v[236:239], v[128:131]\n\t"
    "v_exp_f32 v113, v113\n\t"
    "v_mfma_f32_16x16x32_bf16 v[128:131], a[28:31], v[240:243], v[128:131]\n\t"
    "v_exp_f32 v114, v114\n\t"
    "v_mfma_f32_16x16x32_bf16 v[96:99], a[240:243], v[132:135], v[96:99]\n\t"
    "ds_read_b128 a[4:7], v169\n\t"
    "v_cvt_pk_bf16_f32 v148, v112, v113\n\t"
    "v_mfma_f32_16x16x32_bf16 v[96:99], a[244:247], v[136:139], v[96:99]\n\t"
    "Lw0%=:\n\t"
    "s_sub_u32 s75, s75, 2\n\t"
    "s_cbranch_scc0 Lw0%=\n\t"
    "v_mfma_f32_16x16x32_bf16 v[96:99], a[248:251], v[140:143], v[96:99]\n\t"
    "s_waitcnt lgkmcnt(2)\n\t"
    "v_exp_f32 v115, v115\n\t"
    "v_mfma_f32_16x16x32_bf16 v[64:67], a[32:35], v[140:143], v[64:67]\n\t"
    "buffer_load_dwordx4 v193, s[40:43], s67 offen offset:1024 nt lds\n\t"
    "v_mfma_f32_16x16x32_bf16 v[32:35], a[32:35], v[136:139], v[32:35]\n\t"
    "v_exp_f32 v116, v116\n\t"
    "v_mfma_f32_16x16x32_bf16 v[0:3], a[32:35], v[132:135], v[0:3]\n\t"
    "ds_read_b128 a[8:11], v170\n\t"
    "v_cvt_pk_bf16_f32 v149, v114, v115\n\t"
    "v_mfma_f32_16x16x32_bf16 v[4:7], a[36:39], v[132:135], v[4:7]\n\t"
    "v_exp_f32 v117, v117\n\t"
    "v_mfma_f32_16x16x32_bf16 v[36:39], a[36:39], v[136:139], v[36:39]\n\t"
    "v_exp_f32 v118, v118\n\t"
    "v_mfma_f32_16x16x32_bf16 v[68:71], a[36:39], v[140:143], v[68:71]\n\t"
    "ds_read_b128 a[12:15], v171\n\t"
    "v_cvt_pk_bf16_f32 v152, v116, v117\n\t"
    "v_mfma_f32_16x16x32_bf16 v[72:75], a[40:43], v[140:143], v[72:75]\n\t"
    "v_exp_f32 v119, v119\n\t"
    "v_mfma_f32_16x16x32_bf16 v[40:43], a[40:43], v[136:139], v[40:43]\n\t"
    "s_add_u32 m0, s68, 0x1e000\n\t"
    "v_exp_f32 v120, v120\n\t"
    "v_mfma_f32_16x16x32_bf16 v[8:11], a[40:43], v[132:135], v[8:11]\n\t"
    "ds_read_b128 a[16:19], v168 offset:4096\n\t"
    "v_cvt_pk_bf16_f32 v153, v118, v119\n\t"
    "v_mfma_f32_16x16x32_bf16 v[12:15], a[44:47], v[132:135], v[12:15]\n\t"
    "buffer_load_dwordx4 v194, s[44:47], s67 offen nt lds\n\t"
    "v_mfma_f32_16x16x32_bf16 v[44:47], a[44:47], v[136:139], v[44:47]\n\t"
    "v_exp_f32 v121, v121\n\t"
    "v_mfma_f32_16x16x32_bf16 v[76:79], a[44:47], v[140:143], v[76:79]\n\t"
    "s_memrealtime s[70:71]\n\t"
    "v_exp_f32 v122, v122\n\t"
    "v_mfma_f32_16x16x32_bf16 v[80:83], a[48:51], v[140:143], v[80:83]\n\t"
    "ds_read_b128 a[20:23], v169 offset:4096\n\t"
    "v_cvt_pk_bf16_f32 v146, v120, v121\n\t"
    "v_mfma_f32_16x16x32_bf16 v[48:51], a[48:51], v[136:139], v[48:51]\n\t"
    "v_exp_f32 v123, v123\n\t"
    "v_mfma_f32_16x16x32_bf16 v[16:19], a[48:51], v[132:135], v[16:19]\n\t"
    "v_exp_f32 v124, v124\n\t"
    "v_mfma_f32_16x16x32_bf16 v[20:23], a[52:55], v[132:135], v[20:23]\n\t"
    "ds_read_b128 a[24:27], v170 offset:4096\n\t"
    "v_cvt_pk_bf16_f32 v147, v122, v123\n\t"
    "v_mfma_f32_16x16x32_bf16 v[52:55], a[52:55], v[136:139], v[52:55]\n\t"
    "v_exp_f32 v125, v125\n\t"
    "v_mfma_f32_16x16x32_bf16 v[84:87], a[52:55], v[140:143], v[84:87]\n\t"
    "v_exp_f32 v126, v126\n\t"
    "v_mfma_f32_16x16x32_bf16 v[88:91], a[56:59], v[140:143], v[88:91]\n\t"
    "ds_read_b128 a[28:31], v171 offset:4096\n\t"
    "v_cvt_pk_bf16_f32 v150, v124, v125\n\t"
    "v_mfma_f32_16x16x32_bf16 v[56:59], a[56:59], v[136:139], v[56:59]\n\t"
    "buffer_load_dwordx4 v195, s[44:47], s67 offen offset:1024 nt lds\n\t"
    "v_mfma_f32_16x16x32_bf16 v[24:27], a[56:59], v[132:135], v[24:27]\n\t"
    "s_add_u32 s67, s67, 0x2000\n\t"
    "v_mfma_f32_16x16x32_bf16 v[28:31], a[60:63], v[132:135], v[28:31]\n\t"
    "v_mfma_f32_16x16x32_bf16 v[60:63], a[60:63], v[136:139], v[60:63]\n\t"
    "s_waitcnt vmcnt(12) lgkmcnt(0)\n\t"
    "v_mfma_f32_16x16x32_bf16 v[92:95], a[60:63], v[140:143], v[92:95]\n\t"
    "s_barrier\n\t"
    "v_mfma_f32_16x16x32_bf16 v[108:111], a[0:3], v[196:199], v[156:159]\n\t"
    "ds_read_b64_tr_b16 a[32:33], v176 offset:24576\n\t"
    "ds_read_b64_tr_b16 a[34:35], v176 offset:28672\n\t"
    "v_mfma_f32_16x16x32_bf16 v[108:111], a[4:7], v[200:203], v[108:111]\n\t"
    "s_sub_u32 s75, s39, s70\n\t"
    "v_exp_f32 v127, v127\n\t"
    "v_mfma_f32_16x16x32_bf16 v[108:111], a[8:11], v[204:207], v[108:111]\n\t"
    "s_max_i32 s39, s75, 0\n\t"
    "v_exp_f32 v128, v128\n\t"
    "v_mfma_f32_16x16x32_bf16 v[108:111], a[12:15], v[208:211], v[108:111]\n\t"
    "s_add_u32 m0, s68, 0\n\t"
    "ds_read_b64_tr_b16 a[36:37], v177 offset:24576\n\t"
    "v_cvt_pk_bf16_f32 v151, v126, v127\n\t"
    "v_mfma_f32_16x16x32_bf16 v[112:115], a[0:3], v[212:215], v[160:163]\n\t"
    "s_sub_u32 s21, s39, s75\n\t"
    "buffer_load_dwordx4 v192, s[40:43], s67 offen nt lds\n\t"
    "v_mfma_f32_16x16x32_bf16 v[112:115], a[4:7], v[216:219], v[112:115]\n\t"
    "s_add_u32 s39, s39, s70\n\t"
    "v_exp_f32 v129, v129\n\t"
    "v_mfma_f32_16x16x32_bf16 v[112:115], a[8:11], v[220:223], v[112:115]\n\t"
    "s_max_i32 s75, s75, 0\n\t"
    "v_exp_f32 v130, v130\n\t"
    "v_mfma_f32_16x16x32_bf16 v[112:115], a[12:15], v[224:227], v[112:115]\n\t"
    "s_min_i32 s21, s21, s77\n\t"
    "ds_read_b64_tr_b16 a[38:39], v177 offset:28672\n\t"
    "v_cvt_pk_bf16_f32 v154, v128, v129\n\t"
    "v_mfma_f32_16x16x32_bf16 v[116:119], a[0:3], v[228:231], v[164:167]\n\t"
    "s_add_u32 s39, s39, s19\n\t"
    "v_exp_f32 v131, v131\n\t"
    "v_mfma_f32_16x16x32_bf16 v[116:119], a[4:7], v[232:235], v[116:119]\n\t"
    "s_add_u32 s18, s18, s21\n\t"
    "ds_read_b64_tr_b16 a[40:41], v178 offset:24576\n\t"
    "v_cvt_pk_bf16_f32 v155, v130, v131\n\t"
    "v_mfma_f32_16x16x32_bf16 v[116:119], a[8:11], v[236:239], v[116:119]\n\t"
    "s_sub_u32 s18, s18, 6\n\t"
    "ds_read_b64_tr_b16 a[42:43], v178 offset:28672\n\t"
    "ds_read_b64_tr_b16 a[44:45], v179 offset:24576\n\t"
    "v_mfma_f32_16x16x32_bf16 v[116:119], a[12:15], v[240:243], v[116:119]\n\t"
    "s_max_i32 s18, s18, 0\n\t"
    "v_exp_f32 v108, v108\n\t"
    "v_mfma_f32_16x16x32_bf16 v[120:123], a[16:19], v[196:199], v[156:159]\n\t"
    "ds_read_b64_tr_b16 a[46:47], v179 offset:28672\n\t"
    "ds_read_b64_tr_b16 a[48:49], v180 offset:24576\n\t"
    "v_mfma_f32_16x16x32_bf16 v[120:123], a[20:23], v[200:203], v[120:123]\n\t"
    "s_min_i32 s18, s18, 60\n\t"
    "v_exp_f32 v109, v109\n\t"
    "v_mfma_f32_16x16x32_bf16 v[120:123], a[24:27], v[204:207], v[120:123]\n\t"
    "ds_read_b64_tr_b16 a[50:51], v180 offset:28672\n\t"
    "ds_read_b64_tr_b16 a[52:53], v181 offset:24576\n\t"
    "v_mfma_f32_16x16x32_bf16 v[120:123], a[28:31], v[208:211], v[120:123]\n\t"
    "s_lshr_b32 s21, s18, 1\n\t"
    "v_exp_f32 v110, v110\n\t"
    "v_mfma_f32_16x16x32_bf16 v[124:127], a[16:19], v[212:215], v[160:163]\n\t"
    "ds_read_b64_tr_b16 a[54:55], v181 offset:28672\n\t"
    "v_cvt_pk_bf16_f32 v132, v108, v109\n\t"
    "v_mfma_f32_16x16x32_bf16 v[124:127], a[20:23], v[216:219], v[124:127]\n\t"
    "s_add_u32 s39, s39, s21\n\t"
    "v_exp_f32 v111, v111\n\t"
    "v_mfma_f32_16x16x32_bf16 v[124:127], a[24:27], v[220:223], v[124:127]\n\t"
    "ds_read_b64_tr_b16 a[56:57], v182 offset:24576\n\t"
    "ds_read_b64_tr_b16 a[58:59], v182 offset:28672\n\t"
    "v_mfma_f32_16x16x32_bf16 v[124:127], a[28:31], v[224:227], v[124:127]\n\t"
    "v_exp_f32 v112, v112\n\t"
    "v_mfma_f32_16x16x32_bf16 v[128:131], a[16:19], v[228:231], v[164:167]\n\t"
    "ds_read_b64_tr_b16 a[60:61], v183 offset:24576\n\t"
    "v_cvt_pk_bf16_f32 v133, v110, v111\n\t"
    "v_mfma_f32_16x16x32_bf16 v[128:131], a[20:23], v[232:235], v[128:131]\n\t"
    "ds_read_b64_tr_b16 a[62:63], v183 offset:28672\n\t"
    "ds_read_b128 a[0:3], v168 offset:16384\n\t"
    "v_mfma_f32_16x16x32_bf16 v[128:131], a[24:27], v[236:239], v[128:131]\n\t"
    "v_exp_f32 v113, v113\n\t"
    "v_mfma_f32_16x16x32_bf16 v[128:131], a[28:31], v[240:243], v[128:131]\n\t"
    "v_exp_f32 v114, v114\n\t"
    "v_mfma_f32_16x16x32_bf16 v[96:99], a[240:243], v[144:147], v[96:99]\n\t"
    "ds_read_b128 a[4:7], v169 offset:16384\n\t"
    "v_cvt_pk_bf16_f32 v136, v112, v113\n\t"
    "v_mfma_f32_16x16x32_bf16 v[96:99], a[244:247], v[148:151], v[96:99]\n\t"
    "Lw1%=:\n\t"
    "s_sub_u32 s75, s75, 2\n\t"
    "s_cbranch_scc0 Lw1%=\n\t"
    "v_mfma_f32_16x16x32_bf16 v[96:99], a[248:251], v[152:155], v[96:99]\n\t"
    "s_waitcnt lgkmcnt(2)\n\t"
    "v_exp_f32 v115, v115\n\t"
    "v_mfma_f32_16x16x32_bf16 v[64:67], a[32:35], v[152:155], v[64:67]\n\t"
    "buffer_load_dwordx4 v193, s[40:43], s67 offen offset:1024 nt lds\n\t"
    "v_mfma_f32_16x16x32_bf16 v[32:35], a[32:35], v[148:151], v[32:35]\n\t"
    "v_exp_f32 v116, v116\n\t"
    "v_mfma_f32_16x16x32_bf16 v[0:3], a[32:35], v[144:147], v[0:3]\n\t"
    "ds_read_b128 a[8:11], v170 offset:16384\n\t"
    "v_cvt_pk_bf16_f32 v137, v114, v115\n\t"
    "v_mfma_f32_16x16x32_bf16 v[4:7], a[36:39], v[144:147], v[4:7]\n\t"
    "v_exp_f32 v117, v117\n\t"
    "v_mfma_f32_16x16x32_bf16 v[36:39], a[36:39], v[148:151], v[36:39]\n\t"
    "v_exp_f32 v118, v118\n\t"
    "v_mfma_f32_16x16x32_bf16 v[68:71], a[36:39], v[152:155], v[68:71]\n\t"
    "ds_read_b128 a[12:15], v171 offset:16384\n\t"
    "v_cvt_pk_bf16_f32 v140, v116, v117\n\t"
    "v_mfma_f32_16x16x32_bf16 v[72:75], a[40:43], v[152:155], v[72:75]\n\t"
    "v_exp_f32 v119, v119\n\t"
    "v_mfma_f32_16x16x32_bf16 v[40:43], a[40:43], v[148:151], v[40:43]\n\t"
    "s_add_u32 m0, s68, 0x2000\n\t"
    "v_exp_f32 v120, v120\n\t"
    "v_mfma_f32_16x16x32_bf16 v[8:11], a[40:43], v[144:147], v[8:11]\n\t"
    "ds_read_b128 a[16:19], v168 offset:20480\n\t"
    "v_cvt_pk_bf16_f32 v141, v118, v119\n\t"
    "v_mfma_f32_16x16x32_bf16 v[12:15], a[44:47], v[144:147], v[12:15]\n\t"
    "buffer_load_dwordx4 v194, s[44:47], s67 offen nt lds\n\t"
    "v_mfma_f32_16x16x32_bf16 v[44:47], a[44:47], v[148:151], v[44:47]\n\t"
    "v_exp_f32 v121, v121\n\t"
    "v_mfma_f32_16x16x32_bf16 v[76:79], a[44:47], v[152:155], v[76:79]\n\t"
    "s_memrealtime s[70:71]\n\t"
    "v_exp_f32 v122, v122\n\t"
    "v_mfma_f32_16x16x32_bf16 v[80:83], a[48:51], v[152:155], v[80:83]\n\t"
    "ds_read_b128 a[20:23], v169 offset:20480\n\t"
    "v_cvt_pk_bf16_f32 v134, v120, v121\n\t"
    "v_mfma_f32_16x16x32_bf16 v[48:51], a[48:51], v[148:151], v[48:51]\n\t"
    "v_exp_f32 v123, v123\n\t"
    "v_mfma_f32_16x16x32_bf16 v[16:19], a[48:51], v[144:147], v[16:19]\n\t"
    "v_exp_f32 v124, v124\n\t"
    "v_mfma_f32_16x16x32_bf16 v[20:23], a[52:55], v[144:147], v[20:23]\n\t"
    "ds_read_b128 a[24:27], v170 offset:20480\n\t"
    "v_cvt_pk_bf16_f32 v135, v122, v123\n\t"
    "v_mfma_f32_16x16x32_bf16 v[52:55], a[52:55], v[148:151], v[52:55]\n\t"
    "v_exp_f32 v125, v125\n\t"
    "v_mfma_f32_16x16x32_bf16 v[84:87], a[52:55], v[152:155], v[84:87]\n\t"
    "v_exp_f32 v126, v126\n\t"
    "v_mfma_f32_16x16x32_bf16 v[88:91], a[56:59], v[152:155], v[88:91]\n\t"
    "ds_read_b128 a[28:31], v171 offset:20480\n\t"
    "v_cvt_pk_bf16_f32 v138, v124, v125\n\t"
    "v_mfma_f32_16x16x32_bf16 v[56:59], a[56:59], v[148:151], v[56:59]\n\t"
    "buffer_load_dwordx4 v195, s[44:47], s67 offen offset:1024 nt lds\n\t"
    "v_mfma_f32_16x16x32_bf16 v[24:27], a[56:59], v[144:147], v[24:27]\n\t"
    "s_add_u32 s67, s67, 0x2000\n\t"
    "v_mfma_f32_16x16x32_bf16 v[28:31], a[60:63], v[144:147], v[28:31]\n\t"
    "v_mfma_f32_16x16x32_bf16 v[60:63], a[60:63], v[148:151], v[60:63]\n\t"
    "s_waitcnt vmcnt(12) lgkmcnt(0)\n\t"
    "v_mfma_f32_16x16x32_bf16 v[92:95], a[60:63], v[152:155], v[92:95]\n\t"
    "s_barrier\n\t"
    "v_mfma_f32_16x16x32_bf16 v[108:111], a[0:3], v[196:199], v[156:159]\n\t"
    "ds_read_b64_tr_b16 a[32:33], v176 offset:40960\n\t"
    "ds_read_b64_tr_b16 a[34:35], v176 offset:45056\n\t"
    "v_mfma_f32_16x16x32_bf16 v[108:111], a[4:7], v[200:203], v[108:111]\n\t"
    "s_sub_u32 s75, s39, s70\n\t"
    "v_exp_f32 v127, v127\n\t"
    "v_mfma_f32_16x16x32_bf16 v[108:111], a[8:11], v[204:207], v[108:111]\n\t"
    "s_max_i32 s39, s75, 0\n\t"
    "v_exp_f32 v128, v128\n\t"
    "v_mfma_f32_16x16x32_bf16 v[108:111], a[12:15], v[208:211], v[108:111]\n\t"
    "s_add_u32 m0, s68, 0x4000\n\t"
    "ds_read_b64_tr_b16 a[36:37], v177 offset:40960\n\t"
    "v_cvt_pk_bf16_f32 v139, v126, v127\n\t"
    "v_mfma_f32_16x16x32_bf16 v[112:115], a[0:3], v[212:215], v[160:163]\n\t"
    "s_sub_u32 s21, s39, s75\n\t"
    "buffer_load_dwordx4 v192, s[40:43], s67 offen nt lds\n\t"
    "v_mfma_f32_16x16x32_bf16 v[112:115], a[4:7], v[216:219], v[112:115]\n\t"
    "s_add_u32 s39, s39, s70\n\t"
    "v_exp_f32 v129, v129\n\t"
    "v_mfma_f32_16x16x32_bf16 v[112:115], a[8:11], v[220:223], v[112:115]\n\t"
    "s_max_i32 s75, s75, 0\n\t"
    "v_exp_f32 v130, v130\n\t"
    "v_mfma_f32_16x16x32_bf16 v[112:115], a[12:15], v[224:227], v[112:115]\n\t"
    "s_min_i32 s21, s21, s77\n\t"
    "ds_read_b64_tr_b16 a[38:39], v177 offset:45056\n\t"
    "v_cvt_pk_bf16_f32 v142, v128, v129\n\t"
    "v_mfma_f32_16x16x32_bf16 v[116:119], a[0:3], v[228:231], v[164:167]\n\t"
    "s_add_u32 s39, s39, s19\n\t"
    "v_exp_f32 v131, v131\n\t"
    "v_mfma_f32_16x16x32_bf16 v[116:119], a[4:7], v[232:235], v[116:119]\n\t"
    "s_add_u32 s18, s18, s21\n\t"
    "ds_read_b64_tr_b16 a[40:41], v178 offset:40960\n\t"
    "v_cvt_pk_bf16_f32 v143, v130, v131\n\t"
    "v_mfma_f32_16x16x32_bf16 v[116:119], a[8:11], v[236:239], v[116:119]\n\t"
    "s_sub_u32 s18, s18, 6\n\t"
    "ds_read_b64_tr_b16 a[42:43], v178 offset:45056\n\t"
    "ds_read_b64_tr_b16 a[44:45], v179 offset:40960\n\t"
    "v_mfma_f32_16x16x32_bf16 v[116:119], a[12:15], v[240:243], v[116:119]\n\t"
    "s_max_i32 s18, s18, 0\n\t"
    "v_exp_f32 v108, v108\n\t"
    "v_mfma_f32_16x16x32_bf16 v[120:123], a[16:19], v[196:199], v[156:159]\n\t"
    "ds_read_b64_tr_b16 a[46:47], v179 offset:45056\n\t"
    "ds_read_b64_tr_b16 a[48:49], v180 offset:40960\n\t"
    "v_mfma_f32_16x16x32_bf16 v[120:123], a[20:23], v[200:203], v[120:123]\n\t"
    "s_min_i32 s18, s18, 60\n\t"
    "v_exp_f32 v109, v109\n\t"
    "v_mfma_f32_16x16x32_bf16 v[120:123], a[24:27], v[204:207], v[120:123]\n\t"
    "ds_read_b64_tr_b16 a[50:51], v180 offset:45056\n\t"
    "ds_read_b64_tr_b16 a[52:53], v181 offset:40960\n\t"
    "v_mfma_f32_16x16x32_bf16 v[120:123], a[28:31], v[208:211], v[120:123]\n\t"
    "s_lshr_b32 s21, s18, 1\n\t"
    "v_exp_f32 v110, v110\n\t"
    "v_mfma_f32_16x16x32_bf16 v[124:127], a[16:19], v[212:215], v[160:163]\n\t"
    "ds_read_b64_tr_b16 a[54:55], v181 offset:45056\n\t"
    "v_cvt_pk_bf16_f32 v144, v108, v109\n\t"
    "v_mfma_f32_16x16x32_bf16 v[124:127], a[20:23], v[216:219], v[124:127]\n\t"
    "s_add_u32 s39, s39, s21\n\t"
    "v_exp_f32 v111, v111\n\t"
    "v_mfma_f32_16x16x32_bf16 v[124:127], a[24:27], v[220:223], v[124:127]\n\t"
    "ds_read_b64_tr_b16 a[56:57], v182 offset:40960\n\t"
    "ds_read_b64_tr_b16 a[58:59], v182 offset:45056\n\t"
    "v_mfma_f32_16x16x32_bf16 v[124:127], a[28:31], v[224:227], v[124:127]\n\t"
    "v_exp_f32 v112, v112\n\t"
    "v_mfma_f32_16x16x32_bf16 v[128:131], a[16:19], v[228:231], v[164:167]\n\t"
    "ds_read_b64_tr_b16 a[60:61], v183 offset:40960\n\t"
    "v_cvt_pk_bf16_f32 v145, v110, v111\n\t"
    "v_mfma_f32_16x16x32_bf16 v[128:131], a[20:23], v[232:235], v[128:131]\n\t"
    "ds_read_b64_tr_b16 a[62:63], v183 offset:45056\n\t"
    "ds_read_b128 a[0:3], v168 offset:32768\n\t"
    "v_mfma_f32_16x16x32_bf16 v[128:131], a[24:27], v[236:239], v[128:131]\n\t"
    "v_exp_f32 v113, v113\n\t"
    "v_mfma_f32_16x16x32_bf16 v[128:131], a[28:31], v[240:243], v[128:131]\n\t"
    "v_exp_f32 v114, v114\n\t"
    "v_mfma_f32_16x16x32_bf16 v[96:99], a[240:243], v[132:135], v[96:99]\n\t"
    "ds_read_b128 a[4:7], v169 offset:32768\n\t"
    "v_cvt_pk_bf16_f32 v148, v112, v113\n\t"
    "v_mfma_f32_16x16x32_bf16 v[96:99], a[244:247], v[136:139], v[96:99]\n\t"
    "Lw2%=:\n\t"
    "s_sub_u32 s75, s75, 2\n\t"
    "s_cbranch_scc0 Lw2%=\n\t"
    "v_mfma_f32_16x16x32_bf16 v[96:99], a[248:251], v[140:143], v[96:99]\n\t"
    "s_waitcnt lgkmcnt(2)\n\t"
    "v_exp_f32 v115, v115\n\t"
    "v_mfma_f32_16x16x32_bf16 v[64:67], a[32:35], v[140:143], v[64:67]\n\t"
    "buffer_load_dwordx4 v193, s[40:43], s67 offen offset:1024 nt lds\n\t"
    "v_mfma_f32_16x16x32_bf16 v[32:35], a[32:35], v[136:139], v[32:35]\n\t"
    "v_exp_f32 v116, v116\n\t"
    "v_mfma_f32_16x16x32_bf16 v[0:3], a[32:35], v[132:135], v[0:3]\n\t"
    "ds_read_b128 a[8:11], v170 offset:32768\n\t"
    "v_cvt_pk_bf16_f32 v149, v114, v115\n\t"
    "v_mfma_f32_16x16x32_bf16 v[4:7], a[36:39], v[132:135], v[4:7]\n\t"
    "v_exp_f32 v117, v117\n\t"
    "v_mfma_f32_16x16x32_bf16 v[36:39], a[36:39], v[136:139], v[36:39]\n\t"
    "v_exp_f32 v118, v118\n\t"
    "v_mfma_f32_16x16x32_bf16 v[68:71], a[36:39], v[140:143], v[68:71]\n\t"
    "ds_read_b128 a[12:15], v171 offset:32768\n\t"
    "v_cvt_pk_bf16_f32 v152, v116, v117\n\t"
    "v_mfma_f32_16x16x32_bf16 v[72:75], a[40:43], v[140:143], v[72:75]\n\t"
    "v_exp_f32 v119, v119\n\t"
    "v_mfma_f32_16x16x32_bf16 v[40:43], a[40:43], v[136:139], v[40:43]\n\t"
    "s_add_u32 m0, s68, 0x6000\n\t"
    "v_exp_f32 v120, v120\n\t"
    "v_mfma_f32_16x16x32_bf16 v[8:11], a[40:43], v[132:135], v[8:11]\n\t"
    "ds_read_b128 a[16:19], v168 offset:36864\n\t"
    "v_cvt_pk_bf16_f32 v153, v118, v119\n\t"
    "v_mfma_f32_16x16x32_bf16 v[12:15], a[44:47], v[132:135], v[12:15]\n\t"
    "buffer_load_dwordx4 v194, s[44:47], s67 offen nt lds\n\t"
    "v_mfma_f32_16x16x32_bf16 v[44:47], a[44:47], v[136:139], v[44:47]\n\t"
    "v_exp_f32 v121, v121\n\t"
    "v_mfma_f32_16x16x32_bf16 v[76:79], a[44:47], v[140:143], v[76:79]\n\t"
    "s_memrealtime s[70:71]\n\t"
    "v_exp_f32 v122, v122\n\t"
    "v_mfma_f32_16x16x32_bf16 v[80:83], a[48:51], v[140:143], v[80:83]\n\t"
    "ds_read_b128 a[20:23], v169 offset:36864\n\t"
    "v_cvt_pk_bf16_f32 v146, v120, v121\n\t"
    "v_mfma_f32_16x16x32_bf16 v[48:51], a[48:51], v[136:139], v[48:51]\n\t"
    "v_exp_f32 v123, v123\n\t"
    "v_mfma_f32_16x16x32_bf16 v[16:19], a[48:51], v[132:135], v[16:19]\n\t"
    "v_exp_f32 v124, v124\n\t"
    "v_mfma_f32_16x16x32_bf16 v[20:23], a[52:55], v[132:135], v[20:23]\n\t"
    "ds_read_b128 a[24:27], v170 offset:36864\n\t"
    "v_cvt_pk_bf16_f32 v147, v122, v123\n\t"
    "v_mfma_f32_16x16x32_bf16 v[52:55], a[52:55], v[136:139], v[52:55]\n\t"
    "v_exp_f32 v125, v125\n\t"
    "v_mfma_f32_16x16x32_bf16 v[84:87], a[52:55], v[140:143], v[84:87]\n\t"
    "v_exp_f32 v126, v126\n\t"
    "v_mfma_f32_16x16x32_bf16 v[88:91], a[56:59], v[140:143], v[88:91]\n\t"
    "ds_read_b128 a[28:31], v171 offset:36864\n\t"
    "v_cvt_pk_bf16_f32 v150, v124, v125\n\t"
    "v_mfma_f32_16x16x32_bf16 v[56:59], a[56:59], v[136:139], v[56:59]\n\t"
    "buffer_load_dwordx4 v195, s[44:47], s67 offen offset:1024 nt lds\n\t"
    "v_mfma_f32_16x16x32_bf16 v[24:27], a[56:59], v[132:135], v[24:27]\n\t"
    "s_add_u32 s67, s67, 0x2000\n\t"
    "v_mfma_f32_16x16x32_bf16 v[28:31], a[60:63], v[132:135], v[28:31]\n\t"
    "v_mfma_f32_16x16x32_bf16 v[60:63], a[60:63], v[136:139], v[60:63]\n\t"
    "s_waitcnt vmcnt(12) lgkmcnt(0)\n\t"
    "v_mfma_f32_16x16x32_bf16 v[92:95], a[60:63], v[140:143], v[92:95]\n\t"
    "s_barrier\n\t"
    "v_mfma_f32_16x16x32_bf16 v[108:111], a[0:3], v[196:199], v[156:159]\n\t"
    "ds_read_b64_tr_b16 a[32:33], v176 offset:57344\n\t"
    "ds_read_b64_tr_b16 a[34:35], v176 offset:61440\n\t"
    "v_mfma_f32_16x16x32_bf16 v[108:111], a[4:7], v[200:203], v[108:111]\n\t"
    "s_sub_u32 s75, s39, s70\n\t"
    "v_exp_f32 v127, v127\n\t"
    "v_mfma_f32_16x16x32_bf16 v[108:111], a[8:11], v[204:207], v[108:111]\n\t"
    "s_max_i32 s39, s75, 0\n\t"
    "v_exp_f32 v128, v128\n\t"
    "v_mfma_f32_16x16x32_bf16 v[108:111], a[12:15], v[208:211], v[108:111]\n\t"
    "s_add_u32 m0, s68, 0x8000\n\t"
    "ds_read_b64_tr_b16 a[36:37], v177 offset:57344\n\t"
    "v_cvt_pk_bf16_f32 v151, v126, v127\n\t"
    "v_mfma_f32_16x16x32_bf16 v[112:115], a[0:3], v[212:215], v[160:163]\n\t"
    "s_sub_u32 s21, s39, s75\n\t"
    "buffer_load_dwordx4 v192, s[40:43], s67 offen nt lds\n\t"
    "v_mfma_f32_16x16x32_bf16 v[112:115], a[4:7], v[216:219], v[112:115]\n\t"
    "s_add_u32 s39, s39, s70\n\t"
    "v_exp_f32 v129, v129\n\t"
    "v_mfma_f32_16x16x32_bf16 v[112:115], a[8:11], v[220:223], v[112:115]\n\t"
    "s_max_i32 s75, s75, 0\n\t"
    "v_exp_f32 v130, v130\n\t"
    "v_mfma_f32_16x16x32_bf16 v[112:115], a[12:15], v[224:227], v[112:115]\n\t"
    "s_min_i32 s21, s21, s77\n\t"
    "ds_read_b64_tr_b16 a[38:39], v177 offset:61440\n\t"
    "v_cvt_pk_bf16_f32 v154, v128, v129\n\t"
    "v_mfma_f32_16x16x32_bf16 v[116:119], a[0:3], v[228:231], v[164:167]\n\t"
    "s_add_u32 s39, s39, s19\n\t"
    "v_exp_f32 v131, v131\n\t"
    "v_mfma_f32_16x16x32_bf16 v[116:119], a[4:7], v[232:235], v[116:119]\n\t"
    "s_add_u32 s18, s18, s21\n\t"
    "ds_read_b64_tr_b16 a[40:41], v178 offset:57344\n\t"
    "v_cvt_pk_bf16_f32 v155, v130, v131\n\t"
    "v_mfma_f32_16x16x32_bf16 v[116:119], a[8:11], v[236:239], v[116:119]\n\t"
    "s_sub_u32 s18, s18, 6\n\t"
    "ds_read_b64_tr_b16 a[42:43], v178 offset:61440\n\t"
    "ds_read_b64_tr_b16 a[44:45], v179 offset:57344\n\t"
    "v_mfma_f32_16x16x32_bf16 v[116:119], a[12:15], v[240:243], v[116:119]\n\t"
    "s_max_i32 s18, s18, 0\n\t"
    "v_exp_f32 v108, v108\n\t"
    "v_mfma_f32_16x16x32_bf16 v[120:123], a[16:19], v[196:199], v[156:159]\n\t"
    "ds_read_b64_tr_b16 a[46:47], v179 offset:61440\n\t"
    "ds_read_b64_tr_b16 a[48:49], v180 offset:57344\n\t"
    "v_mfma_f32_16x16x32_bf16 v[120:123], a[20:23], v[200:203], v[120:123]\n\t"
    "s_min_i32 s18, s18, 60\n\t"
    "v_exp_f32 v109, v109\n\t"
    "v_mfma_f32_16x16x32_bf16 v[120:123], a[24:27], v[204:207], v[120:123]\n\t"
    "ds_read_b64_tr_b16 a[50:51], v180 offset:61440\n\t"
    "ds_read_b64_tr_b16 a[52:53], v181 offset:57344\n\t"
    "v_mfma_f32_16x16x32_bf16 v[120:123], a[28:31], v[208:211], v[120:123]\n\t"
    "s_lshr_b32 s21, s18, 1\n\t"
    "v_exp_f32 v110, v110\n\t"
    "v_mfma_f32_16x16x32_bf16 v[124:127], a[16:19], v[212:215], v[160:163]\n\t"
    "ds_read_b64_tr_b16 a[54:55], v181 offset:61440\n\t"
    "v_cvt_pk_bf16_f32 v132, v108, v109\n\t"
    "v_mfma_f32_16x16x32_bf16 v[124:127], a[20:23], v[216:219], v[124:127]\n\t"
    "s_add_u32 s39, s39, s21\n\t"
    "v_exp_f32 v111, v111\n\t"
    "v_mfma_f32_16x16x32_bf16 v[124:127], a[24:27], v[220:223], v[124:127]\n\t"
    "ds_read_b64_tr_b16 a[56:57], v182 offset:57344\n\t"
    "ds_read_b64_tr_b16 a[58:59], v182 offset:61440\n\t"
    "v_mfma_f32_16x16x32_bf16 v[124:127], a[28:31], v[224:227], v[124:127]\n\t"
    "v_exp_f32 v112, v112\n\t"
    "v_mfma_f32_16x16x32_bf16 v[128:131], a[16:19], v[228:231], v[164:167]\n\t"
    "ds_read_b64_tr_b16 a[60:61], v183 offset:57344\n\t"
    "v_cvt_pk_bf16_f32 v133, v110, v111\n\t"
    "v_mfma_f32_16x16x32_bf16 v[128:131], a[20:23], v[232:235], v[128:131]\n\t"
    "ds_read_b64_tr_b16 a[62:63], v183 offset:61440\n\t"
    "ds_read_b128 a[0:3], v168 offset:49152\n\t"
    "v_mfma_f32_16x16x32_bf16 v[128:131], a[24:27], v[236:239], v[128:131]\n\t"
    "v_exp_f32 v113, v113\n\t"
    "v_mfma_f32_16x16x32_bf16 v[128:131], a[28:31], v[240:243], v[128:131]\n\t"
    "v_exp_f32 v114, v114\n\t"
    "v_mfma_f32_16x16x32_bf16 v[96:99], a[240:243], v[144:147], v[96:99]\n\t"
    "ds_read_b128 a[4:7], v169 offset:49152\n\t"
    "v_cvt_pk_bf16_f32 v136, v112, v113\n\t"
    "v_mfma_f32_16x16x32_bf16 v[96:99], a[244:247], v[148:151], v[96:99]\n\t"
    "Lw3%=:\n\t"
    "s_sub_u32 s75, s75, 2\n\t"
    "s_cbranch_scc0 Lw3%=\n\t"
    "v_mfma_f32_16x16x32_bf16 v[96:99], a[248:251], v[152:155], v[96:99]\n\t"
    "s_waitcnt lgkmcnt(2)\n\t"
    "v_exp_f32 v115, v115\n\t"
    "v_mfma_f32_16x16x32_bf16 v[64:67], a[32:35], v[152:155], v[64:67]\n\t"
    "buffer_load_dwordx4 v193, s[40:43], s67 offen offset:1024 nt lds\n\t"
    "v_mfma_f32_16x16x32_bf16 v[32:35], a[32:35], v[148:151], v[32:35]\n\t"
    "v_exp_f32 v116, v116\n\t"
    "v_mfma_f32_16x16x32_bf16 v[0:3], a[32:35], v[144:147], v[0:3]\n\t"
    "ds_read_b128 a[8:11], v170 offset:49152\n\t"
    "v_cvt_pk_bf16_f32 v137, v114, v115\n\t"
    "v_mfma_f32_16x16x32_bf16 v[4:7], a[36:39], v[144:147], v[4:7]\n\t"
    "v_exp_f32 v117, v117\n\t"
    "v_mfma_f32_16x16x32_bf16 v[36:39], a[36:39], v[148:151], v[36:39]\n\t"
    "v_exp_f32 v118, v118\n\t"
    "v_mfma_f32_16x16x32_bf16 v[68:71], a[36:39], v[152:155], v[68:71]\n\t"
    "ds_read_b128 a[12:15], v171 offset:49152\n\t"
    "v_cvt_pk_bf16_f32 v140, v116, v117\n\t"
    "v_mfma_f32_16x16x32_bf16 v[72:75], a[40:43], v[152:155], v[72:75]\n\t"
    "v_exp_f32 v119, v119\n\t"
    "v_mfma_f32_16x16x32_bf16 v[40:43], a[40:43], v[148:151], v[40:43]\n\t"
    "s_add_u32 m0, s68, 0xa000\n\t"
    "v_exp_f32 v120, v120\n\t"
    "v_mfma_f32_16x16x32_bf16 v[8:11], a[40:43], v[144:147], v[8:11]\n\t"
    "ds_read_b128 a[16:19], v168 offset:53248\n\t"
    "v_cvt_pk_bf16_f32 v141, v118, v119\n\t"
    "v_mfma_f32_16x16x32_bf16 v[12:15], a[44:47], v[144:147], v[12:15]\n\t"
    "buffer_load_dwordx4 v194, s[44:47], s67 offen nt lds\n\t"
    "v_mfma_f32_16x16x32_bf16 v[44:47], a[44:47], v[148:151], v[44:47]\n\t"
    "v_exp_f32 v121, v121\n\t"
    "v_mfma_f32_16x16x32_bf16 v[76:79], a[44:47], v[152:155], v[76:79]\n\t"
    "s_memrealtime s[70:71]\n\t"
    "v_exp_f32 v122, v122\n\t"
    "v_mfma_f32_16x16x32_bf16 v[80:83], a[48:51], v[152:155], v[80:83]\n\t"
    "ds_read_b128 a[20:23], v169 offset:53248\n\t"
    "v_cvt_pk_bf16_f32 v134, v120, v121\n\t"
    "v_mfma_f32_16x16x32_bf16 v[48:51], a[48:51], v[148:151], v[48:51]\n\t"
    "v_exp_f32 v123, v123\n\t"
    "v_mfma_f32_16x16x32_bf16 v[16:19], a[48:51], v[144:147], v[16:19]\n\t"
    "v_exp_f32 v124, v124\n\t"
    "v_mfma_f32_16x16x32_bf16 v[20:23], a[52:55], v[144:147], v[20:23]\n\t"
    "ds_read_b128 a[24:27], v170 offset:53248\n\t"
    "v_cvt_pk_bf16_f32 v135, v122, v123\n\t"
    "v_mfma_f32_16x16x32_bf16 v[52:55], a[52:55], v[148:151], v[52:55]\n\t"
    "v_exp_f32 v125, v125\n\t"
    "v_mfma_f32_16x16x32_bf16 v[84:87], a[52:55], v[152:155], v[84:87]\n\t"
    "v_exp_f32 v126, v126\n\t"
    "v_mfma_f32_16x16x32_bf16 v[88:91], a[56:59], v[152:155], v[88:91]\n\t"
    "ds_read_b128 a[28:31], v171 offset:53248\n\t"
    "v_cvt_pk_bf16_f32 v138, v124, v125\n\t"
    "v_mfma_f32_16x16x32_bf16 v[56:59], a[56:59], v[148:151], v[56:59]\n\t"
    "buffer_load_dwordx4 v195, s[44:47], s67 offen offset:1024 nt lds\n\t"
    "v_mfma_f32_16x16x32_bf16 v[24:27], a[56:59], v[144:147], v[24:27]\n\t"
    "s_add_u32 s67, s67, 0x2000\n\t"
    "v_mfma_f32_16x16x32_bf16 v[28:31], a[60:63], v[144:147], v[28:31]\n\t"
    "v_mfma_f32_16x16x32_bf16 v[60:63], a[60:63], v[148:151], v[60:63]\n\t"
    "s_waitcnt vmcnt(12) lgkmcnt(0)\n\t"
    "v_mfma_f32_16x16x32_bf16 v[92:95], a[60:63], v[152:155], v[92:95]\n\t"
    "s_barrier\n\t"
    "v_mfma_f32_16x16x32_bf16 v[108:111], a[0:3], v[196:199], v[156:159]\n\t"
    "ds_read_b64_tr_b16 a[32:33], v184 offset:8192\n\t"
    "ds_read_b64_tr_b16 a[34:35], v184 offset:12288\n\t"
    "v_mfma_f32_16x16x32_bf16 v[108:111], a[4:7], v[200:203], v[108:111]\n\t"
    "s_sub_u32 s75, s39, s70\n\t"
    "v_exp_f32 v127, v127\n\t"
    "v_mfma_f32_16x16x32_bf16 v[108:111], a[8:11], v[204:207], v[108:111]\n\t"
    "s_max_i32 s39, s75, 0\n\t"
    "v_exp_f32 v128, v128\n\t"
    "v_mfma_f32_16x16x32_bf16 v[108:111], a[12:15], v[208:211], v[108:111]\n\t"
    "s_add_u32 m0, s68, 0xc000\n\t"
    "ds_read_b64_tr_b16 a[36:37], v185 offset:8192\n\t"
    "v_cvt_pk_bf16_f32 v139, v126, v127\n\t"
    "v_mfma_f32_16x16x32_bf16 v[112:115], a[0:3], v[212:215], v[160:163]\n\t"
    "s_sub_u32 s21, s39, s75\n\t"
    "buffer_load_dwordx4 v192, s[40:43], s67 offen nt lds\n\t"
    "v_mfma_f32_16x16x32_bf16 v[112:115], a[4:7], v[216:219], v[112:115]\n\t"
    "s_add_u32 s39, s39, s70\n\t"
    "v_exp_f32 v129, v129\n\t"
    "v_mfma_f32_16x16x32_bf16 v[112:115], a[8:11], v[220:223], v[112:115]\n\t"
    "s_max_i32 s75, s75, 0\n\t"
    "v_exp_f32 v130, v130\n\t"
    "v_mfma_f32_16x16x32_bf16 v[112:115], a[12:15], v[224:227], v[112:115]\n\t"
    "s_min_i32 s21, s21, s77\n\t"
    "ds_read_b64_tr_b16 a[38:39], v185 offset:12288\n\t"
    "v_cvt_pk_bf16_f32 v142, v128, v129\n\t"
    "v_mfma_f32_16x16x32_bf16 v[116:119], a[0:3], v[228:231], v[164:167]\n\t"
    "s_add_u32 s39, s39, s19\n\t"
    "v_exp_f32 v131, v131\n\t"
    "v_mfma_f32_16x16x32_bf16 v[116:119], a[4:7], v[232:235], v[116:119]\n\t"
    "s_add_u32 s18, s18, s21\n\t"
    "ds_read_b64_tr_b16 a[40:41], v186 offset:8192\n\t"
    "v_cvt_pk_bf16_f32 v143, v130, v131\n\t"
    "v_mfma_f32_16x16x32_bf16 v[116:119], a[8:11], v[236:239], v[116:119]\n\t"
    "s_sub_u32 s18, s18, 6\n\t"
    "ds_read_b64_tr_b16 a[42:43], v186 offset:12288\n\t"
    "ds_read_b64_tr_b16 a[44:45], v187 offset:8192\n\t"
    "v_mfma_f32_16x16x32_bf16 v[116:119], a[12:15], v[240:243], v[116:119]\n\t"
    "s_max_i32 s18, s18, 0\n\t"
    "v_exp_f32 v108, v108\n\t"
    "v_mfma_f32_16x16x32_bf16 v[120:123], a[16:19], v[196:199], v[156:159]\n\t"
    "ds_read_b64_tr_b16 a[46:47], v187 offset:12288\n\t"
    "ds_read_b64_tr_b16 a[48:49], v188 offset:8192\n\t"
    "v_mfma_f32_16x16x32_bf16 v[120:123], a[20:23], v[200:203], v[120:123]\n\t"
    "s_min_i32 s18, s18, 60\n\t"
    "v_exp_f32 v109, v109\n\t"
    "v_mfma_f32_16x16x32_bf16 v[120:123], a[24:27], v[204:207], v[120:123]\n\t"
    "ds_read_b64_tr_b16 a[50:51], v188 offset:12288\n\t"
    "ds_read_b64_tr_b16 a[52:53], v189 offset:8192\n\t"
    "v_mfma_f32_16x16x32_bf16 v[120:123], a[28:31], v[208:211], v[120:123]\n\t"
    "s_lshr_b32 s21, s18, 1\n\t"
    "v_exp_f32 v110, v110\n\t"
    "v_mfma_f32_16x16x32_bf16 v[124:127], a[16:19], v[212:215], v[160:163]\n\t"
    "ds_read_b64_tr_b16 a[54:55], v189 offset:12288\n\t"
    "v_cvt_pk_bf16_f32 v144, v108, v109\n\t"
    "v_mfma_f32_16x16x32_bf16 v[124:127], a[20:23], v[216:219], v[124:127]\n\t"
    "s_branch Lcs11%=\n\t"
    ".p2align 12\n\t"
    "Lc11%=:\n\t"
    "s_setpc_b64 s[22:23]\n\t"
    "Lcs11%=:\n\t"
    "s_add_u32 s39, s39, s21\n\t"
    "v_exp_f32 v111, v111\n\t"
    "v_mfma_f32_16x16x32_bf16 v[124:127], a[24:27], v[220:223], v[124:127]\n\t"
    "ds_read_b64_tr_b16 a[56:57], v190 offset:8192\n\t"
    "ds_read_b64_tr_b16 a[58:59], v190 offset:12288\n\t"
    "v_mfma_f32_16x16x32_bf16 v[124:127], a[28:31], v[224:227], v[124:127]\n\t"
    "v_exp_f32 v112, v112\n\t"
    "v_mfma_f32_16x16x32_bf16 v[128:131], a[16:19], v[228:231], v[164:167]\n\t"
    "ds_read_b64_tr_b16 a[60:61], v191 offset:8192\n\t"
    "v_cvt_pk_bf16_f32 v145, v110, v111\n\t"
    "v_mfma_f32_16x16x32_bf16 v[128:131], a[20:23], v[232:235], v[128:131]\n\t"
    "ds_read_b64_tr_b16 a[62:63], v191 offset:12288\n\t"
    "ds_read_b128 a[0:3], v172\n\t"
    "v_mfma_f32_16x16x32_bf16 v[128:131], a[24:27], v[236:239], v[128:131]\n\t"
    "v_exp_f32 v113, v113\n\t"
    "v_mfma_f32_16x16x32_bf16 v[128:131], a[28:31], v[240:243], v[128:131]\n\t"
    "v_exp_f32 v114, v114\n\t"
    "v_mfma_f32_16x16x32_bf16 v[96:99], a[240:243], v[132:135], v[96:99]\n\t"
    "ds_read_b128 a[4:7], v173\n\t"
    "v_cvt_pk_bf16_f32 v148, v112, v113\n\t"
    "v_mfma_f32_16x16x32_bf16 v[96:99], a[244:247], v[136:139], v[96:99]\n\t"
    "Lw4%=:\n\t"
    "s_sub_u32 s75, s75, 2\n\t"
    "s_cbranch_scc0 Lw4%=\n\t"
    "v_mfma_f32_16x16x32_bf16 v[96:99], a[248:251], v[140:143], v[96:99]\n\t"
    "s_waitcnt lgkmcnt(2)\n\t"
    "v_exp_f32 v115, v115\n\t"
    "v_mfma_f32_16x16x32_bf16 v[64:67], a[32:35], v[140:143], v[64:67]\n\t"
    "buffer_load_dwordx4 v193, s[40:43], s67 offen offset:1024 nt lds\n\t"
    "v_mfma_f32_16x16x32_bf16 v[32:35], a[32:35], v[136:139], v[32:35]\n\t"
    "v_exp_f32 v116, v116\n\t"
    "v_mfma_f32_16x16x32_bf16 v[0:3], a[32:35], v[132:135], v[0:3]\n\t"
    "ds_read_b128 a[8:11], v174\n\t"
    "v_cvt_pk_bf16_f32 v149, v114, v115\n\t"
    "v_mfma_f32_16x16x32_bf16 v[4:7], a[36:39], v[132:135], v[4:7]\n\t"
    "v_exp_f32 v117, v117\n\t"
    "v_mfma_f32_16x16x32_bf16 v[36:39], a[36:39], v[136:139], v[36:39]\n\t"
    "v_exp_f32 v118, v118\n\t"
    "v_mfma_f32_16x16x32_bf16 v[68:71], a[36:39], v[140:143], v[68:71]\n\t"
    "ds_read_b128 a[12:15], v175\n\t"
    "v_cvt_pk_bf16_f32 v152, v116, v117\n\t"
    "v_mfma_f32_16x16x32_bf16 v[72:75], a[40:43], v[140:143], v[72:75]\n\t"
    "v_exp_f32 v119, v119\n\t"
    "v_mfma_f32_16x16x32_bf16 v[40:43], a[40:43], v[136:139], v[40:43]\n\t"
    "s_add_u32 m0, s68, 0xe000\n\t"
    "v_exp_f32 v120, v120\n\t"
    "v_mfma_f32_16x16x32_bf16 v[8:11], a[40:43], v[132:135], v[8:11]\n\t"
    "ds_read_b128 a[16:19], v172 offset:4096\n\t"
    "v_cvt_pk_bf16_f32 v153, v118, v119\n\t"
    "v_mfma_f32_16x16x32_bf16 v[12:15], a[44:47], v[132:135], v[12:15]\n\t"
    "buffer_load_dwordx4 v194, s[44:47], s67 offen nt lds\n\t"
    "v_mfma_f32_16x16x32_bf16 v[44:47], a[44:47], v[136:139], v[44:47]\n\t"
    "v_exp_f32 v121, v121\n\t"
    "v_mfma_f32_16x16x32_bf16 v[76:79], a[44:47], v[140:143], v[76:79]\n\t"
    "s_memrealtime s[70:71]\n\t"
    "v_exp_f32 v122, v122\n\t"
    "v_mfma_f32_16x16x32_bf16 v[80:83], a[48:51], v[140:143], v[80:83]\n\t"
    "ds_read_b128 a[20:23], v173 offset:4096\n\t"
    "v_cvt_pk_bf16_f32 v146, v120, v121\n\t"
    "v_mfma_f32_16x16x32_bf16 v[48:51], a[48:51], v[136:139], v[48:51]\n\t"
    "v_exp_f32 v123, v123\n\t"
    "v_mfma_f32_16x16x32_bf16 v[16:19], a[48:51], v[132:135], v[16:19]\n\t"
    "v_exp_f32 v124, v124\n\t"
    "v_mfma_f32_16x16x32_bf16 v[20:23], a[52:55], v[132:135], v[20:23]\n\t"
    "ds_read_b128 a[24:27], v174 offset:4096\n\t"
    "v_cvt_pk_bf16_f32 v147, v122, v123\n\t"
    "v_mfma_f32_16x16x32_bf16 v[52:55], a[52:55], v[136:139], v[52:55]\n\t"
    "v_exp_f32 v125, v125\n\t"
    "v_mfma_f32_16x16x32_bf16 v[84:87], a[52:55], v[140:143], v[84:87]\n\t"
    "v_exp_f32 v126, v126\n\t"
    "v_mfma_f32_16x16x32_bf16 v[88:91], a[56:59], v[140:143], v[88:91]\n\t"
    "ds_read_b128 a[28:31], v175 offset:4096\n\t"
    "v_cvt_pk_bf16_f32 v150, v124, v125\n\t"
    "v_mfma_f32_16x16x32_bf16 v[56:59], a[56:59], v[136:139], v[56:59]\n\t"
    "buffer_load_dwordx4 v195, s[44:47], s67 offen offset:1024 nt lds\n\t"
    "v_mfma_f32_16x16x32_bf16 v[24:27], a[56:59], v[132:135], v[24:27]\n\t"
    "s_add_u32 s67, s67, 0x2000\n\t"
    "v_mfma_f32_16x16x32_bf16 v[28:31], a[60:63], v[132:135], v[28:31]\n\t"
    "v_mfma_f32_16x16x32_bf16 v[60:63], a[60:63], v[136:139], v[60:63]\n\t"
    "s_waitcnt vmcnt(12) lgkmcnt(0)\n\t"
    "v_mfma_f32_16x16x32_bf16 v[92:95], a[60:63], v[140:143], v[92:95]\n\t"
    "s_barrier\n\t"
    "v_mfma_f32_16x16x32_bf16 v[108:111], a[0:3], v[196:199], v[156:159]\n\t"
    "ds_read_b64_tr_b16 a[32:33], v184 offset:24576\n\t"
    "ds_read_b64_tr_b16 a[34:35], v184 offset:28672\n\t"
    "v_mfma_f32_16x16x32_bf16 v[108:111], a[4:7], v[200:203], v[108:111]\n\t"
    "s_sub_u32 s75, s39, s70\n\t"
    "v_exp_f32 v127, v127\n\t"
    "v_mfma_f32_16x16x32_bf16 v[108:111], a[8:11], v[204:207], v[108:111]\n\t"
    "s_max_i32 s39, s75, 0\n\t"
    "v_exp_f32 v128, v128\n\t"
    "v_mfma_f32_16x16x32_bf16 v[108:111], a[12:15], v[208:211], v[108:111]\n\t"
    "s_add_u32 m0, s68, 0x10000\n\t"
    "ds_read_b64_tr_b16 a[36:37], v185 offset:24576\n\t"
    "v_cvt_pk_bf16_f32 v151, v126, v127\n\t"
    "v_mfma_f32_16x16x32_bf16 v[112:115], a[0:3], v[212:215], v[160:163]\n\t"
    "s_sub_u32 s21, s39, s75\n\t"
    "buffer_load_dwordx4 v192, s[40:43], s67 offen nt lds\n\t"
    "v_mfma_f32_16x16x32_bf16 v[112:115], a[4:7], v[216:219], v[112:115]\n\t"
    "s_add_u32 s39, s39, s70\n\t"
    "v_exp_f32 v129, v129\n\t"
    "v_mfma_f32_16x16x32_bf16 v[112:115], a[8:11], v[220:223], v[112:115]\n\t"
    "s_max_i32 s75, s75, 0\n\t"
    "v_exp_f32 v130, v130\n\t"
    "v_mfma_f32_16x16x32_bf16 v[112:115], a[12:15], v[224:227], v[112:115]\n\t"
    "s_min_i32 s21, s21, s77\n\t"
    "ds_read_b64_tr_b16 a[38:39], v185 offset:28672\n\t"
    "v_cvt_pk_bf16_f32 v154, v128, v129\n\t"
    "v_mfma_f32_16x16x32_bf16 v[116:119], a[0:3], v[228:231], v[164:167]\n\t"
    "s_add_u32 s39, s39, s19\n\t"
    "v_exp_f32 v131, v131\n\t"
    "v_mfma_f32_16x16x32_bf16 v[116:119], a[4:7], v[232:235], v[116:119]\n\t"
    "s_add_u32 s18, s18, s21\n\t"
    "ds_read_b64_tr_b16 a[40:41], v186 offset:24576\n\t"
    "v_cvt_pk_bf16_f32 v155, v130, v131\n\t"
    "v_mfma_f32_16x16x32_bf16 v[116:119], a[8:11], v[236:239], v[116:119]\n\t"
    "s_sub_u32 s18, s18, 6\n\t"
    "ds_read_b64_tr_b16 a[42:43], v186 offset:28672\n\t"
    "ds_read_b64_tr_b16 a[44:45], v187 offset:24576\n\t"
    "v_mfma_f32_16x16x32_bf16 v[116:119], a[12:15], v[240:243], v[116:119]\n\t"
    "s_max_i32 s18, s18, 0\n\t"
    "v_exp_f32 v108, v108\n\t"
    "v_mfma_f32_16x16x32_bf16 v[120:123], a[16:19], v[196:199], v[156:159]\n\t"
    "ds_read_b64_tr_b16 a[46:47], v187 offset:28672\n\t"
    "ds_read_b64_tr_b16 a[48:49], v188 offset:24576\n\t"
    "v_mfma_f32_16x16x32_bf16 v[120:123], a[20:23], v[200:203], v[120:123]\n\t"
    "s_min_i32 s18, s18, 60\n\t"
    "v_exp_f32 v109, v109\n\t"
    "v_mfma_f32_16x16x32_bf16 v[120:123], a[24:27], v[204:207], v[120:123]\n\t"
    "ds_read_b64_tr_b16 a[50:51], v188 offset:28672\n\t"
    "ds_read_b64_tr_b16 a[52:53], v189 offset:24576\n\t"
    "v_mfma_f32_16x16x32_bf16 v[120:123], a[28:31], v[208:211], v[120:123]\n\t"
    "s_lshr_b32 s21, s18, 1\n\t"
    "v_exp_f32 v110, v110\n\t"
    "v_mfma_f32_16x16x32_bf16 v[124:127], a[16:19], v[212:215], v[160:163]\n\t"
    "ds_read_b64_tr_b16 a[54:55], v189 offset:28672\n\t"
    "v_cvt_pk_bf16_f32 v132, v108, v109\n\t"
    "v_mfma_f32_16x16x32_bf16 v[124:127], a[20:23], v[216:219], v[124:127]\n\t"
    "s_add_u32 s39, s39, s21\n\t"
    "v_exp_f32 v111, v111\n\t"
    "v_mfma_f32_16x16x32_bf16 v[124:127], a[24:27], v[220:223], v[124:127]\n\t"
    "ds_read_b64_tr_b16 a[56:57], v190 offset:24576\n\t"
    "ds_read_b64_tr_b16 a[58:59], v190 offset:28672\n\t"
    "v_mfma_f32_16x16x32_bf16 v[124:127], a[28:31], v[224:227], v[124:127]\n\t"
    "v_exp_f32 v112, v112\n\t"
    "v_mfma_f32_16x16x32_bf16 v[128:131], a[16:19], v[228:231], v[164:167]\n\t"
    "ds_read_b64_tr_b16 a[60:61], v191 offset:24576\n\t"
    "v_cvt_pk_bf16_f32 v133, v110, v111\n\t"
    "v_mfma_f32_16x16x32_bf16 v[128:131], a[20:23], v[232:235], v[128:131]\n\t"
    "ds_read_b64_tr_b16 a[62:63], v191 offset:28672\n\t"
    "ds_read_b128 a[0:3], v172 offset:16384\n\t"
    "v_mfma_f32_16x16x32_bf16 v[128:131], a[24:27], v[236:239], v[128:131]\n\t"
    "v_exp_f32 v113, v113\n\t"
    "v_mfma_f32_16x16x32_bf16 v[128:131], a[28:31], v[240:243], v[128:131]\n\t"
    "v_exp_f32 v114, v114\n\t"
    "v_mfma_f32_16x16x32_bf16 v[96:99], a[240:243], v[144:147], v[96:99]\n\t"
    "ds_read_b128 a[4:7], v173 offset:16384\n\t"
    "v_cvt_pk_bf16_f32 v136, v112, v113\n\t"
    "v_mfma_f32_16x16x32_bf16 v[96:99], a[244:247], v[148:151], v[96:99]\n\t"
    "Lw5%=:\n\t"
    "s_sub_u32 s75, s75, 2\n\t"
    "s_cbranch_scc0 Lw5%=\n\t"
    "v_mfma_f32_16x16x32_bf16 v[96:99], a[248:251], v[152:155], v[96:99]\n\t"
    "s_waitcnt lgkmcnt(2)\n\t"
    "v_exp_f32 v115, v115\n\t"
    "v_mfma_f32_16x16x32_bf16 v[64:67], a[32:35], v[152:155], v[64:67]\n\t"
    "buffer_load_dwordx4 v193, s[40:43], s67 offen offset:1024 nt lds\n\t"
    "v_mfma_f32_16x16x32_bf16 v[32:35], a[32:35], v[148:151], v[32:35]\n\t"
    "v_exp_f32 v116, v116\n\t"
    "v_mfma_f32_16x16x32_bf16 v[0:3], a[32:35], v[144:147], v[0:3]\n\t"
    "ds_read_b128 a[8:11], v174 offset:16384\n\t"
    "v_cvt_pk_bf16_f32 v137, v114, v115\n\t"
    "v_mfma_f32_16x16x32_bf16 v[4:7], a[36:39], v[144:147], v[4:7]\n\t"
    "v_exp_f32 v117, v117\n\t"
    "v_mfma_f32_16x16x32_bf16 v[36:39], a[36:39], v[148:151], v[36:39]\n\t"
    "v_exp_f32 v118, v118\n\t"
    "v_mfma_f32_16x16x32_bf16 v[68:71], a[36:39], v[152:155], v[68:71]\n\t"
    "ds_read_b128 a[12:15], v175 offset:16384\n\t"
    "v_cvt_pk_bf16_f32 v140, v116, v117\n\t"
    "v_mfma_f32_16x16x32_bf16 v[72:75], a[40:43], v[152:155], v[72:75]\n\t"
    "v_exp_f32 v119, v119\n\t"
    "v_mfma_f32_16x16x32_bf16 v[40:43], a[40:43], v[148:151], v[40:43]\n\t"
    "s_add_u32 m0, s68, 0x12000\n\t"
    "v_exp_f32 v120, v120\n\t"
    "v_mfma_f32_16x16x32_bf16 v[8:11], a[40:43], v[144:147], v[8:11]\n\t"
    "ds_read_b128 a[16:19], v172 offset:20480\n\t"
    "v_cvt_pk_bf16_f32 v141, v118, v119\n\t"
    "v_mfma_f32_16x16x32_bf16 v[12:15], a[44:47], v[144:147], v[12:15]\n\t"
    "buffer_load_dwordx4 v194, s[44:47], s67 offen nt lds\n\t"
    "v_mfma_f32_16x16x32_bf16 v[44:47], a[44:47], v[148:151], v[44:47]\n\t"
    "v_exp_f32 v121, v121\n\t"
    "v_mfma_f32_16x16x32_bf16 v[76:79], a[44:47], v[152:155], v[76:79]\n\t"
    "s_memrealtime s[70:71]\n\t"
    "v_exp_f32 v122, v122\n\t"
    "v_mfma_f32_16x16x32_bf16 v[80:83], a[48:51], v[152:155], v[80:83]\n\t"
    "ds_read_b128 a[20:23], v173 offset:20480\n\t"
    "v_cvt_pk_bf16_f32 v134, v120, v121\n\t"
    "v_mfma_f32_16x16x32_bf16 v[48:51], a[48:51], v[148:151], v[48:51]\n\t"
    "v_exp_f32 v123, v123\n\t"
    "v_mfma_f32_16x16x32_bf16 v[16:19], a[48:51], v[144:147], v[16:19]\n\t"
    "v_exp_f32 v124, v124\n\t"
    "v_mfma_f32_16x16x32_bf16 v[20:23], a[52:55], v[144:147], v[20:23]\n\t"
    "ds_read_b128 a[24:27], v174 offset:20480\n\t"
    "v_cvt_pk_bf16_f32 v135, v122, v123\n\t"
    "v_mfma_f32_16x16x32_bf16 v[52:55], a[52:55], v[148:151], v[52:55]\n\t"
    "v_exp_f32 v125, v125\n\t"
    "v_mfma_f32_16x16x32_bf16 v[84:87], a[52:55], v[152:155], v[84:87]\n\t"
    "v_exp_f32 v126, v126\n\t"
    "v_mfma_f32_16x16x32_bf16 v[88:91], a[56:59], v[152:155], v[88:91]\n\t"
    "ds_read_b128 a[28:31], v175 offset:20480\n\t"
    "v_cvt_pk_bf16_f32 v138, v124, v125\n\t"
    "v_mfma_f32_16x16x32_bf16 v[56:59], a[56:59], v[148:151], v[56:59]\n\t"
    "buffer_load_dwordx4 v195, s[44:47], s67 offen offset:1024 nt lds\n\t"
    "v_mfma_f32_16x16x32_bf16 v[24:27], a[56:59], v[144:147], v[24:27]\n\t"
    "s_add_u32 s67, s67, 0x2000\n\t"
    "v_mfma_f32_16x16x32_bf16 v[28:31], a[60:63], v[144:147], v[28:31]\n\t"
    "v_mfma_f32_16x16x32_bf16 v[60:63], a[60:63], v[148:151], v[60:63]\n\t"
    "s_waitcnt vmcnt(12) lgkmcnt(0)\n\t"
    "v_mfma_f32_16x16x32_bf16 v[92:95], a[60:63], v[152:155], v[92:95]\n\t"
    "s_barrier\n\t"
    "v_mfma_f32_16x16x32_bf16 v[108:111], a[0:3], v[196:199], v[156:159]\n\t"
    "ds_read_b64_tr_b16 a[32:33], v184 offset:40960\n\t"
    "ds_read_b64_tr_b16 a[34:35], v184 offset:45056\n\t"
    "v_mfma_f32_16x16x32_bf16 v[108:111], a[4:7], v[200:203], v[108:111]\n\t"
    "s_sub_u32 s75, s39, s70\n\t"
    "v_exp_f32 v127, v127\n\t"
    "v_mfma_f32_16x16x32_bf16 v[108:111], a[8:11], v[204:207], v[108:111]\n\t"
    "s_max_i32 s39, s75, 0\n\t"
    "v_exp_f32 v128, v128\n\t"
    "v_mfma_f32_16x16x32_bf16 v[108:111], a[12:15], v[208:211], v[108:111]\n\t"
    "s_add_u32 m0, s68, 0x14000\n\t"
    "ds_read_b64_tr_b16 a[36:37], v185 offset:40960\n\t"
    "v_cvt_pk_bf16_f32 v139, v126, v127\n\t"
    "v_mfma_f32_16x16x32_bf16 v[112:115], a[0:3], v[212:215], v[160:163]\n\t"
    "s_sub_u32 s21, s39, s75\n\t"
    "buffer_load_dwordx4 v192, s[40:43], s67 offen nt lds\n\t"
    "v_mfma_f32_16x16x32_bf16 v[112:115], a[4:7], v[216:219], v[112:115]\n\t"
    "s_add_u32 s39, s39, s70\n\t"
    "v_exp_f32 v129, v129\n\t"
    "v_mfma_f32_16x16x32_bf16 v[112:115], a[8:11], v[220:223], v[112:115]\n\t"
    "s_max_i32 s75, s75, 0\n\t"
    "v_exp_f32 v130, v130\n\t"
    "v_mfma_f32_16x16x32_bf16 v[112:115], a[12:15], v[224:227], v[112:115]\n\t"
    "s_min_i32 s21, s21, s77\n\t"
    "ds_read_b64_tr_b16 a[38:39], v185 offset:45056\n\t"
    "v_cvt_pk_bf16_f32 v142, v128, v129\n\t"
    "v_mfma_f32_16x16x32_bf16 v[116:119], a[0:3], v[228:231], v[164:167]\n\t"
    "s_add_u32 s39, s39, s19\n\t"
    "v_exp_f32 v131, v131\n\t"
    "v_mfma_f32_16x16x32_bf16 v[116:119], a[4:7], v[232:235], v[116:119]\n\t"
    "s_add_u32 s18, s18, s21\n\t"
    "ds_read_b64_tr_b16 a[40:41], v186 offset:40960\n\t"
    "v_cvt_pk_bf16_f32 v143, v130, v131\n\t"
    "v_mfma_f32_16x16x32_bf16 v[116:119], a[8:11], v[236:239], v[116:119]\n\t"
    "s_sub_u32 s18, s18, 6\n\t"
    "ds_read_b64_tr_b16 a[42:43], v186 offset:45056\n\t"
    "ds_read_b64_tr_b16 a[44:45], v187 offset:40960\n\t"
    "v_mfma_f32_16x16x32_bf16 v[116:119], a[12:15], v[240:243], v[116:119]\n\t"
    "s_max_i32 s18, s18, 0\n\t"
    "v_exp_f32 v108, v108\n\t"
    "v_mfma_f32_16x16x32_bf16 v[120:123], a[16:19], v[196:199], v[156:159]\n\t"
    "ds_read_b64_tr_b16 a[46:47], v187 offset:45056\n\t"
    "ds_read_b64_tr_b16 a[48:49], v188 offset:40960\n\t"
    "v_mfma_f32_16x16x32_bf16 v[120:123], a[20:23], v[200:203], v[120:123]\n\t"
    "s_min_i32 s18, s18, 60\n\t"
    "v_exp_f32 v109, v109\n\t"
    "v_mfma_f32_16x16x32_bf16 v[120:123], a[24:27], v[204:207], v[120:123]\n\t"
    "ds_read_b64_tr_b16 a[50:51], v188 offset:45056\n\t"
    "ds_read_b64_tr_b16 a[52:53], v189 offset:40960\n\t"
    "v_mfma_f32_16x16x32_bf16 v[120:123], a[28:31], v[208:211], v[120:123]\n\t"
    "s_lshr_b32 s21, s18, 1\n\t"
    "v_exp_f32 v110, v110\n\t"
    "v_mfma_f32_16x16x32_bf16 v[124:127], a[16:19], v[212:215], v[160:163]\n\t"
    "ds_read_b64_tr_b16 a[54:55], v189 offset:45056\n\t"
    "v_cvt_pk_bf16_f32 v144, v108, v109\n\t"
    "v_mfma_f32_16x16x32_bf16 v[124:127], a[20:23], v[216:219], v[124:127]\n\t"
    "s_add_u32 s39, s39, s21\n\t"
    "v_exp_f32 v111, v111\n\t"
    "v_mfma_f32_16x16x32_bf16 v[124:127], a[24:27], v[220:223], v[124:127]\n\t"
    "ds_read_b64_tr_b16 a[56:57], v190 offset:40960\n\t"
    "ds_read_b64_tr_b16 a[58:59], v190 offset:45056\n\t"
    "v_mfma_f32_16x16x32_bf16 v[124:127], a[28:31], v[224:227], v[124:127]\n\t"
    "v_exp_f32 v112, v112\n\t"
    "v_mfma_f32_16x16x32_bf16 v[128:131], a[16:19], v[228:231], v[164:167]\n\t"
    "ds_read_b64_tr_b16 a[60:61], v191 offset:40960\n\t"
    "v_cvt_pk_bf16_f32 v145, v110, v111\n\t"
    "v_mfma_f32_16x16x32_bf16 v[128:131], a[20:23], v[232:235], v[128:131]\n\t"
    "ds_read_b64_tr_b16 a[62:63], v191 offset:45056\n\t"
    "ds_read_b128 a[0:3], v172 offset:32768\n\t"
    "v_mfma_f32_16x16x32_bf16 v[128:131], a[24:27], v[236:239], v[128:131]\n\t"
    "v_exp_f32 v113, v113\n\t"
    "v_mfma_f32_16x16x32_bf16 v[128:131], a[28:31], v[240:243], v[128:131]\n\t"
    "v_exp_f32 v114, v114\n\t"
    "v_mfma_f32_16x16x32_bf16 v[96:99], a[240:243], v[132:135], v[96:99]\n\t"
    "ds_read_b128 a[4:7], v173 offset:32768\n\t"
    "v_cvt_pk_bf16_f32 v148, v112, v113\n\t"
    "v_mfma_f32_16x16x32_bf16 v[96:99], a[244:247], v[136:139], v[96:99]\n\t"
    "Lw6%=:\n\t"
    "s_sub_u32 s75, s75, 2\n\t"
    "s_cbranch_scc0 Lw6%=\n\t"
    "v_mfma_f32_16x16x32_bf16 v[96:99], a[248:251], v[140:143], v[96:99]\n\t"
    "s_waitcnt lgkmcnt(2)\n\t"
    "v_exp_f32 v115, v115\n\t"
    "v_mfma_f32_16x16x32_bf16 v[64:67], a[32:35], v[140:143], v[64:67]\n\t"
    "buffer_load_dwordx4 v193, s[40:43], s67 offen offset:1024 nt lds\n\t"
    "v_mfma_f32_16x16x32_bf16 v[32:35], a[32:35], v[136:139], v[32:35]\n\t"
    "v_exp_f32 v116, v116\n\t"
    "v_mfma_f32_16x16x32_bf16 v[0:3], a[32:35], v[132:135], v[0:3]\n\t"
    "ds_read_b128 a[8:11], v174 offset:32768\n\t"
    "v_cvt_pk_bf16_f32 v149, v114, v115\n\t"
    "v_mfma_f32_16x16x32_bf16 v[4:7], a[36:39], v[132:135], v[4:7]\n\t"
    "v_exp_f32 v117, v117\n\t"
    "v_mfma_f32_16x16x32_bf16 v[36:39], a[36:39], v[136:139], v[36:39]\n\t"
    "v_exp_f32 v118, v118\n\t"
    "v_mfma_f32_16x16x32_bf16 v[68:71], a[36:39], v[140:143], v[68:71]\n\t"
    "ds_read_b128 a[12:15], v175 offset:32768\n\t"
    "v_cvt_pk_bf16_f32 v152, v116, v117\n\t"
    "v_mfma_f32_16x16x32_bf16 v[72:75], a[40:43], v[140:143], v[72:75]\n\t"
    "v_exp_f32 v119, v119\n\t"
    "v_mfma_f32_16x16x32_bf16 v[40:43], a[40:43], v[136:139], v[40:43]\n\t"
    "s_add_u32 m0, s68, 0x16000\n\t"
    "v_exp_f32 v120, v120\n\t"
    "v_mfma_f32_16x16x32_bf16 v[8:11], a[40:43], v[132:135], v[8:11]\n\t"
    "ds_read_b128 a[16:19], v172 offset:36864\n\t"
    "v_cvt_pk_bf16_f32 v153, v118, v119\n\t"
    "v_mfma_f32_16x16x32_bf16 v[12:15], a[44:47], v[132:135], v[12:15]\n\t"
    "buffer_load_dwordx4 v194, s[44:47], s67 offen nt lds\n\t"
    "v_mfma_f32_16x16x32_bf16 v[44:47], a[44:47], v[136:139], v[44:47]\n\t"
    "v_exp_f32 v121, v121\n\t"
    "v_mfma_f32_16x16x32_bf16 v[76:79], a[44:47], v[140:143], v[76:79]\n\t"
    "s_memrealtime s[70:71]\n\t"
    "v_exp_f32 v122, v122\n\t"
    "v_mfma_f32_16x16x32_bf16 v[80:83], a[48:51], v[140:143], v[80:83]\n\t"
    "ds_read_b128 a[20:23], v173 offset:36864\n\t"
    "v_cvt_pk_bf16_f32 v146, v120, v121\n\t"
    "v_mfma_f32_16x16x32_bf16 v[48:51], a[48:51], v[136:139], v[48:51]\n\t"
    "v_exp_f32 v123, v123\n\t"
    "v_mfma_f32_16x16x32_bf16 v[16:19], a[48:51], v[132:135], v[16:19]\n\t"
    "v_exp_f32 v124, v124\n\t"
    "v_mfma_f32_16x16x32_bf16 v[20:23], a[52:55], v[132:135], v[20:23]\n\t"
    "ds_read_b128 a[24:27], v174 offset:36864\n\t"
    "v_cvt_pk_bf16_f32 v147, v122, v123\n\t"
    "v_mfma_f32_16x16x32_bf16 v[52:55], a[52:55], v[136:139], v[52:55]\n\t"
    "v_exp_f32 v125, v125\n\t"
    "v_mfma_f32_16x16x32_bf16 v[84:87], a[52:55], v[140:143], v[84:87]\n\t"
    "v_exp_f32 v126, v126\n\t"
    "v_mfma_f32_16x16x32_bf16 v[88:91], a[56:59], v[140:143], v[88:91]\n\t"
    "ds_read_b128 a[28:31], v175 offset:36864\n\t"
    "v_cvt_pk_bf16_f32 v150, v124, v125\n\t"
    "v_mfma_f32_16x16x32_bf16 v[56:59], a[56:59], v[136:139], v[56:59]\n\t"
    "buffer_load_dwordx4 v195, s[44:47], s67 offen offset:1024 nt lds\n\t"
    "v_mfma_f32_16x16x32_bf16 v[24:27], a[56:59], v[132:135], v[24:27]\n\t"
    "s_add_u32 s67, s67, 0x2000\n\t"
    "v_mfma_f32_16x16x32_bf16 v[28:31], a[60:63], v[132:135], v[28:31]\n\t"
    "v_mfma_f32_16x16x32_bf16 v[60:63], a[60:63], v[136:139], v[60:63]\n\t"
    "s_waitcnt vmcnt(12) lgkmcnt(0)\n\t"
    "v_mfma_f32_16x16x32_bf16 v[92:95], a[60:63], v[140:143], v[92:95]\n\t"
    "s_setpc_b64 s[36:37]\n\t"
    "Lalt%=:\n\t"
    "s_waitcnt lgkmcnt(0)\n\t"
    "s_barrier\n\t"
    "v_mfma_f32_16x16x32_bf16 v[108:111], a[0:3], v[196:199], v[156:159]\n\t"
    "ds_read_b64_tr_b16 a[32:33], v176 offset:8192\n\t"
    "ds_read_b64_tr_b16 a[34:35], v176 offset:12288\n\t"
    "v_mfma_f32_16x16x32_bf16 v[108:111], a[4:7], v[200:203], v[108:111]\n\t"
    "s_sub_u32 s75, s39, s70\n\t"
    "v_exp_f32 v127, v127\n\t"
    "v_mfma_f32_16x16x32_bf16 v[108:111], a[8:11], v[204:207], v[108:111]\n\t"
    "s_max_i32 s39, s75, 0\n\t"
    "v_exp_f32 v128, v128\n\t"
    "v_mfma_f32_16x16x32_bf16 v[108:111], a[12:15], v[208:211], v[108:111]\n\t"
    "s_add_u32 m0, s68, 0x1c000\n\t"
    "ds_read_b64_tr_b16 a[36:37], v177 offset:8192\n\t"
    "v_cvt_pk_bf16_f32 v139, v126, v127\n\t"
    "v_mfma_f32_16x16x32_bf16 v[112:115], a[0:3], v[212:215], v[160:163]\n\t"
    "buffer_load_dwordx4 v192, s[40:43], s67 offen lds\n\t"
    "v_mfma_f32_16x16x32_bf16 v[112:115], a[4:7], v[216:219], v[112:115]\n\t"
    "s_add_u32 s39, s39, s70\n\t"
    "v_exp_f32 v129, v129\n\t"
    "v_mfma_f32_16x16x32_bf16 v[112:115], a[8:11], v[220:223], v[112:115]\n\t"
    "s_max_i32 s75, s75, 0\n\t"
    "v_exp_f32 v130, v130\n\t"
    "v_mfma_f32_16x16x32_bf16 v[112:115], a[12:15], v[224:227], v[112:115]\n\t"
    "ds_read_b64_tr_b16 a[38:39], v177 offset:12288\n\t"
    "v_cvt_pk_bf16_f32 v142, v128, v129\n\t"
    "v_mfma_f32_16x16x32_bf16 v[116:119], a[0:3], v[228:231], v[164:167]\n\t"
    "s_add_u32 s39, s39, s19\n\t"
    "v_exp_f32 v131, v131\n\t"
    "v_mfma_f32_16x16x32_bf16 v[116:119], a[4:7], v[232:235], v[116:119]\n\t"
    "ds_read_b64_tr_b16 a[40:41], v178 offset:8192\n\t"
    "v_cvt_pk_bf16_f32 v143, v130, v131\n\t"
    "v_mfma_f32_16x16x32_bf16 v[116:119], a[8:11], v[236:239], v[116:119]\n\t"
    "ds_read_b64_tr_b16 a[42:43], v178 offset:12288\n\t"
    "ds_read_b64_tr_b16 a[44:45], v179 offset:8192\n\t"
    "v_mfma_f32_16x16x32_bf16 v[116:119], a[12:15], v[240:243], v[116:119]\n\t"
    "v_exp_f32 v108, v108\n\t"
    "v_mfma_f32_16x16x32_bf16 v[120:123], a[16:19], v[196:199], v[156:159]\n\t"
    "ds_read_b64_tr_b16 a[46:47], v179 offset:12288\n\t"
    "ds_read_b64_tr_b16 a[48:49], v180 offset:8192\n\t"
    "v_mfma_f32_16x16x32_bf16 v[120:123], a[20:23], v[200:203], v[120:123]\n\t"
    "v_exp_f32 v109, v109\n\t"
    "v_mfma_f32_16x16x32_bf16 v[120:123], a[24:27], v[204:207], v[120:123]\n\t"
    "ds_read_b64_tr_b16 a[50:51], v180 offset:12288\n\t"
    "ds_read_b64_tr_b16 a[52:53], v181 offset:8192\n\t"
    "v_mfma_f32_16x16x32_bf16 v[120:123], a[28:31], v[208:211], v[120:123]\n\t"
    "v_exp_f32 v110, v110\n\t"
    "v_mfma_f32_16x16x32_bf16 v[124:127], a[16:19], v[212:215], v[160:163]\n\t"
    "ds_read_b64_tr_b16 a[54:55], v181 offset:12288\n\t"
    "v_cvt_pk_bf16_f32 v144, v108, v109\n\t"
    "v_mfma_f32_16x16x32_bf16 v[124:127], a[20:23], v[216:219], v[124:127]\n\t"
    "v_exp_f32 v111, v111\n\t"
    "v_mfma_f32_16x16x32_bf16 v[124:127], a[24:27], v[220:223], v[124:127]\n\t"
    "ds_read_b64_tr_b16 a[56:57], v182 offset:8192\n\t"
    "ds_read_b64_tr_b16 a[58:59], v182 offset:12288\n\t"
    "v_mfma_f32_16x16x32_bf16 v[124:127], a[28:31], v[224:227], v[124:127]\n\t"
    "v_exp_f32 v112, v112\n\t"
    "v_mfma_f32_16x16x32_bf16 v[128:131], a[16:19], v[228:231], v[164:167]\n\t"
    "ds_read_b64_tr_b16 a[60:61], v183 offset:8192\n\t"
    "v_cvt_pk_bf16_f32 v145, v110, v111\n\t"
    "v_mfma_f32_16x16x32_bf16 v[128:131], a[20:23], v[232:235], v[128:131]\n\t"
    "ds_read_b64_tr_b16 a[62:63], v183 offset:12288\n\t"
    "ds_read_b128 a[0:3], v168\n\t"
    "v_mfma_f32_16x16x32_bf16 v[128:131], a[24:27], v[236:239], v[128:131]\n\t"
    "v_exp_f32 v113, v113\n\t"
    "v_mfma_f32_16x16x32_bf16 v[128:131], a[28:31], v[240:243], v[128:131]\n\t"
    "v_exp_f32 v114, v114\n\t"
    "v_mfma_f32_16x16x32_bf16 v[96:99], a[240:243], v[132:135], v[96:99]\n\t"
    "ds_read_b128 a[4:7], v169\n\t"
    "v_cvt_pk_bf16_f32 v148, v112, v113\n\t"
    "v_mfma_f32_16x16x32_bf16 v[96:99], a[244:247], v[136:139], v[96:99]\n\t"
    "Lx0%=:\n\t"
    "s_sub_u32 s75, s75, 2\n\t"
    "s_cbranch_scc0 Lx0%=\n\t"
    "v_mfma_f32_16x16x32_bf16 v[96:99], a[248:251], v[140:143], v[96:99]\n\t"
    "s_waitcnt lgkmcnt(2)\n\t"
    "v_exp_f32 v115, v115\n\t"
    "v_mfma_f32_16x16x32_bf16 v[64:67], a[32:35], v[140:143], v[64:67]\n\t"
    "buffer_load_dwordx4 v193, s[40:43], s67 offen offset:1024 lds\n\t"
    "v_mfma_f32_16x16x32_bf16 v[32:35], a[32:35], v[136:139], v[32:35]\n\t"
    "v_exp_f32 v116, v116\n\t"
    "v_mfma_f32_16x16x32_bf16 v[0:3], a[32:35], v[132:135], v[0:3]\n\t"
    "ds_read_b128 a[8:11], v170\n\t"
    "v_cvt_pk_bf16_f32 v149, v114, v115\n\t"
    "v_mfma_f32_16x16x32_bf16 v[4:7], a[36:39], v[132:135], v[4:7]\n\t"
    "v_exp_f32 v117, v117\n\t"
    "v_mfma_f32_16x16x32_bf16 v[36:39], a[36:39], v[136:139], v[36:39]\n\t"
    "v_exp_f32 v118, v118\n\t"
    "v_mfma_f32_16x16x32_bf16 v[68:71], a[36:39], v[140:143], v[68:71]\n\t"
    "ds_read_b128 a[12:15], v171\n\t"
    "v_cvt_pk_bf16_f32 v152, v116, v117\n\t"
    "v_mfma_f32_16x16x32_bf16 v[72:75], a[40:43], v[140:143], v[72:75]\n\t"
    "v_exp_f32 v119, v119\n\t"
    "v_mfma_f32_16x16x32_bf16 v[40:43], a[40:43], v[136:139], v[40:43]\n\t"
    "s_add_u32 m0, s68, 0x1e000\n\t"
    "v_exp_f32 v120, v120\n\t"
    "v_mfma_f32_16x16x32_bf16 v[8:11], a[40:43], v[132:135], v[8:11]\n\t"
    "ds_read_b128 a[16:19], v168 offset:4096\n\t"
    "v_cvt_pk_bf16_f32 v153, v118, v119\n\t"
    "v_mfma_f32_16x16x32_bf16 v[12:15], a[44:47], v[132:135], v[12:15]\n\t"
    "buffer_load_dwordx4 v194, s[44:47], s67 offen lds\n\t"
    "v_mfma_f32_16x16x32_bf16 v[44:47], a[44:47], v[136:139], v[44:47]\n\t"
    "v_exp_f32 v121, v121\n\t"
    "v_mfma_f32_16x16x32_bf16 v[76:79], a[44:47], v[140:143], v[76:79]\n\t"
    "s_memrealtime s[70:71]\n\t"
    "v_exp_f32 v122, v122\n\t"
    "v_mfma_f32_16x16x32_bf16 v[80:83], a[48:51], v[140:143], v[80:83]\n\t"
    "ds_read_b128 a[20:23], v169 offset:4096\n\t"
    "v_cvt_pk_bf16_f32 v146, v120, v121\n\t"
    "v_mfma_f32_16x16x32_bf16 v[48:51], a[48:51], v[136:139], v[48:51]\n\t"
    "v_exp_f32 v123, v123\n\t"
    "v_mfma_f32_16x16x32_bf16 v[16:19], a[48:51], v[132:135], v[16:19]\n\t"
    "v_exp_f32 v124, v124\n\t"
    "v_mfma_f32_16x16x32_bf16 v[20:23], a[52:55], v[132:135], v[20:23]\n\t"
    "ds_read_b128 a[24:27], v170 offset:4096\n\t"
    "v_cvt_pk_bf16_f32 v147, v122, v123\n\t"
    "v_mfma_f32_16x16x32_bf16 v[52:55], a[52:55], v[136:139], v[52:55]\n\t"
    "v_exp_f32 v125, v125\n\t"
    "v_mfma_f32_16x16x32_bf16 v[84:87], a[52:55], v[140:143], v[84:87]\n\t"
    "v_exp_f32 v126, v126\n\t"
    "v_mfma_f32_16x16x32_bf16 v[88:91], a[56:59], v[140:143], v[88:91]\n\t"
    "ds_read_b128 a[28:31], v171 offset:4096\n\t"
    "v_cvt_pk_bf16_f32 v150, v124, v125\n\t"
    "v_mfma_f32_16x16x32_bf16 v[56:59], a[56:59], v[136:139], v[56:59]\n\t"
    "buffer_load_dwordx4 v195, s[44:47], s67 offen offset:1024 lds\n\t"
    "v_mfma_f32_16x16x32_bf16 v[24:27], a[56:59], v[132:135], v[24:27]\n\t"
    "s_add_u32 s67, s67, 0x2000\n\t"
    "v_mfma_f32_16x16x32_bf16 v[28:31], a[60:63], v[132:135], v[28:31]\n\t"
    "v_mfma_f32_16x16x32_bf16 v[60:63], a[60:63], v[136:139], v[60:63]\n\t"
    "s_waitcnt vmcnt(12) lgkmcnt(0)\n\t"
    "v_mfma_f32_16x16x32_bf16 v[92:95], a[60:63], v[140:143], v[92:95]\n\t"
    "s_barrier\n\t"
    "v_mfma_f32_16x16x32_bf16 v[108:111], a[0:3], v[196:199], v[156:159]\n\t"
    "ds_read_b64_tr_b16 a[32:33], v176 offset:24576\n\t"
    "ds_read_b64_tr_b16 a[34:35], v176 offset:28672\n\t"
    "v_mfma_f32_16x16x32_bf16 v[108:111], a[4:7], v[200:203], v[108:111]\n\t"
    "s_sub_u32 s75, s39, s70\n\t"
    "v_exp_f32 v127, v127\n\t"
    "v_mfma_f32_16x16x32_bf16 v[108:111], a[8:11], v[204:207], v[108:111]\n\t"
    "s_max_i32 s39, s75, 0\n\t"
    "v_exp_f32 v128, v128\n\t"
    "v_mfma_f32_16x16x32_bf16 v[108:111], a[12:15], v[208:211], v[108:111]\n\t"
    "s_add_u32 m0, s68, 0\n\t"
    "ds_read_b64_tr_b16 a[36:37], v177 offset:24576\n\t"
    "v_cvt_pk_bf16_f32 v151, v126, v127\n\t"
    "v_mfma_f32_16x16x32_bf16 v[112:115], a[0:3], v[212:215], v[160:163]\n\t"
    "buffer_load_dwordx4 v192, s[40:43], s67 offen lds\n\t"
    "v_mfma_f32_16x16x32_bf16 v[112:115], a[4:7], v[216:219], v[112:115]\n\t"
    "s_add_u32 s39, s39, s70\n\t"
    "v_exp_f32 v129, v129\n\t"
    "v_mfma_f32_16x16x32_bf16 v[112:115], a[8:11], v[220:223], v[112:115]\n\t"
    "s_max_i32 s75, s75, 0\n\t"
    "v_exp_f32 v130, v130\n\t"
    "v_mfma_f32_16x16x32_bf16 v[112:115], a[12:15], v[224:227], v[112:115]\n\t"
    "ds_read_b64_tr_b16 a[38:39], v177 offset:28672\n\t"
    "v_cvt_pk_bf16_f32 v154, v128, v129\n\t"
    "v_mfma_f32_16x16x32_bf16 v[116:119], a[0:3], v[228:231], v[164:167]\n\t"
    "s_add_u32 s39, s39, s19\n\t"
    "v_exp_f32 v131, v131\n\t"
    "v_mfma_f32_16x16x32_bf16 v[116:119], a[4:7], v[232:235], v[116:119]\n\t"
    "ds_read_b64_tr_b16 a[40:41], v178 offset:24576\n\t"
    "v_cvt_pk_bf16_f32 v155, v130, v131\n\t"
    "v_mfma_f32_16x16x32_bf16 v[116:119], a[8:11], v[236:239], v[116:119]\n\t"
    "ds_read_b64_tr_b16 a[42:43], v178 offset:28672\n\t"
    "ds_read_b64_tr_b16 a[44:45], v179 offset:24576\n\t"
    "v_mfma_f32_16x16x32_bf16 v[116:119], a[12:15], v[240:243], v[116:119]\n\t"
    "v_exp_f32 v108, v108\n\t"
    "v_mfma_f32_16x16x32_bf16 v[120:123], a[16:19], v[196:199], v[156:159]\n\t"
    "ds_read_b64_tr_b16 a[46:47], v179 offset:28672\n\t"
    "ds_read_b64_tr_b16 a[48:49], v180 offset:24576\n\t"
    "v_mfma_f32_16x16x32_bf16 v[120:123], a[20:23], v[200:203], v[120:123]\n\t"
    "v_exp_f32 v109, v109\n\t"
    "v_mfma_f32_16x16x32_bf16 v[120:123], a[24:27], v[204:207], v[120:123]\n\t"
    "ds_read_b64_tr_b16 a[50:51], v180 offset:28672\n\t"
    "ds_read_b64_tr_b16 a[52:53], v181 offset:24576\n\t"
    "v_mfma_f32_16x16x32_bf16 v[120:123], a[28:31], v[208:211], v[120:123]\n\t"
    "v_exp_f32 v110, v110\n\t"
    "v_mfma_f32_16x16x32_bf16 v[124:127], a[16:19], v[212:215], v[160:163]\n\t"
    "ds_read_b64_tr_b16 a[54:55], v181 offset:28672\n\t"
    "v_cvt_pk_bf16_f32 v132, v108, v109\n\t"
    "v_mfma_f32_16x16x32_bf16 v[124:127], a[20:23], v[216:219], v[124:127]\n\t"
    "v_exp_f32 v111, v111\n\t"
    "v_mfma_f32_16x16x32_bf16 v[124:127], a[24:27], v[220:223], v[124:127]\n\t"
    "ds_read_b64_tr_b16 a[56:57], v182 offset:24576\n\t"
    "ds_read_b64_tr_b16 a[58:59], v182 offset:28672\n\t"
    "v_mfma_f32_16x16x32_bf16 v[124:127], a[28:31], v[224:227], v[124:127]\n\t"
    "v_exp_f32 v112, v112\n\t"
    "v_mfma_f32_16x16x32_bf16 v[128:131], a[16:19], v[228:231], v[164:167]\n\t"
    "ds_read_b64_tr_b16 a[60:61], v183 offset:24576\n\t"
    "v_cvt_pk_bf16_f32 v133, v110, v111\n\t"
    "v_mfma_f32_16x16x32_bf16 v[128:131], a[20:23], v[232:235], v[128:131]\n\t"
    "ds_read_b64_tr_b16 a[62:63], v183 offset:28672\n\t"
    "ds_read_b128 a[0:3], v168 offset:16384\n\t"
    "v_mfma_f32_16x16x32_bf16 v[128:131], a[24:27], v[236:239], v[128:131]\n\t"
    "v_exp_f32 v113, v113\n\t"
    "v_mfma_f32_16x16x32_bf16 v[128:131], a[28:31], v[240:243], v[128:131]\n\t"
    "v_exp_f32 v114, v114\n\t"
    "v_mfma_f32_16x16x32_bf16 v[96:99], a[240:243], v[144:147], v[96:99]\n\t"
    "ds_read_b128 a[4:7], v169 offset:16384\n\t"
    "v_cvt_pk_bf16_f32 v136, v112, v113\n\t"
    "v_mfma_f32_16x16x32_bf16 v[96:99], a[244:247], v[148:151], v[96:99]\n\t"
    "Lx1%=:\n\t"
    "s_sub_u32 s75, s75, 2\n\t"
    "s_cbranch_scc0 Lx1%=\n\t"
    "v_mfma_f32_16x16x32_bf16 v[96:99], a[248:251], v[152:155], v[96:99]\n\t"
    "s_waitcnt lgkmcnt(2)\n\t"
    "v_exp_f32 v115, v115\n\t"
    "v_mfma_f32_16x16x32_bf16 v[64:67], a[32:35], v[152:155], v[64:67]\n\t"
    "buffer_load_dwordx4 v193, s[40:43], s67 offen offset:1024 lds\n\t"
    "v_mfma_f32_16x16x32_bf16 v[32:35], a[32:35], v[148:151], v[32:35]\n\t"
    "v_exp_f32 v116, v116\n\t"
    "v_mfma_f32_16x16x32_bf16 v[0:3], a[32:35], v[144:147], v[0:3]\n\t"
    "ds_read_b128 a[8:11], v170 offset:16384\n\t"
    "v_cvt_pk_bf16_f32 v137, v114, v115\n\t"
    "v_mfma_f32_16x16x32_bf16 v[4:7], a[36:39], v[144:147], v[4:7]\n\t"
    "v_exp_f32 v117, v117\n\t"
    "v_mfma_f32_16x16x32_bf16 v[36:39], a[36:39], v[148:151], v[36:39]\n\t"
    "v_exp_f32 v118, v118\n\t"
    "v_mfma_f32_16x16x32_bf16 v[68:71], a[36:39], v[152:155], v[68:71]\n\t"
    "ds_read_b128 a[12:15], v171 offset:16384\n\t"
    "v_cvt_pk_bf16_f32 v140, v116, v117\n\t"
    "v_mfma_f32_16x16x32_bf16 v[72:75], a[40:43], v[152:155], v[72:75]\n\t"
    "v_exp_f32 v119, v119\n\t"
    "v_mfma_f32_16x16x32_bf16 v[40:43], a[40:43], v[148:151], v[40:43]\n\t"
    "s_add_u32 m0, s68, 0x2000\n\t"
    "v_exp_f32 v120, v120\n\t"
    "v_mfma_f32_16x16x32_bf16 v[8:11], a[40:43], v[144:147], v[8:11]\n\t"
    "ds_read_b128 a[16:19], v168 offset:20480\n\t"
    "v_cvt_pk_bf16_f32 v141, v118, v119\n\t"
    "v_mfma_f32_16x16x32_bf16 v[12:15], a[44:47], v[144:147], v[12:15]\n\t"
    "buffer_load_dwordx4 v194, s[44:47], s67 offen lds\n\t"
    "v_mfma_f32_16x16x32_bf16 v[44:47], a[44:47], v[148:151], v[44:47]\n\t"
    "v_exp_f32 v121, v121\n\t"
    "v_mfma_f32_16x16x32_bf16 v[76:79], a[44:47], v[152:155], v[76:79]\n\t"
    "s_memrealtime s[70:71]\n\t"
    "v_exp_f32 v122, v122\n\t"
    "v_mfma_f32_16x16x32_bf16 v[80:83], a[48:51], v[152:155], v[80:83]\n\t"
    "ds_read_b128 a[20:23], v169 offset:20480\n\t"
    "v_cvt_pk_bf16_f32 v134, v120, v121\n\t"
    "v_mfma_f32_16x16x32_bf16 v[48:51], a[48:51], v[148:151], v[48:51]\n\t"
    "v_exp_f32 v123, v123\n\t"
    "v_mfma_f32_16x16x32_bf16 v[16:19], a[48:51], v[144:147], v[16:19]\n\t"
    "v_exp_f32 v124, v124\n\t"
    "v_mfma_f32_16x16x32_bf16 v[20:23], a[52:55], v[144:147], v[20:23]\n\t"
    "ds_read_b128 a[24:27], v170 offset:20480\n\t"
    "v_cvt_pk_bf16_f32 v135, v122, v123\n\t"
    "v_mfma_f32_16x16x32_bf16 v[52:55], a[52:55], v[148:151], v[52:55]\n\t"
    "v_exp_f32 v125, v125\n\t"
    "v_mfma_f32_16x16x32_bf16 v[84:87], a[52:55], v[152:155], v[84:87]\n\t"
    "v_exp_f32 v126, v126\n\t"
    "v_mfma_f32_16x16x32_bf16 v[88:91], a[56:59], v[152:155], v[88:91]\n\t"
    "ds_read_b128 a[28:31], v171 offset:20480\n\t"
    "v_cvt_pk_bf16_f32 v138, v124, v125\n\t"
    "v_mfma_f32_16x16x32_bf16 v[56:59], a[56:59], v[148:151], v[56:59]\n\t"
    "buffer_load_dwordx4 v195, s[44:47], s67 offen offset:1024 lds\n\t"
    "v_mfma_f32_16x16x32_bf16 v[24:27], a[56:59], v[144:147], v[24:27]\n\t"
    "s_add_u32 s67, s67, 0x2000\n\t"
    "v_mfma_f32_16x16x32_bf16 v[28:31], a[60:63], v[144:147], v[28:31]\n\t"
    "v_mfma_f32_16x16x32_bf16 v[60:63], a[60:63], v[148:151], v[60:63]\n\t"
    "s_waitcnt vmcnt(12) lgkmcnt(0)\n\t"
    "v_mfma_f32_16x16x32_bf16 v[92:95], a[60:63], v[152:155], v[92:95]\n\t"
    "s_barrier\n\t"
    "v_mfma_f32_16x16x32_bf16 v[108:111], a[0:3], v[196:199], v[156:159]\n\t"
    "ds_read_b64_tr_b16 a[32:33], v176 offset:40960\n\t"
    "ds_read_b64_tr_b16 a[34:35], v176 offset:45056\n\t"
    "v_mfma_f32_16x16x32_bf16 v[108:111], a[4:7], v[200:203], v[108:111]\n\t"
    "s_sub_u32 s75, s39, s70\n\t"
    "v_exp_f32 v127, v127\n\t"
    "v_mfma_f32_16x16x32_bf16 v[108:111], a[8:11], v[204:207], v[108:111]\n\t"
    "s_max_i32 s39, s75, 0\n\t"
    "v_exp_f32 v128, v128\n\t"
    "v_mfma_f32_16x16x32_bf16 v[108:111], a[12:15], v[208:211], v[108:111]\n\t"
    "s_add_u32 m0, s68, 0x4000\n\t"
    "ds_read_b64_tr_b16 a[36:37], v177 offset:40960\n\t"
    "v_cvt_pk_bf16_f32 v139, v126, v127\n\t"
    "v_mfma_f32_16x16x32_bf16 v[112:115], a[0:3], v[212:215], v[160:163]\n\t"
    "buffer_load_dwordx4 v192, s[40:43], s67 offen lds\n\t"
    "v_mfma_f32_16x16x32_bf16 v[112:115], a[4:7], v[216:219], v[112:115]\n\t"
    "s_add_u32 s39, s39, s70\n\t"
    "v_exp_f32 v129, v129\n\t"
    "v_mfma_f32_16x16x32_bf16 v[112:115], a[8:11], v[220:223], v[112:115]\n\t"
    "s_max_i32 s75, s75, 0\n\t"
    "v_exp_f32 v130, v130\n\t"
    "v_mfma_f32_16x16x32_bf16 v[112:115], a[12:15], v[224:227], v[112:115]\n\t"
    "ds_read_b64_tr_b16 a[38:39], v177 offset:45056\n\t"
    "v_cvt_pk_bf16_f32 v142, v128, v129\n\t"
    "v_mfma_f32_16x16x32_bf16 v[116:119], a[0:3], v[228:231], v[164:167]\n\t"
    "s_add_u32 s39, s39, s19\n\t"
    "v_exp_f32 v131, v131\n\t"
    "v_mfma_f32_16x16x32_bf16 v[116:119], a[4:7], v[232:235], v[116:119]\n\t"
    "ds_read_b64_tr_b16 a[40:41], v178 offset:40960\n\t"
    "v_cvt_pk_bf16_f32 v143, v130, v131\n\t"
    "v_mfma_f32_16x16x32_bf16 v[116:119], a[8:11], v[236:239], v[116:119]\n\t"
    "ds_read_b64_tr_b16 a[42:43], v178 offset:45056\n\t"
    "ds_read_b64_tr_b16 a[44:45], v179 offset:40960\n\t"
    "v_mfma_f32_16x16x32_bf16 v[116:119], a[12:15], v[240:243], v[116:119]\n\t"
    "v_exp_f32 v108, v108\n\t"
    "v_mfma_f32_16x16x32_bf16 v[120:123], a[16:19], v[196:199], v[156:159]\n\t"
    "ds_read_b64_tr_b16 a[46:47], v179 offset:45056\n\t"
    "ds_read_b64_tr_b16 a[48:49], v180 offset:40960\n\t"
    "v_mfma_f32_16x16x32_bf16 v[120:123], a[20:23], v[200:203], v[120:123]\n\t"
    "v_exp_f32 v109, v109\n\t"
    "v_mfma_f32_16x16x32_bf16 v[120:123], a[24:27], v[204:207], v[120:123]\n\t"
    "ds_read_b64_tr_b16 a[50:51], v180 offset:45056\n\t"
    "ds_read_b64_tr_b16 a[52:53], v181 offset:40960\n\t"
    "v_mfma_f32_16x16x32_bf16 v[120:123], a[28:31], v[208:211], v[120:123]\n\t"
    "v_exp_f32 v110, v110\n\t"
    "v_mfma_f32_16x16x32_bf16 v[124:127], a[16:19], v[212:215], v[160:163]\n\t"
    "ds_read_b64_tr_b16 a[54:55], v181 offset:45056\n\t"
    "v_cvt_pk_bf16_f32 v144, v108, v109\n\t"
    "v_mfma_f32_16x16x32_bf16 v[124:127], a[20:23], v[216:219], v[124:127]\n\t"
    "v_exp_f32 v111, v111\n\t"
    "v_mfma_f32_16x16x32_bf16 v[124:127], a[24:27], v[220:223], v[124:127]\n\t"
    "ds_read_b64_tr_b16 a[56:57], v182 offset:40960\n\t"
    "ds_read_b64_tr_b16 a[58:59], v182 offset:45056\n\t"
    "v_mfma_f32_16x16x32_bf16 v[124:127], a[28:31], v[224:227], v[124:127]\n\t"
    "v_exp_f32 v112, v112\n\t"
    "v_mfma_f32_16x16x32_bf16 v[128:131], a[16:19], v[228:231], v[164:167]\n\t"
    "ds_read_b64_tr_b16 a[60:61], v183 offset:40960\n\t"
    "v_cvt_pk_bf16_f32 v145, v110, v111\n\t"
    "v_mfma_f32_16x16x32_bf16 v[128:131], a[20:23], v[232:235], v[128:131]\n\t"
    "ds_read_b64_tr_b16 a[62:63], v183 offset:45056\n\t"
    "ds_read_b128 a[0:3], v168 offset:32768\n\t"
    "v_mfma_f32_16x16x32_bf16 v[128:131], a[24:27], v[236:239], v[128:131]\n\t"
    "v_exp_f32 v113, v113\n\t"
    "v_mfma_f32_16x16x32_bf16 v[128:131], a[28:31], v[240:243], v[128:131]\n\t"
    "v_exp_f32 v114, v114\n\t"
    "v_mfma_f32_16x16x32_bf16 v[96:99], a[240:243], v[132:135], v[96:99]\n\t"
    "ds_read_b128 a[4:7], v169 offset:32768\n\t"
    "v_cvt_pk_bf16_f32 v148, v112, v113\n\t"
    "v_mfma_f32_16x16x32_bf16 v[96:99], a[244:247], v[136:139], v[96:99]\n\t"
    "Lx2%=:\n\t"
    "s_sub_u32 s75, s75, 2\n\t"
    "s_cbranch_scc0 Lx2%=\n\t"
    "v_mfma_f32_16x16x32_bf16 v[96:99], a[248:251], v[140:143], v[96:99]\n\t"
    "s_waitcnt lgkmcnt(2)\n\t"
    "v_exp_f32 v115, v115\n\t"
    "v_mfma_f32_16x16x32_bf16 v[64:67], a[32:35], v[140:143], v[64:67]\n\t"
    "buffer_load_dwordx4 v193, s[40:43], s67 offen offset:1024 lds\n\t"
    "v_mfma_f32_16x16x32_bf16 v[32:35], a[32:35], v[136:139], v[32:35]\n\t"
    "v_exp_f32 v116, v116\n\t"
    "v_mfma_f32_16x16x32_bf16 v[0:3], a[32:35], v[132:135], v[0:3]\n\t"
    "ds_read_b128 a[8:11], v170 offset:32768\n\t"
    "v_cvt_pk_bf16_f32 v149, v114, v115\n\t"
    "v_mfma_f32_16x16x32_bf16 v[4:7], a[36:39], v[132:135], v[4:7]\n\t"
    "v_exp_f32 v117, v117\n\t"
    "v_mfma_f32_16x16x32_bf16 v[36:39], a[36:39], v[136:139], v[36:39]\n\t"
    "v_exp_f32 v118, v118\n\t"
    "v_mfma_f32_16x16x32_bf16 v[68:71], a[36:39], v[140:143], v[68:71]\n\t"
    "ds_read_b128 a[12:15], v171 offset:32768\n\t"
    "v_cvt_pk_bf16_f32 v152, v116, v117\n\t"
    "v_mfma_f32_16x16x32_bf16 v[72:75], a[40:43], v[140:143], v[72:75]\n\t"
    "v_exp_f32 v119, v119\n\t"
    "v_mfma_f32_16x16x32_bf16 v[40:43], a[40:43], v[136:139], v[40:43]\n\t"
    "s_add_u32 m0, s68, 0x6000\n\t"
    "v_exp_f32 v120, v120\n\t"
    "v_mfma_f32_16x16x32_bf16 v[8:11], a[40:43], v[132:135], v[8:11]\n\t"
    "ds_read_b128 a[16:19], v168 offset:36864\n\t"
    "v_cvt_pk_bf16_f32 v153, v118, v119\n\t"
    "v_mfma_f32_16x16x32_bf16 v[12:15], a[44:47], v[132:135], v[12:15]\n\t"
    "buffer_load_dwordx4 v194, s[44:47], s67 offen lds\n\t"
    "v_mfma_f32_16x16x32_bf16 v[44:47], a[44:47], v[136:139], v[44:47]\n\t"
    "v_exp_f32 v121, v121\n\t"
    "v_mfma_f32_16x16x32_bf16 v[76:79], a[44:47], v[140:143], v[76:79]\n\t"
    "s_memrealtime s[70:71]\n\t"
    "v_exp_f32 v122, v122\n\t"
    "v_mfma_f32_16x16x32_bf16 v[80:83], a[48:51], v[140:143], v[80:83]\n\t"
    "ds_read_b128 a[20:23], v169 offset:36864\n\t"
    "v_cvt_pk_bf16_f32 v146, v120, v121\n\t"
    "v_mfma_f32_16x16x32_bf16 v[48:51], a[48:51], v[136:139], v[48:51]\n\t"
    "v_exp_f32 v123, v123\n\t"
    "v_mfma_f32_16x16x32_bf16 v[16:19], a[48:51], v[132:135], v[16:19]\n\t"
    "v_exp_f32 v124, v124\n\t"
    "v_mfma_f32_16x16x32_bf16 v[20:23], a[52:55], v[132:135], v[20:23]\n\t"
    "ds_read_b128 a[24:27], v170 offset:36864\n\t"
    "v_cvt_pk_bf16_f32 v147, v122, v123\n\t"
    "v_mfma_f32_16x16x32_bf16 v[52:55], a[52:55], v[136:139], v[52:55]\n\t"
    "v_exp_f32 v125, v125\n\t"
    "v_mfma_f32_16x16x32_bf16 v[84:87], a[52:55], v[140:143], v[84:87]\n\t"
    "v_exp_f32 v126, v126\n\t"
    "v_mfma_f32_16x16x32_bf16 v[88:91], a[56:59], v[140:143], v[88:91]\n\t"
    "ds_read_b128 a[28:31], v171 offset:36864\n\t"
    "v_cvt_pk_bf16_f32 v150, v124, v125\n\t"
    "v_mfma_f32_16x16x32_bf16 v[56:59], a[56:59], v[136:139], v[56:59]\n\t"
    "buffer_load_dwordx4 v195, s[44:47], s67 offen offset:1024 lds\n\t"
    "v_mfma_f32_16x16x32_bf16 v[24:27], a[56:59], v[132:135], v[24:27]\n\t"
    "s_add_u32 s67, s67, 0x2000\n\t"
    "v_mfma_f32_16x16x32_bf16 v[28:31], a[60:63], v[132:135], v[28:31]\n\t"
    "v_mfma_f32_16x16x32_bf16 v[60:63], a[60:63], v[136:139], v[60:63]\n\t"
    "s_waitcnt vmcnt(12) lgkmcnt(0)\n\t"
    "v_mfma_f32_16x16x32_bf16 v[92:95], a[60:63], v[140:143], v[92:95]\n\t"
    "s_barrier\n\t"
    "v_mfma_f32_16x16x32_bf16 v[108:111], a[0:3], v[196:199], v[156:159]\n\t"
    "ds_read_b64_tr_b16 a[32:33], v176 offset:57344\n\t"
    "ds_read_b64_tr_b16 a[34:35], v176 offset:61440\n\t"
    "v_mfma_f32_16x16x32_bf16 v[108:111], a[4:7], v[200:203], v[108:111]\n\t"
    "s_sub_u32 s75, s39, s70\n\t"
    "v_exp_f32 v127, v127\n\t"
    "v_mfma_f32_16x16x32_bf16 v[108:111], a[8:11], v[204:207], v[108:111]\n\t"
    "s_max_i32 s39, s75, 0\n\t"
    "v_exp_f32 v128, v128\n\t"
    "v_mfma_f32_16x16x32_bf16 v[108:111], a[12:15], v[208:211], v[108:111]\n\t"
    "s_add_u32 m0, s68, 0x8000\n\t"
    "ds_read_b64_tr_b16 a[36:37], v177 offset:57344\n\t"
    "v_cvt_pk_bf16_f32 v151, v126, v127\n\t"
    "v_mfma_f32_16x16x32_bf16 v[112:115], a[0:3], v[212:215], v[160:163]\n\t"
    "buffer_load_dwordx4 v192, s[40:43], s67 offen lds\n\t"
    "v_mfma_f32_16x16x32_bf16 v[112:115], a[4:7], v[216:219], v[112:115]\n\t"
    "s_add_u32 s39, s39, s70\n\t"
    "v_exp_f32 v129, v129\n\t"
    "v_mfma_f32_16x16x32_bf16 v[112:115], a[8:11], v[220:223], v[112:115]\n\t"
    "s_max_i32 s75, s75, 0\n\t"
    "v_exp_f32 v130, v130\n\t"
    "v_mfma_f32_16x16x32_bf16 v[112:115], a[12:15], v[224:227], v[112:115]\n\t"
    "ds_read_b64_tr_b16 a[38:39], v177 offset:61440\n\t"
    "v_cvt_pk_bf16_f32 v154, v128, v129\n\t"
    "v_mfma_f32_16x16x32_bf16 v[116:119], a[0:3], v[228:231], v[164:167]\n\t"
    "s_add_u32 s39, s39, s19\n\t"
    "v_exp_f32 v131, v131\n\t"
    "v_mfma_f32_16x16x32_bf16 v[116:119], a[4:7], v[232:235], v[116:119]\n\t"
    "ds_read_b64_tr_b16 a[40:41], v178 offset:57344\n\t"
    "v_cvt_pk_bf16_f32 v155, v130, v131\n\t"
    "v_mfma_f32_16x16x32_bf16 v[116:119], a[8:11], v[236:239], v[116:119]\n\t"
    "ds_read_b64_tr_b16 a[42:43], v178 offset:61440\n\t"
    "ds_read_b64_tr_b16 a[44:45], v179 offset:57344\n\t"
    "v_mfma_f32_16x16x32_bf16 v[116:119], a[12:15], v[240:243], v[116:119]\n\t"
    "v_exp_f32 v108, v108\n\t"
    "v_mfma_f32_16x16x32_bf16 v[120:123], a[16:19], v[196:199], v[156:159]\n\t"
    "ds_read_b64_tr_b16 a[46:47], v179 offset:61440\n\t"
    "ds_read_b64_tr_b16 a[48:49], v180 offset:57344\n\t"
    "v_mfma_f32_16x16x32_bf16 v[120:123], a[20:23], v[200:203], v[120:123]\n\t"
    "v_exp_f32 v109, v109\n\t"
    "v_mfma_f32_16x16x32_bf16 v[120:123], a[24:27], v[204:207], v[120:123]\n\t"
    "ds_read_b64_tr_b16 a[50:51], v180 offset:61440\n\t"
    "ds_read_b64_tr_b16 a[52:53], v181 offset:57344\n\t"
    "v_mfma_f32_16x16x32_bf16 v[120:123], a[28:31], v[208:211], v[120:123]\n\t"
    "v_exp_f32 v110, v110\n\t"
    "v_mfma_f32_16x16x32_bf16 v[124:127], a[16:19], v[212:215], v[160:163]\n\t"
    "ds_read_b64_tr_b16 a[54:55], v181 offset:61440\n\t"
    "v_cvt_pk_bf16_f32 v132, v108, v109\n\t"
    "v_mfma_f32_16x16x32_bf16 v[124:127], a[20:23], v[216:219], v[124:127]\n\t"
    "v_exp_f32 v111, v111\n\t"
    "v_mfma_f32_16x16x32_bf16 v[124:127], a[24:27], v[220:223], v[124:127]\n\t"
    "ds_read_b64_tr_b16 a[56:57], v182 offset:57344\n\t"
    "ds_read_b64_tr_b16 a[58:59], v182 offset:61440\n\t"
    "v_mfma_f32_16x16x32_bf16 v[124:127], a[28:31], v[224:227], v[124:127]\n\t"
    "v_exp_f32 v112, v112\n\t"
    "v_mfma_f32_16x16x32_bf16 v[128:131], a[16:19], v[228:231], v[164:167]\n\t"
    "ds_read_b64_tr_b16 a[60:61], v183 offset:57344\n\t"
    "v_cvt_pk_bf16_f32 v133, v110, v111\n\t"
    "v_mfma_f32_16x16x32_bf16 v[128:131], a[20:23], v[232:235], v[128:131]\n\t"
    "ds_read_b64_tr_b16 a[62:63], v183 offset:61440\n\t"
    "ds_read_b128 a[0:3], v168 offset:49152\n\t"
    "v_mfma_f32_16x16x32_bf16 v[128:131], a[24:27], v[236:239], v[128:131]\n\t"
    "v_exp_f32 v113, v113\n\t"
    "v_mfma_f32_16x16x32_bf16 v[128:131], a[28:31], v[240:243], v[128:131]\n\t"
    "v_exp_f32 v114, v114\n\t"
    "v_mfma_f32_16x16x32_bf16 v[96:99], a[240:243], v[144:147], v[96:99]\n\t"
    "ds_read_b128 a[4:7], v169 offset:49152\n\t"
    "v_cvt_pk_bf16_f32 v136, v112, v113\n\t"
    "v_mfma_f32_16x16x32_bf16 v[96:99], a[244:247], v[148:151], v[96:99]\n\t"
    "Lx3%=:\n\t"
    "s_sub_u32 s75, s75, 2\n\t"
    "s_cbranch_scc0 Lx3%=\n\t"
    "v_mfma_f32_16x16x32_bf16 v[96:99], a[248:251], v[152:155], v[96:99]\n\t"
    "s_waitcnt lgkmcnt(2)\n\t"
    "v_exp_f32 v115, v115\n\t"
    "v_mfma_f32_16x16x32_bf16 v[64:67], a[32:35], v[152:155], v[64:67]\n\t"
    "buffer_load_dwordx4 v193, s[40:43], s67 offen offset:1024 lds\n\t"
    "v_mfma_f32_16x16x32_bf16 v[32:35], a[32:35], v[148:151], v[32:35]\n\t"
    "v_exp_f32 v116, v116\n\t"
    "v_mfma_f32_16x16x32_bf16 v[0:3], a[32:35], v[144:147], v[0:3]\n\t"
    "ds_read_b128 a[8:11], v170 offset:49152\n\t"
    "v_cvt_pk_bf16_f32 v137, v114, v115\n\t"
    "v_mfma_f32_16x16x32_bf16 v[4:7], a[36:39], v[144:147], v[4:7]\n\t"
    "v_exp_f32 v117, v117\n\t"
    "v_mfma_f32_16x16x32_bf16 v[36:39], a[36:39], v[148:151], v[36:39]\n\t"
    "v_exp_f32 v118, v118\n\t"
    "v_mfma_f32_16x16x32_bf16 v[68:71], a[36:39], v[152:155], v[68:71]\n\t"
    "ds_read_b128 a[12:15], v171 offset:49152\n\t"
    "v_cvt_pk_bf16_f32 v140, v116, v117\n\t"
    "v_mfma_f32_16x16x32_bf16 v[72:75], a[40:43], v[152:155], v[72:75]\n\t"
    "v_exp_f32 v119, v119\n\t"
    "v_mfma_f32_16x16x32_bf16 v[40:43], a[40:43], v[148:151], v[40:43]\n\t"
    "s_add_u32 m0, s68, 0xa000\n\t"
    "v_exp_f32 v120, v120\n\t"
    "v_mfma_f32_16x16x32_bf16 v[8:11], a[40:43], v[144:147], v[8:11]\n\t"
    "ds_read_b128 a[16:19], v168 offset:53248\n\t"
    "v_cvt_pk_bf16_f32 v141, v118, v119\n\t"
    "v_mfma_f32_16x16x32_bf16 v[12:15], a[44:47], v[144:147], v[12:15]\n\t"
    "buffer_load_dwordx4 v194, s[44:47], s67 offen lds\n\t"
    "v_mfma_f32_16x16x32_bf16 v[44:47], a[44:47], v[148:151], v[44:47]\n\t"
    "v_exp_f32 v121, v121\n\t"
    "v_mfma_f32_16x16x32_bf16 v[76:79], a[44:47], v[152:155], v[76:79]\n\t"
    "s_memrealtime s[70:71]\n\t"
    "v_exp_f32 v122, v122\n\t"
    "v_mfma_f32_16x16x32_bf16 v[80:83], a[48:51], v[152:155], v[80:83]\n\t"
    "ds_read_b128 a[20:23], v169 offset:53248\n\t"
    "v_cvt_pk_bf16_f32 v134, v120, v121\n\t"
    "v_mfma_f32_16x16x32_bf16 v[48:51], a[48:51], v[148:151], v[48:51]\n\t"
    "v_exp_f32 v123, v123\n\t"
    "v_mfma_f32_16x16x32_bf16 v[16:19], a[48:51], v[144:147], v[16:19]\n\t"
    "v_exp_f32 v124, v124\n\t"
    "v_mfma_f32_16x16x32_bf16 v[20:23], a[52:55], v[144:147], v[20:23]\n\t"
    "ds_read_b128 a[24:27], v170 offset:53248\n\t"
    "v_cvt_pk_bf16_f32 v135, v122, v123\n\t"
    "v_mfma_f32_16x16x32_bf16 v[52:55], a[52:55], v[148:151], v[52:55]\n\t"
    "v_exp_f32 v125, v125\n\t"
    "v_mfma_f32_16x16x32_bf16 v[84:87], a[52:55], v[152:155], v[84:87]\n\t"
    "v_exp_f32 v126, v126\n\t"
    "v_mfma_f32_16x16x32_bf16 v[88:91], a[56:59], v[152:155], v[88:91]\n\t"
    "ds_read_b128 a[28:31], v171 offset:53248\n\t"
    "v_cvt_pk_bf16_f32 v138, v124, v125\n\t"
    "v_mfma_f32_16x16x32_bf16 v[56:59], a[56:59], v[148:151], v[56:59]\n\t"
    "buffer_load_dwordx4 v195, s[44:47], s67 offen offset:1024 lds\n\t"
    "v_mfma_f32_16x16x32_bf16 v[24:27], a[56:59], v[144:147], v[24:27]\n\t"
    "s_add_u32 s67, s67, 0x2000\n\t"
    "v_mfma_f32_16x16x32_bf16 v[28:31], a[60:63], v[144:147], v[28:31]\n\t"
    "v_mfma_f32_16x16x32_bf16 v[60:63], a[60:63], v[148:151], v[60:63]\n\t"
    "s_waitcnt vmcnt(12) lgkmcnt(0)\n\t"
    "v_mfma_f32_16x16x32_bf16 v[92:95], a[60:63], v[152:155], v[92:95]\n\t"
    "s_barrier\n\t"
    "v_mfma_f32_16x16x32_bf16 v[108:111], a[0:3], v[196:199], v[156:159]\n\t"
    "ds_read_b64_tr_b16 a[32:33], v184 offset:8192\n\t"
    "ds_read_b64_tr_b16 a[34:35], v184 offset:12288\n\t"
    "v_mfma_f32_16x16x32_bf16 v[108:111], a[4:7], v[200:203], v[108:111]\n\t"
    "s_sub_u32 s75, s39, s70\n\t"
    "v_exp_f32 v127, v127\n\t"
    "v_mfma_f32_16x16x32_bf16 v[108:111], a[8:11], v[204:207], v[108:111]\n\t"
    "s_max_i32 s39, s75, 0\n\t"
    "v_exp_f32 v128, v128\n\t"
    "v_mfma_f32_16x16x32_bf16 v[108:111], a[12:15], v[208:211], v[108:111]\n\t"
    "s_add_u32 m0, s68, 0xc000\n\t"
    "ds_read_b64_tr_b16 a[36:37], v185 offset:8192\n\t"
    "v_cvt_pk_bf16_f32 v139, v126, v127\n\t"
    "v_mfma_f32_16x16x32_bf16 v[112:115], a[0:3], v[212:215], v[160:163]\n\t"
    "buffer_load_dwordx4 v192, s[40:43], s67 offen lds\n\t"
    "v_mfma_f32_16x16x32_bf16 v[112:115], a[4:7], v[216:219], v[112:115]\n\t"
    "s_add_u32 s39, s39, s70\n\t"
    "v_exp_f32 v129, v129\n\t"
    "v_mfma_f32_16x16x32_bf16 v[112:115], a[8:11], v[220:223], v[112:115]\n\t"
    "s_max_i32 s75, s75, 0\n\t"
    "v_exp_f32 v130, v130\n\t"
    "v_mfma_f32_16x16x32_bf16 v[112:115], a[12:15], v[224:227], v[112:115]\n\t"
    "ds_read_b64_tr_b16 a[38:39], v185 offset:12288\n\t"
    "v_cvt_pk_bf16_f32 v142, v128, v129\n\t"
    "v_mfma_f32_16x16x32_bf16 v[116:119], a[0:3], v[228:231], v[164:167]\n\t"
    "s_add_u32 s39, s39, s19\n\t"
    "v_exp_f32 v131, v131\n\t"
    "v_mfma_f32_16x16x32_bf16 v[116:119], a[4:7], v[232:235], v[116:119]\n\t"
    "ds_read_b64_tr_b16 a[40:41], v186 offset:8192\n\t"
    "v_cvt_pk_bf16_f32 v143, v130, v131\n\t"
    "v_mfma_f32_16x16x32_bf16 v[116:119], a[8:11], v[236:239], v[116:119]\n\t"
    "ds_read_b64_tr_b16 a[42:43], v186 offset:12288\n\t"
    "ds_read_b64_tr_b16 a[44:45], v187 offset:8192\n\t"
    "v_mfma_f32_16x16x32_bf16 v[116:119], a[12:15], v[240:243], v[116:119]\n\t"
    "v_exp_f32 v108, v108\n\t"
    "v_mfma_f32_16x16x32_bf16 v[120:123], a[16:19], v[196:199], v[156:159]\n\t"
    "ds_read_b64_tr_b16 a[46:47], v187 offset:12288\n\t"
    "ds_read_b64_tr_b16 a[48:49], v188 offset:8192\n\t"
    "v_mfma_f32_16x16x32_bf16 v[120:123], a[20:23], v[200:203], v[120:123]\n\t"
    "v_exp_f32 v109, v109\n\t"
    "v_mfma_f32_16x16x32_bf16 v[120:123], a[24:27], v[204:207], v[120:123]\n\t"
    "ds_read_b64_tr_b16 a[50:51], v188 offset:12288\n\t"
    "ds_read_b64_tr_b16 a[52:53], v189 offset:8192\n\t"
    "v_mfma_f32_16x16x32_bf16 v[120:123], a[28:31], v[208:211], v[120:123]\n\t"
    "v_exp_f32 v110, v110\n\t"
    "v_mfma_f32_16x16x32_bf16 v[124:127], a[16:19], v[212:215], v[160:163]\n\t"
    "ds_read_b64_tr_b16 a[54:55], v189 offset:12288\n\t"
    "v_cvt_pk_bf16_f32 v144, v108, v109\n\t"
    "v_mfma_f32_16x16x32_bf16 v[124:127], a[20:23], v[216:219], v[124:127]\n\t"
    "v_exp_f32 v111, v111\n\t"
    "v_mfma_f32_16x16x32_bf16 v[124:127], a[24:27], v[220:223], v[124:127]\n\t"
    "ds_read_b64_tr_b16 a[56:57], v190 offset:8192\n\t"
    "ds_read_b64_tr_b16 a[58:59], v190 offset:12288\n\t"
    "v_mfma_f32_16x16x32_bf16 v[124:127], a[28:31], v[224:227], v[124:127]\n\t"
    "v_exp_f32 v112, v112\n\t"
    "v_mfma_f32_16x16x32_bf16 v[128:131], a[16:19], v[228:231], v[164:167]\n\t"
    "ds_read_b64_tr_b16 a[60:61], v191 offset:8192\n\t"
    "v_cvt_pk_bf16_f32 v145, v110, v111\n\t"
    "v_mfma_f32_16x16x32_bf16 v[128:131], a[20:23], v[232:235], v[128:131]\n\t"
    "ds_read_b64_tr_b16 a[62:63], v191 offset:12288\n\t"
    "ds_read_b128 a[0:3], v172\n\t"
    "v_mfma_f32_16x16x32_bf16 v[128:131], a[24:27], v[236:239], v[128:131]\n\t"
    "v_exp_f32 v113, v113\n\t"
    "v_mfma_f32_16x16x32_bf16 v[128:131], a[28:31], v[240:243], v[128:131]\n\t"
    "v_exp_f32 v114, v114\n\t"
    "v_mfma_f32_16x16x32_bf16 v[96:99], a[240:243], v[132:135], v[96:99]\n\t"
    "ds_read_b128 a[4:7], v173\n\t"
    "v_cvt_pk_bf16_f32 v148, v112, v113\n\t"
    "v_mfma_f32_16x16x32_bf16 v[96:99], a[244:247], v[136:139], v[96:99]\n\t"
    "Lx4%=:\n\t"
    "s_sub_u32 s75, s75, 2\n\t"
    "s_cbranch_scc0 Lx4%=\n\t"
    "v_mfma_f32_16x16x32_bf16 v[96:99], a[248:251], v[140:143], v[96:99]\n\t"
    "s_waitcnt lgkmcnt(2)\n\t"
    "v_exp_f32 v115, v115\n\t"
    "v_mfma_f32_16x16x32_bf16 v[64:67], a[32:35], v[140:143], v[64:67]\n\t"
    "buffer_load_dwordx4 v193, s[40:43], s67 offen offset:1024 lds\n\t"
    "v_mfma_f32_16x16x32_bf16 v[32:35], a[32:35], v[136:139], v[32:35]\n\t"
    "v_exp_f32 v116, v116\n\t"
    "v_mfma_f32_16x16x32_bf16 v[0:3], a[32:35], v[132:135], v[0:3]\n\t"
    "ds_read_b128 a[8:11], v174\n\t"
    "v_cvt_pk_bf16_f32 v149, v114, v115\n\t"
    "v_mfma_f32_16x16x32_bf16 v[4:7], a[36:39], v[132:135], v[4:7]\n\t"
    "v_exp_f32 v117, v117\n\t"
    "v_mfma_f32_16x16x32_bf16 v[36:39], a[36:39], v[136:139], v[36:39]\n\t"
    "v_exp_f32 v118, v118\n\t"
    "v_mfma_f32_16x16x32_bf16 v[68:71], a[36:39], v[140:143], v[68:71]\n\t"
    "ds_read_b128 a[12:15], v175\n\t"
    "v_cvt_pk_bf16_f32 v152, v116, v117\n\t"
    "v_mfma_f32_16x16x32_bf16 v[72:75], a[40:43], v[140:143], v[72:75]\n\t"
    "v_exp_f32 v119, v119\n\t"
    "v_mfma_f32_16x16x32_bf16 v[40:43], a[40:43], v[136:139], v[40:43]\n\t"
    "s_add_u32 m0, s68, 0xe000\n\t"
    "v_exp_f32 v120, v120\n\t"
    "v_mfma_f32_16x16x32_bf16 v[8:11], a[40:43], v[132:135], v[8:11]\n\t"
    "ds_read_b128 a[16:19], v172 offset:4096\n\t"
    "v_cvt_pk_bf16_f32 v153, v118, v119\n\t"
    "v_mfma_f32_16x16x32_bf16 v[12:15], a[44:47], v[132:135], v[12:15]\n\t"
    "buffer_load_dwordx4 v194, s[44:47], s67 offen lds\n\t"
    "v_mfma_f32_16x16x32_bf16 v[44:47], a[44:47], v[136:139], v[44:47]\n\t"
    "v_exp_f32 v121, v121\n\t"
    "v_mfma_f32_16x16x32_bf16 v[76:79], a[44:47], v[140:143], v[76:79]\n\t"
    "s_memrealtime s[70:71]\n\t"
    "v_exp_f32 v122, v122\n\t"
    "v_mfma_f32_16x16x32_bf16 v[80:83], a[48:51], v[140:143], v[80:83]\n\t"
    "ds_read_b128 a[20:23], v173 offset:4096\n\t"
    "v_cvt_pk_bf16_f32 v146, v120, v121\n\t"
    "v_mfma_f32_16x16x32_bf16 v[48:51], a[48:51], v[136:139], v[48:51]\n\t"
    "v_exp_f32 v123, v123\n\t"
    "v_mfma_f32_16x16x32_bf16 v[16:19], a[48:51], v[132:135], v[16:19]\n\t"
    "v_exp_f32 v124, v124\n\t"
    "v_mfma_f32_16x16x32_bf16 v[20:23], a[52:55], v[132:135], v[20:23]\n\t"
    "ds_read_b128 a[24:27], v174 offset:4096\n\t"
    "v_cvt_pk_bf16_f32 v147, v122, v123\n\t"
    "v_mfma_f32_16x16x32_bf16 v[52:55], a[52:55], v[136:139], v[52:55]\n\t"
    "v_exp_f32 v125, v125\n\t"
    "v_mfma_f32_16x16x32_bf16 v[84:87], a[52:55], v[140:143], v[84:87]\n\t"
    "v_exp_f32 v126, v126\n\t"
    "v_mfma_f32_16x16x32_bf16 v[88:91], a[56:59], v[140:143], v[88:91]\n\t"
    "ds_read_b128 a[28:31], v175 offset:4096\n\t"
    "v_cvt_pk_bf16_f32 v150, v124, v125\n\t"
    "v_mfma_f32_16x16x32_bf16 v[56:59], a[56:59], v[136:139], v[56:59]\n\t"
    "buffer_load_dwordx4 v195, s[44:47], s67 offen offset:1024 lds\n\t"
    "v_mfma_f32_16x16x32_bf16 v[24:27], a[56:59], v[132:135], v[24:27]\n\t"
    "s_add_u32 s67, s67, 0x2000\n\t"
    "v_mfma_f32_16x16x32_bf16 v[28:31], a[60:63], v[132:135], v[28:31]\n\t"
    "v_mfma_f32_16x16x32_bf16 v[60:63], a[60:63], v[136:139], v[60:63]\n\t"
    "s_waitcnt vmcnt(12) lgkmcnt(0)\n\t"
    "v_mfma_f32_16x16x32_bf16 v[92:95], a[60:63], v[140:143], v[92:95]\n\t"
    "s_barrier\n\t"
    "v_mfma_f32_16x16x32_bf16 v[108:111], a[0:3], v[196:199], v[156:159]\n\t"
    "ds_read_b64_tr_b16 a[32:33], v184 offset:24576\n\t"
    "ds_read_b64_tr_b16 a[34:35], v184 offset:28672\n\t"
    "v_mfma_f32_16x16x32_bf16 v[108:111], a[4:7], v[200:203], v[108:111]\n\t"
    "s_sub_u32 s75, s39, s70\n\t"
    "v_exp_f32 v127, v127\n\t"
    "v_mfma_f32_16x16x32_bf16 v[108:111], a[8:11], v[204:207], v[108:111]\n\t"
    "s_max_i32 s39, s75, 0\n\t"
    "v_exp_f32 v128, v128\n\t"
    "v_mfma_f32_16x16x32_bf16 v[108:111], a[12:15], v[208:211], v[108:111]\n\t"
    "s_add_u32 m0, s68, 0x10000\n\t"
    "ds_read_b64_tr_b16 a[36:37], v185 offset:24576\n\t"
    "v_cvt_pk_bf16_f32 v151, v126, v127\n\t"
    "v_mfma_f32_16x16x32_bf16 v[112:115], a[0:3], v[212:215], v[160:163]\n\t"
    "buffer_load_dwordx4 v192, s[40:43], s67 offen lds\n\t"
    "v_mfma_f32_16x16x32_bf16 v[112:115], a[4:7], v[216:219], v[112:115]\n\t"
    "s_add_u32 s39, s39, s70\n\t"
    "v_exp_f32 v129, v129\n\t"
    "v_mfma_f32_16x16x32_bf16 v[112:115], a[8:11], v[220:223], v[112:115]\n\t"
    "s_max_i32 s75, s75, 0\n\t"
    "v_exp_f32 v130, v130\n\t"
    "v_mfma_f32_16x16x32_bf16 v[112:115], a[12:15], v[224:227], v[112:115]\n\t"
    "ds_read_b64_tr_b16 a[38:39], v185 offset:28672\n\t"
    "v_cvt_pk_bf16_f32 v154, v128, v129\n\t"
    "v_mfma_f32_16x16x32_bf16 v[116:119], a[0:3], v[228:231], v[164:167]\n\t"
    "s_add_u32 s39, s39, s19\n\t"
    "v_exp_f32 v131, v131\n\t"
    "v_mfma_f32_16x16x32_bf16 v[116:119], a[4:7], v[232:235], v[116:119]\n\t"
    "ds_read_b64_tr_b16 a[40:41], v186 offset:24576\n\t"
    "v_cvt_pk_bf16_f32 v155, v130, v131\n\t"
    "v_mfma_f32_16x16x32_bf16 v[116:119], a[8:11], v[236:239], v[116:119]\n\t"
    "ds_read_b64_tr_b16 a[42:43], v186 offset:28672\n\t"
    "ds_read_b64_tr_b16 a[44:45], v187 offset:24576\n\t"
    "v_mfma_f32_16x16x32_bf16 v[116:119], a[12:15], v[240:243], v[116:119]\n\t"
    "v_exp_f32 v108, v108\n\t"
    "v_mfma_f32_16x16x32_bf16 v[120:123], a[16:19], v[196:199], v[156:159]\n\t"
    "ds_read_b64_tr_b16 a[46:47], v187 offset:28672\n\t"
    "ds_read_b64_tr_b16 a[48:49], v188 offset:24576\n\t"
    "v_mfma_f32_16x16x32_bf16 v[120:123], a[20:23], v[200:203], v[120:123]\n\t"
    "v_exp_f32 v109, v109\n\t"
    "v_mfma_f32_16x16x32_bf16 v[120:123], a[24:27], v[204:207], v[120:123]\n\t"
    "ds_read_b64_tr_b16 a[50:51], v188 offset:28672\n\t"
    "ds_read_b64_tr_b16 a[52:53], v189 offset:24576\n\t"
    "v_mfma_f32_16x16x32_bf16 v[120:123], a[28:31], v[208:211], v[120:123]\n\t"
    "v_exp_f32 v110, v110\n\t"
    "v_mfma_f32_16x16x32_bf16 v[124:127], a[16:19], v[212:215], v[160:163]\n\t"
    "ds_read_b64_tr_b16 a[54:55], v189 offset:28672\n\t"
    "v_cvt_pk_bf16_f32 v132, v108, v109\n\t"
    "v_mfma_f32_16x16x32_bf16 v[124:127], a[20:23], v[216:219], v[124:127]\n\t"
    "v_exp_f32 v111, v111\n\t"
    "v_mfma_f32_16x16x32_bf16 v[124:127], a[24:27], v[220:223], v[124:127]\n\t"
    "ds_read_b64_tr_b16 a[56:57], v190 offset:24576\n\t"
    "ds_read_b64_tr_b16 a[58:59], v190 offset:28672\n\t"
    "v_mfma_f32_16x16x32_bf16 v[124:127], a[28:31], v[224:227], v[124:127]\n\t"
    "v_exp_f32 v112, v112\n\t"
    "v_mfma_f32_16x16x32_bf16 v[128:131], a[16:19], v[228:231], v[164:167]\n\t"
    "ds_read_b64_tr_b16 a[60:61], v191 offset:24576\n\t"
    "v_cvt_pk_bf16_f32 v133, v110, v111\n\t"
    "v_mfma_f32_16x16x32_bf16 v[128:131], a[20:23], v[232:235], v[128:131]\n\t"
    "ds_read_b64_tr_b16 a[62:63], v191 offset:28672\n\t"
    "ds_read_b128 a[0:3], v172 offset:16384\n\t"
    "v_mfma_f32_16x16x32_bf16 v[128:131], a[24:27], v[236:239], v[128:131]\n\t"
    "v_exp_f32 v113, v113\n\t"
    "v_mfma_f32_16x16x32_bf16 v[128:131], a[28:31], v[240:243], v[128:131]\n\t"
    "v_exp_f32 v114, v114\n\t"
    "v_mfma_f32_16x16x32_bf16 v[96:99], a[240:243], v[144:147], v[96:99]\n\t"
    "ds_read_b128 a[4:7], v173 offset:16384\n\t"
    "v_cvt_pk_bf16_f32 v136, v112, v113\n\t"
    "v_mfma_f32_16x16x32_bf16 v[96:99], a[244:247], v[148:151], v[96:99]\n\t"
    "Lx5%=:\n\t"
    "s_sub_u32 s75, s75, 2\n\t"
    "s_cbranch_scc0 Lx5%=\n\t"
    "v_mfma_f32_16x16x32_bf16 v[96:99], a[248:251], v[152:155], v[96:99]\n\t"
    "s_waitcnt lgkmcnt(2)\n\t"
    "v_exp_f32 v115, v115\n\t"
    "v_mfma_f32_16x16x32_bf16 v[64:67], a[32:35], v[152:155], v[64:67]\n\t"
    "buffer_load_dwordx4 v193, s[40:43], s67 offen offset:1024 lds\n\t"
    "v_mfma_f32_16x16x32_bf16 v[32:35], a[32:35], v[148:151], v[32:35]\n\t"
    "v_exp_f32 v116, v116\n\t"
    "v_mfma_f32_16x16x32_bf16 v[0:3], a[32:35], v[144:147], v[0:3]\n\t"
    "ds_read_b128 a[8:11], v174 offset:16384\n\t"
    "v_cvt_pk_bf16_f32 v137, v114, v115\n\t"
    "v_mfma_f32_16x16x32_bf16 v[4:7], a[36:39], v[144:147], v[4:7]\n\t"
    "v_exp_f32 v117, v117\n\t"
    "v_mfma_f32_16x16x32_bf16 v[36:39], a[36:39], v[148:151], v[36:39]\n\t"
    "v_exp_f32 v118, v118\n\t"
    "v_mfma_f32_16x16x32_bf16 v[68:71], a[36:39], v[152:155], v[68:71]\n\t"
    "ds_read_b128 a[12:15], v175 offset:16384\n\t"
    "v_cvt_pk_bf16_f32 v140, v116, v117\n\t"
    "v_mfma_f32_16x16x32_bf16 v[72:75], a[40:43], v[152:155], v[72:75]\n\t"
    "v_exp_f32 v119, v119\n\t"
    "v_mfma_f32_16x16x32_bf16 v[40:43], a[40:43], v[148:151], v[40:43]\n\t"
    "s_add_u32 m0, s68, 0x12000\n\t"
    "v_exp_f32 v120, v120\n\t"
    "v_mfma_f32_16x16x32_bf16 v[8:11], a[40:43], v[144:147], v[8:11]\n\t"
    "ds_read_b128 a[16:19], v172 offset:20480\n\t"
    "v_cvt_pk_bf16_f32 v141, v118, v119\n\t"
    "v_mfma_f32_16x16x32_bf16 v[12:15], a[44:47], v[144:147], v[12:15]\n\t"
    "buffer_load_dwordx4 v194, s[44:47], s67 offen lds\n\t"
    "v_mfma_f32_16x16x32_bf16 v[44:47], a[44:47], v[148:151], v[44:47]\n\t"
    "v_exp_f32 v121, v121\n\t"
    "v_mfma_f32_16x16x32_bf16 v[76:79], a[44:47], v[152:155], v[76:79]\n\t"
    "s_memrealtime s[70:71]\n\t"
    "v_exp_f32 v122, v122\n\t"
    "v_mfma_f32_16x16x32_bf16 v[80:83], a[48:51], v[152:155], v[80:83]\n\t"
    "ds_read_b128 a[20:23], v173 offset:20480\n\t"
    "v_cvt_pk_bf16_f32 v134, v120, v121\n\t"
    "v_mfma_f32_16x16x32_bf16 v[48:51], a[48:51], v[148:151], v[48:51]\n\t"
    "v_exp_f32 v123, v123\n\t"
    "v_mfma_f32_16x16x32_bf16 v[16:19], a[48:51], v[144:147], v[16:19]\n\t"
    "v_exp_f32 v124, v124\n\t"
    "v_mfma_f32_16x16x32_bf16 v[20:23], a[52:55], v[144:147], v[20:23]\n\t"
    "ds_read_b128 a[24:27], v174 offset:20480\n\t"
    "v_cvt_pk_bf16_f32 v135, v122, v123\n\t"
    "v_mfma_f32_16x16x32_bf16 v[52:55], a[52:55], v[148:151], v[52:55]\n\t"
    "v_exp_f32 v125, v125\n\t"
    "v_mfma_f32_16x16x32_bf16 v[84:87], a[52:55], v[152:155], v[84:87]\n\t"
    "v_exp_f32 v126, v126\n\t"
    "v_mfma_f32_16x16x32_bf16 v[88:91], a[56:59], v[152:155], v[88:91]\n\t"
    "ds_read_b128 a[28:31], v175 offset:20480\n\t"
    "v_cvt_pk_bf16_f32 v138, v124, v125\n\t"
    "v_mfma_f32_16x16x32_bf16 v[56:59], a[56:59], v[148:151], v[56:59]\n\t"
    "buffer_load_dwordx4 v195, s[44:47], s67 offen offset:1024 lds\n\t"
    "v_mfma_f32_16x16x32_bf16 v[24:27], a[56:59], v[144:147], v[24:27]\n\t"
    "s_add_u32 s67, s67, 0x2000\n\t"
    "v_mfma_f32_16x16x32_bf16 v[28:31], a[60:63], v[144:147], v[28:31]\n\t"
    "v_mfma_f32_16x16x32_bf16 v[60:63], a[60:63], v[148:151], v[60:63]\n\t"
    "s_waitcnt vmcnt(12) lgkmcnt(0)\n\t"
    "v_mfma_f32_16x16x32_bf16 v[92:95], a[60:63], v[152:155], v[92:95]\n\t"
    "s_barrier\n\t"
    "v_mfma_f32_16x16x32_bf16 v[108:111], a[0:3], v[196:199], v[156:159]\n\t"
    "ds_read_b64_tr_b16 a[32:33], v184 offset:40960\n\t"
    "ds_read_b64_tr_b16 a[34:35], v184 offset:45056\n\t"
    "v_mfma_f32_16x16x32_bf16 v[108:111], a[4:7], v[200:203], v[108:111]\n\t"
    "s_sub_u32 s75, s39, s70\n\t"
    "v_exp_f32 v127, v127\n\t"
    "v_mfma_f32_16x16x32_bf16 v[108:111], a[8:11], v[204:207], v[108:111]\n\t"
    "s_max_i32 s39, s75, 0\n\t"
    "v_exp_f32 v128, v128\n\t"
    "v_mfma_f32_16x16x32_bf16 v[108:111], a[12:15], v[208:211], v[108:111]\n\t"
    "s_add_u32 m0, s68, 0x14000\n\t"
    "ds_read_b64_tr_b16 a[36:37], v185 offset:40960\n\t"
    "v_cvt_pk_bf16_f32 v139, v126, v127\n\t"
    "v_mfma_f32_16x16x32_bf16 v[112:115], a[0:3], v[212:215], v[160:163]\n\t"
    "buffer_load_dwordx4 v192, s[40:43], s67 offen lds\n\t"
    "v_mfma_f32_16x16x32_bf16 v[112:115], a[4:7], v[216:219], v[112:115]\n\t"
    "s_add_u32 s39, s39, s70\n\t"
    "v_exp_f32 v129, v129\n\t"
    "v_mfma_f32_16x16x32_bf16 v[112:115], a[8:11], v[220:223], v[112:115]\n\t"
    "s_max_i32 s75, s75, 0\n\t"
    "v_exp_f32 v130, v130\n\t"
    "v_mfma_f32_16x16x32_bf16 v[112:115], a[12:15], v[224:227], v[112:115]\n\t"
    "ds_read_b64_tr_b16 a[38:39], v185 offset:45056\n\t"
    "v_cvt_pk_bf16_f32 v142, v128, v129\n\t"
    "v_mfma_f32_16x16x32_bf16 v[116:119], a[0:3], v[228:231], v[164:167]\n\t"
    "s_add_u32 s39, s39, s19\n\t"
    "v_exp_f32 v131, v131\n\t"
    "v_mfma_f32_16x16x32_bf16 v[116:119], a[4:7], v[232:235], v[116:119]\n\t"
    "ds_read_b64_tr_b16 a[40:41], v186 offset:40960\n\t"
    "v_cvt_pk_bf16_f32 v143, v130, v131\n\t"
    "v_mfma_f32_16x16x32_bf16 v[116:119], a[8:11], v[236:239], v[116:119]\n\t"
    "ds_read_b64_tr_b16 a[42:43], v186 offset:45056\n\t"
    "ds_read_b64_tr_b16 a[44:45], v187 offset:40960\n\t"
    "v_mfma_f32_16x16x32_bf16 v[116:119], a[12:15], v[240:243], v[116:119]\n\t"
    "v_exp_f32 v108, v108\n\t"
    "v_mfma_f32_16x16x32_bf16 v[120:123], a[16:19], v[196:199], v[156:159]\n\t"
    "ds_read_b64_tr_b16 a[46:47], v187 offset:45056\n\t"
    "ds_read_b64_tr_b16 a[48:49], v188 offset:40960\n\t"
    "v_mfma_f32_16x16x32_bf16 v[120:123], a[20:23], v[200:203], v[120:123]\n\t"
    "v_exp_f32 v109, v109\n\t"
    "v_mfma_f32_16x16x32_bf16 v[120:123], a[24:27], v[204:207], v[120:123]\n\t"
    "ds_read_b64_tr_b16 a[50:51], v188 offset:45056\n\t"
    "ds_read_b64_tr_b16 a[52:53], v189 offset:40960\n\t"
    "v_mfma_f32_16x16x32_bf16 v[120:123], a[28:31], v[208:211], v[120:123]\n\t"
    "v_exp_f32 v110, v110\n\t"
    "v_mfma_f32_16x16x32_bf16 v[124:127], a[16:19], v[212:215], v[160:163]\n\t"
    "ds_read_b64_tr_b16 a[54:55], v189 offset:45056\n\t"
    "v_cvt_pk_bf16_f32 v144, v108, v109\n\t"
    "v_mfma_f32_16x16x32_bf16 v[124:127], a[20:23], v[216:219], v[124:127]\n\t"
    "v_exp_f32 v111, v111\n\t"
    "v_mfma_f32_16x16x32_bf16 v[124:127], a[24:27], v[220:223], v[124:127]\n\t"
    "ds_read_b64_tr_b16 a[56:57], v190 offset:40960\n\t"
    "ds_read_b64_tr_b16 a[58:59], v190 offset:45056\n\t"
    "v_mfma_f32_16x16x32_bf16 v[124:127], a[28:31], v[224:227], v[124:127]\n\t"
    "v_exp_f32 v112, v112\n\t"
    "v_mfma_f32_16x16x32_bf16 v[128:131], a[16:19], v[228:231], v[164:167]\n\t"
    "ds_read_b64_tr_b16 a[60:61], v191 offset:40960\n\t"
    "v_cvt_pk_bf16_f32 v145, v110, v111\n\t"
    "v_mfma_f32_16x16x32_bf16 v[128:131], a[20:23], v[232:235], v[128:131]\n\t"
    "ds_read_b64_tr_b16 a[62:63], v191 offset:45056\n\t"
    "ds_read_b128 a[0:3], v172 offset:32768\n\t"
    "v_mfma_f32_16x16x32_bf16 v[128:131], a[24:27], v[236:239], v[128:131]\n\t"
    "v_exp_f32 v113, v113\n\t"
    "v_mfma_f32_16x16x32_bf16 v[128:131], a[28:31], v[240:243], v[128:131]\n\t"
    "v_exp_f32 v114, v114\n\t"
    "v_mfma_f32_16x16x32_bf16 v[96:99], a[240:243], v[132:135], v[96:99]\n\t"
    "ds_read_b128 a[4:7], v173 offset:32768\n\t"
    "v_cvt_pk_bf16_f32 v148, v112, v113\n\t"
    "v_mfma_f32_16x16x32_bf16 v[96:99], a[244:247], v[136:139], v[96:99]\n\t"
    "Lx6%=:\n\t"
    "s_sub_u32 s75, s75, 2\n\t"
    "s_cbranch_scc0 Lx6%=\n\t"
    "v_mfma_f32_16x16x32_bf16 v[96:99], a[248:251], v[140:143], v[96:99]\n\t"
    "s_waitcnt lgkmcnt(2)\n\t"
    "v_exp_f32 v115, v115\n\t"
    "v_mfma_f32_16x16x32_bf16 v[64:67], a[32:35], v[140:143], v[64:67]\n\t"
    "buffer_load_dwordx4 v193, s[40:43], s67 offen offset:1024 lds\n\t"
    "v_mfma_f32_16x16x32_bf16 v[32:35], a[32:35], v[136:139], v[32:35]\n\t"
    "v_exp_f32 v116, v116\n\t"
    "v_mfma_f32_16x16x32_bf16 v[0:3], a[32:35], v[132:135], v[0:3]\n\t"
    "ds_read_b128 a[8:11], v174 offset:32768\n\t"
    "v_cvt_pk_bf16_f32 v149, v114, v115\n\t"
    "v_mfma_f32_16x16x32_bf16 v[4:7], a[36:39], v[132:135], v[4:7]\n\t"
    "v_exp_f32 v117, v117\n\t"
    "v_mfma_f32_16x16x32_bf16 v[36:39], a[36:39], v[136:139], v[36:39]\n\t"
    "v_exp_f32 v118, v118\n\t"
    "v_mfma_f32_16x16x32_bf16 v[68:71], a[36:39], v[140:143], v[68:71]\n\t"
    "ds_read_b128 a[12:15], v175 offset:32768\n\t"
    "v_cvt_pk_bf16_f32 v152, v116, v117\n\t"
    "v_mfma_f32_16x16x32_bf16 v[72:75], a[40:43], v[140:143], v[72:75]\n\t"
    "v_exp_f32 v119, v119\n\t"
    "v_mfma_f32_16x16x32_bf16 v[40:43], a[40:43], v[136:139], v[40:43]\n\t"
    "s_add_u32 m0, s68, 0x16000\n\t"
    "v_exp_f32 v120, v120\n\t"
    "v_mfma_f32_16x16x32_bf16 v[8:11], a[40:43], v[132:135], v[8:11]\n\t"
    "ds_read_b128 a[16:19], v172 offset:36864\n\t"
    "v_cvt_pk_bf16_f32 v153, v118, v119\n\t"
    "v_mfma_f32_16x16x32_bf16 v[12:15], a[44:47], v[132:135], v[12:15]\n\t"
    "buffer_load_dwordx4 v194, s[44:47], s67 offen lds\n\t"
    "v_mfma_f32_16x16x32_bf16 v[44:47], a[44:47], v[136:139], v[44:47]\n\t"
    "v_exp_f32 v121, v121\n\t"
    "v_mfma_f32_16x16x32_bf16 v[76:79], a[44:47], v[140:143], v[76:79]\n\t"
    "s_memrealtime s[70:71]\n\t"
    "v_exp_f32 v122, v122\n\t"
    "v_mfma_f32_16x16x32_bf16 v[80:83], a[48:51], v[140:143], v[80:83]\n\t"
    "ds_read_b128 a[20:23], v173 offset:36864\n\t"
    "v_cvt_pk_bf16_f32 v146, v120, v121\n\t"
    "v_mfma_f32_16x16x32_bf16 v[48:51], a[48:51], v[136:139], v[48:51]\n\t"
    "v_exp_f32 v123, v123\n\t"
    "v_mfma_f32_16x16x32_bf16 v[16:19], a[48:51], v[132:135], v[16:19]\n\t"
    "v_exp_f32 v124, v124\n\t"
    "v_mfma_f32_16x16x32_bf16 v[20:23], a[52:55], v[132:135], v[20:23]\n\t"
    "ds_read_b128 a[24:27], v174 offset:36864\n\t"
    "v_cvt_pk_bf16_f32 v147, v122, v123\n\t"
    "v_mfma_f32_16x16x32_bf16 v[52:55], a[52:55], v[136:139], v[52:55]\n\t"
    "v_exp_f32 v125, v125\n\t"
    "v_mfma_f32_16x16x32_bf16 v[84:87], a[52:55], v[140:143], v[84:87]\n\t"
    "v_exp_f32 v126, v126\n\t"
    "v_mfma_f32_16x16x32_bf16 v[88:91], a[56:59], v[140:143], v[88:91]\n\t"
    "ds_read_b128 a[28:31], v175 offset:36864\n\t"
    "v_cvt_pk_bf16_f32 v150, v124, v125\n\t"
    "v_mfma_f32_16x16x32_bf16 v[56:59], a[56:59], v[136:139], v[56:59]\n\t"
    "buffer_load_dwordx4 v195, s[44:47], s67 offen offset:1024 lds\n\t"
    "v_mfma_f32_16x16x32_bf16 v[24:27], a[56:59], v[132:135], v[24:27]\n\t"
    "s_add_u32 s67, s67, 0x2000\n\t"
    "v_mfma_f32_16x16x32_bf16 v[28:31], a[60:63], v[132:135], v[28:31]\n\t"
    "v_mfma_f32_16x16x32_bf16 v[60:63], a[60:63], v[136:139], v[60:63]\n\t"
    "s_waitcnt vmcnt(12) lgkmcnt(0)\n\t"
    "v_mfma_f32_16x16x32_bf16 v[92:95], a[60:63], v[140:143], v[92:95]\n\t"
    "s_setpc_b64 s[36:37]\n\t"
    "Lq1%=:\n\t"
    "s_waitcnt vmcnt(8) lgkmcnt(0)\n\t"
    "s_barrier\n\t"
    "v_mfma_f32_16x16x32_bf16 v[0:3], a[64:67], v[196:199], 0\n\t"
    "s_add_u32 m0, s68, 0x26000\n\t"
    "ds_read_b64_tr_b16 a[96:97], v144 offset:8192\n\t"
    "v_mfma_f32_16x16x32_bf16 v[32:35], a[64:67], v[200:203], 0\n\t"
    "ds_read_b64_tr_b16 a[98:99], v144 offset:9216\n\t"
    "ds_read_b64_tr_b16 a[100:101], v145 offset:8192\n\t"
    "v_mfma_f32_16x16x32_bf16 v[64:67], a[64:67], v[204:207], 0\n\t"
    "buffer_load_dwordx4 v140, s[48:51], s67 offen lds\n\t"
    "v_mfma_f32_16x16x32_bf16 v[68:71], a[68:71], v[204:207], 0\n\t"
    "ds_read_b64_tr_b16 a[102:103], v145 offset:9216\n\t"
    "ds_read_b64_tr_b16 a[104:105], v146 offset:8192\n\t"
    "v_mfma_f32_16x16x32_bf16 v[36:39], a[68:71], v[200:203], 0\n\t"
    "buffer_load_dwordx4 v141, s[48:51], s67 offen offset:1024 lds\n\t"
    "v_mfma_f32_16x16x32_bf16 v[4:7], a[68:71], v[196:199], 0\n\t"
    "s_add_u32 s67, s67, 0x40000\n\t"
    "s_cmp_eq_u32 s67, 0x500000\n\t"
    "s_cselect_b32 s67, 0, s67\n\t"
    "v_mfma_f32_16x16x32_bf16 v[8:11], a[72:75], v[196:199], 0\n\t"
    "s_add_u32 m0, s68, 0x10000\n\t"
    "ds_read_b64_tr_b16 a[106:107], v146 offset:9216\n\t"
    "ds_read_b64_tr_b16 a[108:109], v147 offset:8192\n\t"
    "v_mfma_f32_16x16x32_bf16 v[40:43], a[72:75], v[200:203], 0\n\t"
    "buffer_load_dwordx4 v132, s[36:39], s69 offen lds\n\t"
    "v_mfma_f32_16x16x32_bf16 v[72:75], a[72:75], v[204:207], 0\n\t"
    "ds_read_b64_tr_b16 a[110:111], v147 offset:9216\n\t"
    "ds_read_b64_tr_b16 a[112:113], v148 offset:8192\n\t"
    "v_mfma_f32_16x16x32_bf16 v[76:79], a[76:79], v[204:207], 0\n\t"
    "ds_read_b64_tr_b16 a[114:115], v148 offset:9216\n\t"
    "ds_read_b64_tr_b16 a[116:117], v149 offset:8192\n\t"
    "v_mfma_f32_16x16x32_bf16 v[44:47], a[76:79], v[200:203], 0\n\t"
    "buffer_load_dwordx4 v133, s[36:39], s69 offen offset:1024 lds\n\t"
    "v_mfma_f32_16x16x32_bf16 v[12:15], a[76:79], v[196:199], 0\n\t"
    "s_add_u32 m0, s68, 0x20000\n\t"
    "ds_read_b64_tr_b16 a[118:119], v149 offset:9216\n\t"
    "ds_read_b64_tr_b16 a[120:121], v150 offset:8192\n\t"
    "v_mfma_f32_16x16x32_bf16 v[16:19], a[80:83], v[196:199], 0\n\t"
    "ds_read_b64_tr_b16 a[122:123], v150 offset:9216\n\t"
    "ds_read_b64_tr_b16 a[124:125], v151 offset:8192\n\t"
    "v_mfma_f32_16x16x32_bf16 v[48:51], a[80:83], v[200:203], 0\n\t"
    "buffer_load_dwordx4 v140, s[48:51], s67 offen lds\n\t"
    "v_mfma_f32_16x16x32_bf16 v[80:83], a[80:83], v[204:207], 0\n\t"
    "ds_read_b64_tr_b16 a[126:127], v151 offset:9216\n\t"
    "v_mfma_f32_16x16x32_bf16 v[84:87], a[84:87], v[204:207], 0\n\t"
    "buffer_load_dwordx4 v141, s[48:51], s67 offen offset:1024 lds\n\t"
    "v_mfma_f32_16x16x32_bf16 v[52:55], a[84:87], v[200:203], 0\n\t"
    "s_add_u32 s67, s67, 0x40000\n\t"
    "s_add_u32 m0, s68, 0x12000\n\t"
    "v_mfma_f32_16x16x32_bf16 v[20:23], a[84:87], v[196:199], 0\n\t"
    "s_waitcnt vmcnt(12)\n\t"
    "ds_read_b128 v[220:223], v138 offset:24576\n\t"
    "ds_read_b128 v[232:235], v139 offset:24576\n\t"
    "v_mfma_f32_16x16x32_bf16 v[24:27], a[88:91], v[196:199], 0\n\t"
    "buffer_load_dwordx4 v134, s[36:39], s69 offen lds\n\t"
    "v_mfma_f32_16x16x32_bf16 v[56:59], a[88:91], v[200:203], 0\n\t"
    "v_mfma_f32_16x16x32_bf16 v[88:91], a[88:91], v[204:207], 0\n\t"
    "v_mfma_f32_16x16x32_bf16 v[92:95], a[92:95], v[204:207], 0\n\t"
    "buffer_load_dwordx4 v135, s[36:39], s69 offen offset:1024 lds\n\t"
    "v_mfma_f32_16x16x32_bf16 v[60:63], a[92:95], v[200:203], 0\n\t"
    "v_mfma_f32_16x16x32_bf16 v[28:31], a[92:95], v[196:199], 0\n\t"
    "s_waitcnt vmcnt(12) lgkmcnt(2)\n\t"
    "s_barrier\n\t"
    "v_mfma_f32_16x16x32_bf16 v[0:3], a[96:99], v[208:211], v[0:3]\n\t"
    "s_add_u32 m0, s68, 0x14000\n\t"
    "ds_read_b64_tr_b16 a[64:65], v144 offset:16384\n\t"
    "v_mfma_f32_16x16x32_bf16 v[32:35], a[96:99], v[212:215], v[32:35]\n\t"
    "ds_read_b64_tr_b16 a[66:67], v144 offset:17408\n\t"
    "v_mfma_f32_16x16x32_bf16 v[64:67], a[96:99], v[216:219], v[64:67]\n\t"
    "buffer_load_dwordx4 v136, s[36:39], s69 offen lds\n\t"
    "v_mfma_f32_16x16x32_bf16 v[68:71], a[100:103], v[216:219], v[68:71]\n\t"
    "ds_read_b64_tr_b16 a[68:69], v145 offset:16384\n\t"
    "ds_read_b64_tr_b16 a[70:71], v145 offset:17408\n\t"
    "v_mfma_f32_16x16x32_bf16 v[36:39], a[100:103], v[212:215], v[36:39]\n\t"
    "ds_read_b64_tr_b16 a[72:73], v146 offset:16384\n\t"
    "ds_read_b64_tr_b16 a[74:75], v146 offset:17408\n\t"
    "v_mfma_f32_16x16x32_bf16 v[4:7], a[100:103], v[208:211], v[4:7]\n\t"
    "buffer_load_dwordx4 v137, s[36:39], s69 offen offset:1024 lds\n\t"
    "v_mfma_f32_16x16x32_bf16 v[8:11], a[104:107], v[208:211], v[8:11]\n\t"
    "s_add_u32 s69, s69, 0x80\n\t"
    "ds_read_b64_tr_b16 a[76:77], v147 offset:16384\n\t"
    "ds_read_b64_tr_b16 a[78:79], v147 offset:17408\n\t"
    "v_mfma_f32_16x16x32_bf16 v[40:43], a[104:107], v[212:215], v[40:43]\n\t"
    "s_cmp_eq_u32 s69, 0x500\n\t"
    "s_cselect_b32 s69, 0, s69\n\t"
    "s_add_u32 m0, s68, 0x22000\n\t"
    "v_mfma_f32_16x16x32_bf16 v[72:75], a[104:107], v[216:219], v[72:75]\n\t"
    "ds_read_b64_tr_b16 a[80:81], v148 offset:16384\n\t"
    "ds_read_b64_tr_b16 a[82:83], v148 offset:17408\n\t"
    "v_mfma_f32_16x16x32_bf16 v[76:79], a[108:111], v[216:219], v[76:79]\n\t"
    "buffer_load_dwordx4 v140, s[48:51], s67 offen lds\n\t"
    "v_mfma_f32_16x16x32_bf16 v[44:47], a[108:111], v[212:215], v[44:47]\n\t"
    "ds_read_b64_tr_b16 a[84:85], v149 offset:16384\n\t"
    "ds_read_b64_tr_b16 a[86:87], v149 offset:17408\n\t"
    "v_mfma_f32_16x16x32_bf16 v[12:15], a[108:111], v[208:211], v[12:15]\n\t"
    "ds_read_b64_tr_b16 a[88:89], v150 offset:16384\n\t"
    "ds_read_b64_tr_b16 a[90:91], v150 offset:17408\n\t"
    "v_mfma_f32_16x16x32_bf16 v[16:19], a[112:115], v[208:211], v[16:19]\n\t"
    "s_waitcnt lgkmcnt(14)\n\t"
    "ds_read_b64_tr_b16 a[92:93], v151 offset:16384\n\t"
    "ds_read_b64_tr_b16 a[94:95], v151 offset:17408\n\t"
    "v_mfma_f32_16x16x32_bf16 v[48:51], a[112:115], v[212:215], v[48:51]\n\t"
    "buffer_load_dwordx4 v141, s[48:51], s67 offen offset:1024 lds\n\t"
    "v_mfma_f32_16x16x32_bf16 v[80:83], a[112:115], v[216:219], v[80:83]\n\t"
    "s_add_u32 s67, s67, 0x40000\n\t"
    "s_cmp_eq_u32 s67, 0x500000\n\t"
    "s_cselect_b32 s67, 0, s67\n\t"
    "v_mfma_f32_16x16x32_bf16 v[84:87], a[116:119], v[216:219], v[84:87]\n\t"
    "s_add_u32 m0, s68, 0x16000\n\t"
    "v_mfma_f32_16x16x32_bf16 v[52:55], a[116:119], v[212:215], v[52:55]\n\t"
    "s_waitcnt vmcnt(14)\n\t"
    "buffer_load_dwordx4 v132, s[36:39], s69 offen lds\n\t"
    "v_mfma_f32_16x16x32_bf16 v[20:23], a[116:119], v[208:211], v[20:23]\n\t"
    "ds_read_b128 v[224:227], v138 offset:40960\n\t"
    "ds_read_b128 v[236:239], v139 offset:40960\n\t"
    "v_mfma_f32_16x16x32_bf16 v[24:27], a[120:123], v[208:211], v[24:27]\n\t"
    "s_waitcnt vmcnt(13)\n\t"
    "ds_read_b128 v[228:231], v138 offset:57344\n\t"
    "ds_read_b128 v[240:243], v139 offset:57344\n\t"
    "v_mfma_f32_16x16x32_bf16 v[56:59], a[120:123], v[212:215], v[56:59]\n\t"
    "v_mfma_f32_16x16x32_bf16 v[88:91], a[120:123], v[216:219], v[88:91]\n\t"
    "buffer_load_dwordx4 v133, s[36:39], s69 offen offset:1024 lds\n\t"
    "v_mfma_f32_16x16x32_bf16 v[92:95], a[124:127], v[216:219], v[92:95]\n\t"
    "v_mfma_f32_16x16x32_bf16 v[60:63], a[124:127], v[212:215], v[60:63]\n\t"
    "v_mfma_f32_16x16x32_bf16 v[28:31], a[124:127], v[208:211], v[28:31]\n\t"
    "s_waitcnt vmcnt(12) lgkmcnt(4)\n\t"
    "s_barrier\n\t"
    "v_mfma_f32_16x16x32_bf16 v[0:3], a[64:67], v[220:223], v[0:3]\n\t"
    "s_waitcnt lgkmcnt(3)\n\t"
    "s_add_u32 m0, s68, 0x24000\n\t"
    "ds_read_b64_tr_b16 a[96:97], v144 offset:24576\n\t"
    "v_mfma_f32_16x16x32_bf16 v[32:35], a[64:67], v[224:227], v[32:35]\n\t"
    "s_waitcnt lgkmcnt(2)\n\t"
    "ds_read_b64_tr_b16 a[98:99], v144 offset:25600\n\t"
    "ds_read_b64_tr_b16 a[100:101], v145 offset:24576\n\t"
    "v_mfma_f32_16x16x32_bf16 v[64:67], a[64:67], v[228:231], v[64:67]\n\t"
    "buffer_load_dwordx4 v140, s[48:51], s67 offen lds\n\t"
    "v_mfma_f32_16x16x32_bf16 v[68:71], a[68:71], v[228:231], v[68:71]\n\t"
    "ds_read_b64_tr_b16 a[102:103], v145 offset:25600\n\t"
    "ds_read_b64_tr_b16 a[104:105], v146 offset:24576\n\t"
    "v_mfma_f32_16x16x32_bf16 v[36:39], a[68:71], v[224:227], v[36:39]\n\t"
    "ds_read_b64_tr_b16 a[106:107], v146 offset:25600\n\t"
    "ds_read_b64_tr_b16 a[108:109], v147 offset:24576\n\t"
    "v_mfma_f32_16x16x32_bf16 v[4:7], a[68:71], v[220:223], v[4:7]\n\t"
    "buffer_load_dwordx4 v141, s[48:51], s67 offen offset:1024 lds\n\t"
    "v_mfma_f32_16x16x32_bf16 v[8:11], a[72:75], v[220:223], v[8:11]\n\t"
    "s_add_u32 s67, s67, 0x40000\n\t"
    "ds_read_b64_tr_b16 a[110:111], v147 offset:25600\n\t"
    "ds_read_b64_tr_b16 a[112:113], v148 offset:24576\n\t"
    "v_mfma_f32_16x16x32_bf16 v[40:43], a[72:75], v[224:227], v[40:43]\n\t"
    "s_add_u32 m0, s68, 0x1a000\n\t"
    "ds_read_b64_tr_b16 a[114:115], v148 offset:25600\n\t"
    "ds_read_b64_tr_b16 a[116:117], v149 offset:24576\n\t"
    "v_mfma_f32_16x16x32_bf16 v[72:75], a[72:75], v[228:231], v[72:75]\n\t"
    "ds_read_b64_tr_b16 a[118:119], v149 offset:25600\n\t"
    "v_mfma_f32_16x16x32_bf16 v[76:79], a[76:79], v[228:231], v[76:79]\n\t"
    "buffer_load_dwordx4 v134, s[36:39], s69 offen lds\n\t"
    "v_mfma_f32_16x16x32_bf16 v[44:47], a[76:79], v[224:227], v[44:47]\n\t"
    "ds_read_b64_tr_b16 a[120:121], v150 offset:24576\n\t"
    "ds_read_b64_tr_b16 a[122:123], v150 offset:25600\n\t"
    "v_mfma_f32_16x16x32_bf16 v[12:15], a[76:79], v[220:223], v[12:15]\n\t"
    "ds_read_b64_tr_b16 a[124:125], v151 offset:24576\n\t"
    "v_mfma_f32_16x16x32_bf16 v[16:19], a[80:83], v[220:223], v[16:19]\n\t"
    "s_waitcnt lgkmcnt(15)\n\t"
    "ds_read_b64_tr_b16 a[126:127], v151 offset:25600\n\t"
    "v_mfma_f32_16x16x32_bf16 v[48:51], a[80:83], v[224:227], v[48:51]\n\t"
    "buffer_load_dwordx4 v135, s[36:39], s69 offen offset:1024 lds\n\t"
    "v_mfma_f32_16x16x32_bf16 v[80:83], a[80:83], v[228:231], v[80:83]\n\t"
    "s_add_u32 m0, s68, 0x1e000\n\t"
    "v_mfma_f32_16x16x32_bf16 v[84:87], a[84:87], v[228:231], v[84:87]\n\t"
    "v_mfma_f32_16x16x32_bf16 v[52:55], a[84:87], v[224:227], v[52:55]\n\t"
    "s_waitcnt vmcnt(14)\n\t"
    "buffer_load_dwordx4 v136, s[36:39], s69 offen lds\n\t"
    "v_mfma_f32_16x16x32_bf16 v[20:23], a[84:87], v[220:223], v[20:23]\n\t"
    "ds_read_b128 v[196:199], v138\n\t"
    "ds_read_b128 v[208:211], v139\n\t"
    "v_mfma_f32_16x16x32_bf16 v[24:27], a[88:91], v[220:223], v[24:27]\n\t"
    "v_mfma_f32_16x16x32_bf16 v[56:59], a[88:91], v[224:227], v[56:59]\n\t"
    "v_mfma_f32_16x16x32_bf16 v[88:91], a[88:91], v[228:231], v[88:91]\n\t"
    "buffer_load_dwordx4 v137, s[36:39], s69 offen offset:1024 lds\n\t"
    "v_mfma_f32_16x16x32_bf16 v[92:95], a[92:95], v[228:231], v[92:95]\n\t"
    "s_add_u32 s69, s69, 0x80\n\t"
    "s_cmp_eq_u32 s69, 0x500\n\t"
    "s_cselect_b32 s69, 0, s69\n\t"
    "v_mfma_f32_16x16x32_bf16 v[60:63], a[92:95], v[224:227], v[60:63]\n\t"
    "v_mfma_f32_16x16x32_bf16 v[28:31], a[92:95], v[220:223], v[28:31]\n\t"
    "s_waitcnt vmcnt(14) lgkmcnt(2)\n\t"
    "s_barrier\n\t"
    "v_mfma_f32_16x16x32_bf16 v[0:3], a[96:99], v[232:235], v[0:3]\n\t"
    "s_add_u32 m0, s68, 0x26000\n\t"
    "ds_read_b64_tr_b16 a[64:65], v144\n\t"
    "v_mfma_f32_16x16x32_bf16 v[32:35], a[96:99], v[236:239], v[32:35]\n\t"
    "ds_read_b64_tr_b16 a[66:67], v144 offset:1024\n\t"
    "ds_read_b64_tr_b16 a[68:69], v145\n\t"
    "v_mfma_f32_16x16x32_bf16 v[64:67], a[96:99], v[240:243], v[64:67]\n\t"
    "buffer_load_dwordx4 v140, s[48:51], s67 offen lds\n\t"
    "v_mfma_f32_16x16x32_bf16 v[68:71], a[100:103], v[240:243], v[68:71]\n\t"
    "ds_read_b64_tr_b16 a[70:71], v145 offset:1024\n\t"
    "ds_read_b64_tr_b16 a[72:73], v146\n\t"
    "v_mfma_f32_16x16x32_bf16 v[36:39], a[100:103], v[236:239], v[36:39]\n\t"
    "ds_read_b64_tr_b16 a[74:75], v146 offset:1024\n\t"
    "ds_read_b64_tr_b16 a[76:77], v147\n\t"
    "v_mfma_f32_16x16x32_bf16 v[4:7], a[100:103], v[232:235], v[4:7]\n\t"
    "ds_read_b64_tr_b16 a[78:79], v147 offset:1024\n\t"
    "ds_read_b64_tr_b16 a[80:81], v148\n\t"
    "v_mfma_f32_16x16x32_bf16 v[8:11], a[104:107], v[232:235], v[8:11]\n\t"
    "ds_read_b64_tr_b16 a[82:83], v148 offset:1024\n\t"
    "v_mfma_f32_16x16x32_bf16 v[40:43], a[104:107], v[236:239], v[40:43]\n\t"
    "buffer_load_dwordx4 v141, s[48:51], s67 offen offset:1024 lds\n\t"
    "v_mfma_f32_16x16x32_bf16 v[72:75], a[104:107], v[240:243], v[72:75]\n\t"
    "s_add_u32 s67, s67, 0x40000\n\t"
    "ds_read_b64_tr_b16 a[84:85], v149\n\t"
    "ds_read_b64_tr_b16 a[86:87], v149 offset:1024\n\t"
    "v_mfma_f32_16x16x32_bf16 v[76:79], a[108:111], v[240:243], v[76:79]\n\t"
    "s_cmp_eq_u32 s67, 0x500000\n\t"
    "ds_read_b64_tr_b16 a[88:89], v150\n\t"
    "v_mfma_f32_16x16x32_bf16 v[44:47], a[108:111], v[236:239], v[44:47]\n\t"
    "s_cselect_b32 s67, 0, s67\n\t"
    "ds_read_b64_tr_b16 a[90:91], v150 offset:1024\n\t"
    "v_mfma_f32_16x16x32_bf16 v[12:15], a[108:111], v[232:235], v[12:15]\n\t"
    "s_add_u32 m0, s68, 0x10000\n\t"
    "ds_read_b64_tr_b16 a[92:93], v151\n\t"
    "v_mfma_f32_16x16x32_bf16 v[16:19], a[112:115], v[232:235], v[16:19]\n\t"
    "s_waitcnt lgkmcnt(15)\n\t"
    "ds_read_b64_tr_b16 a[94:95], v151 offset:1024\n\t"
    "v_mfma_f32_16x16x32_bf16 v[48:51], a[112:115], v[236:239], v[48:51]\n\t"
    "buffer_load_dwordx4 v132, s[36:39], s69 offen lds\n\t"
    "v_mfma_f32_16x16x32_bf16 v[80:83], a[112:115], v[240:243], v[80:83]\n\t"
    "v_mfma_f32_16x16x32_bf16 v[84:87], a[116:119], v[240:243], v[84:87]\n\t"
    "v_mfma_f32_16x16x32_bf16 v[52:55], a[116:119], v[236:239], v[52:55]\n\t"
    "s_waitcnt vmcnt(15)\n\t"
    "ds_read_b128 v[200:203], v138 offset:8192\n\t"
    "ds_read_b128 v[212:215], v139 offset:8192\n\t"
    "v_mfma_f32_16x16x32_bf16 v[20:23], a[116:119], v[232:235], v[20:23]\n\t"
    "v_mfma_f32_16x16x32_bf16 v[24:27], a[120:123], v[232:235], v[24:27]\n\t"
    "s_waitcnt vmcnt(13)\n\t"
    "buffer_load_dwordx4 v133, s[36:39], s69 offen offset:1024 lds\n\t"
    "v_mfma_f32_16x16x32_bf16 v[56:59], a[120:123], v[236:239], v[56:59]\n\t"
    "ds_read_b128 v[204:207], v138 offset:16384\n\t"
    "ds_read_b128 v[216:219], v139 offset:16384\n\t"
    "v_mfma_f32_16x16x32_bf16 v[88:91], a[120:123], v[240:243], v[88:91]\n\t"
    "v_mfma_f32_16x16x32_bf16 v[92:95], a[124:127], v[240:243], v[92:95]\n\t"
    "v_mfma_f32_16x16x32_bf16 v[60:63], a[124:127], v[236:239], v[60:63]\n\t"
    "v_mfma_f32_16x16x32_bf16 v[28:31], a[124:127], v[232:235], v[28:31]\n\t"
    "s_waitcnt vmcnt(12) lgkmcnt(4)\n\t"
    "s_barrier\n\t"
    "v_mfma_f32_16x16x32_bf16 v[0:3], a[64:67], v[196:199], v[0:3]\n\t"
    "s_waitcnt lgkmcnt(3)\n\t"
    "s_add_u32 m0, s68, 0x20000\n\t"
    "ds_read_b64_tr_b16 a[96:97], v144 offset:8192\n\t"
    "v_mfma_f32_16x16x32_bf16 v[32:35], a[64:67], v[200:203], v[32:35]\n\t"
    "s_waitcnt lgkmcnt(2)\n\t"
    "ds_read_b64_tr_b16 a[98:99], v144 offset:9216\n\t"
    "ds_read_b64_tr_b16 a[100:101], v145 offset:8192\n\t"
    "v_mfma_f32_16x16x32_bf16 v[64:67], a[64:67], v[204:207], v[64:67]\n\t"
    "buffer_load_dwordx4 v140, s[48:51], s67 offen lds\n\t"
    "v_mfma_f32_16x16x32_bf16 v[68:71], a[68:71], v[204:207], v[68:71]\n\t"
    "buffer_load_dwordx4 v141, s[48:51], s67 offen offset:1024 lds\n\t"
    "v_mfma_f32_16x16x32_bf16 v[36:39], a[68:71], v[200:203], v[36:39]\n\t"
    "s_add_u32 s67, s67, 0x40000\n\t"
    "s_add_u32 m0, s68, 0x12000\n\t"
    "v_mfma_f32_16x16x32_bf16 v[4:7], a[68:71], v[196:199], v[4:7]\n\t"
    "s_waitcnt lgkmcnt(3)\n\t"
    "buffer_load_dwordx4 v134, s[36:39], s69 offen lds\n\t"
    "v_mfma_f32_16x16x32_bf16 v[8:11], a[72:75], v[196:199], v[8:11]\n\t"
    "buffer_load_dwordx4 v135, s[36:39], s69 offen offset:1024 lds\n\t"
    "v_mfma_f32_16x16x32_bf16 v[40:43], a[72:75], v[200:203], v[40:43]\n\t"
    "s_add_u32 m0, s68, 0x14000\n\t"
    "ds_read_b64_tr_b16 a[102:103], v145 offset:9216\n\t"
    "ds_read_b64_tr_b16 a[104:105], v146 offset:8192\n\t"
    "v_mfma_f32_16x16x32_bf16 v[72:75], a[72:75], v[204:207], v[72:75]\n\t"
    "buffer_load_dwordx4 v136, s[36:39], s69 offen lds\n\t"
    "v_mfma_f32_16x16x32_bf16 v[76:79], a[76:79], v[204:207], v[76:79]\n\t"
    "ds_read_b64_tr_b16 a[106:107], v146 offset:9216\n\t"
    "ds_read_b64_tr_b16 a[108:109], v147 offset:8192\n\t"
    "v_mfma_f32_16x16x32_bf16 v[44:47], a[76:79], v[200:203], v[44:47]\n\t"
    "buffer_load_dwordx4 v137, s[36:39], s69 offen offset:1024 lds\n\t"
    "v_mfma_f32_16x16x32_bf16 v[12:15], a[76:79], v[196:199], v[12:15]\n\t"
    "s_add_u32 s69, s69, 0x80\n\t"
    "s_cmp_eq_u32 s69, 0x500\n\t"
    "s_cselect_b32 s69, 0, s69\n\t"
    "s_add_u32 m0, s68, 0x18000\n\t"
    "v_mfma_f32_16x16x32_bf16 v[16:19], a[80:83], v[196:199], v[16:19]\n\t"
    "s_mov_b32 s74, 0x2000\n\t"
    "ds_read_b64_tr_b16 a[110:111], v147 offset:9216\n\t"
    "buffer_load_dwordx4 v192, s[40:43], 0 offen nt lds\n\t"
    "v_mfma_f32_16x16x32_bf16 v[48:51], a[80:83], v[200:203], v[48:51]\n\t"
    "ds_read_b64_tr_b16 a[112:113], v148 offset:8192\n\t"
    "ds_read_b64_tr_b16 a[114:115], v148 offset:9216\n\t"
    "v_mfma_f32_16x16x32_bf16 v[80:83], a[80:83], v[204:207], v[80:83]\n\t"
    "buffer_load_dwordx4 v193, s[40:43], 0 offen offset:1024 nt lds\n\t"
    "s_add_u32 m0, s68, 0x1c000\n\t"
    "v_mfma_f32_16x16x32_bf16 v[84:87], a[84:87], v[204:207], v[84:87]\n\t"
    "ds_read_b64_tr_b16 a[116:117], v149 offset:8192\n\t"
    "buffer_load_dwordx4 v192, s[40:43], s74 offen nt lds\n\t"
    "v_mfma_f32_16x16x32_bf16 v[52:55], a[84:87], v[200:203], v[52:55]\n\t"
    "s_waitcnt vmcnt(19)\n\t"
    "ds_read_b64_tr_b16 a[118:119], v149 offset:9216\n\t"
    "ds_read_b64_tr_b16 a[120:121], v150 offset:8192\n\t"
    "v_mfma_f32_16x16x32_bf16 v[20:23], a[84:87], v[196:199], v[20:23]\n\t"
    "buffer_load_dwordx4 v193, s[40:43], s74 offen offset:1024 nt lds\n\t"
    "s_add_u32 m0, s68, 0x2000\n\t"
    "v_mfma_f32_16x16x32_bf16 v[24:27], a[88:91], v[196:199], v[24:27]\n\t"
    "s_mov_b32 s74, 0x4000\n\t"
    "ds_read_b64_tr_b16 a[122:123], v150 offset:9216\n\t"
    "buffer_load_dwordx4 v194, s[44:47], 0 offen nt lds\n\t"
    "v_mfma_f32_16x16x32_bf16 v[56:59], a[88:91], v[200:203], v[56:59]\n\t"
    "ds_read_b64_tr_b16 a[124:125], v151 offset:8192\n\t"
    "ds_read_b64_tr_b16 a[126:127], v151 offset:9216\n\t"
    "v_mfma_f32_16x16x32_bf16 v[88:91], a[88:91], v[204:207], v[88:91]\n\t"
    "buffer_load_dwordx4 v195, s[44:47], 0 offen offset:1024 nt lds\n\t"
    "s_add_u32 m0, s68, 0\n\t"
    "v_mfma_f32_16x16x32_bf16 v[92:95], a[92:95], v[204:207], v[92:95]\n\t"
    "ds_read_b128 v[220:223], v138 offset:24576\n\t"
    "buffer_load_dwordx4 v192, s[40:43], s74 offen nt lds\n\t"
    "v_mfma_f32_16x16x32_bf16 v[60:63], a[92:95], v[200:203], v[60:63]\n\t"
    "ds_read_b128 v[232:235], v139 offset:24576\n\t"
    "buffer_load_dwordx4 v193, s[40:43], s74 offen offset:1024 nt lds\n\t"
    "v_mfma_f32_16x16x32_bf16 v[28:31], a[92:95], v[196:199], v[28:31]\n\t"
    "s_waitcnt vmcnt(22) lgkmcnt(2)\n\t"
    "s_barrier\n\t"
    "v_mfma_f32_16x16x32_bf16 v[0:3], a[96:99], v[208:211], v[0:3]\n\t"
    "s_add_u32 m0, s68, 0x22000\n\t"
    "ds_read_b64_tr_b16 a[64:65], v144 offset:16384\n\t"
    "v_mfma_f32_16x16x32_bf16 v[32:35], a[96:99], v[212:215], v[32:35]\n\t"
    "ds_read_b64_tr_b16 a[66:67], v144 offset:17408\n\t"
    "ds_read_b64_tr_b16 a[68:69], v145 offset:16384\n\t"
    "v_mfma_f32_16x16x32_bf16 v[64:67], a[96:99], v[216:219], v[64:67]\n\t"
    "buffer_load_dwordx4 v140, s[48:51], s67 offen lds\n\t"
    "v_mfma_f32_16x16x32_bf16 v[68:71], a[100:103], v[216:219], v[68:71]\n\t"
    "ds_read_b64_tr_b16 a[70:71], v145 offset:17408\n\t"
    "ds_read_b64_tr_b16 a[72:73], v146 offset:16384\n\t"
    "v_mfma_f32_16x16x32_bf16 v[36:39], a[100:103], v[212:215], v[36:39]\n\t"
    "ds_read_b64_tr_b16 a[74:75], v146 offset:17408\n\t"
    "ds_read_b64_tr_b16 a[76:77], v147 offset:16384\n\t"
    "v_mfma_f32_16x16x32_bf16 v[4:7], a[100:103], v[208:211], v[4:7]\n\t"
    "ds_read_b64_tr_b16 a[78:79], v147 offset:17408\n\t"
    "ds_read_b64_tr_b16 a[80:81], v148 offset:16384\n\t"
    "v_mfma_f32_16x16x32_bf16 v[8:11], a[104:107], v[208:211], v[8:11]\n\t"
    "ds_read_b64_tr_b16 a[82:83], v148 offset:17408\n\t"
    "v_mfma_f32_16x16x32_bf16 v[40:43], a[104:107], v[212:215], v[40:43]\n\t"
    "buffer_load_dwordx4 v141, s[48:51], s67 offen offset:1024 lds\n\t"
    "v_mfma_f32_16x16x32_bf16 v[72:75], a[104:107], v[216:219], v[72:75]\n\t"
    "s_add_u32 s67, s67, 0x40000\n\t"
    "ds_read_b64_tr_b16 a[84:85], v149 offset:16384\n\t"
    "ds_read_b64_tr_b16 a[86:87], v149 offset:17408\n\t"
    "v_mfma_f32_16x16x32_bf16 v[76:79], a[108:111], v[216:219], v[76:79]\n\t"
    "s_cmp_eq_u32 s67, 0x500000\n\t"
    "ds_read_b64_tr_b16 a[88:89], v150 offset:16384\n\t"
    "v_mfma_f32_16x16x32_bf16 v[44:47], a[108:111], v[212:215], v[44:47]\n\t"
    "s_cselect_b32 s67, 0, s67\n\t"
    "ds_read_b64_tr_b16 a[90:91], v150 offset:17408\n\t"
    "v_mfma_f32_16x16x32_bf16 v[12:15], a[108:111], v[208:211], v[12:15]\n\t"
    "s_add_u32 m0, s68, 0x16000\n\t"
    "ds_read_b64_tr_b16 a[92:93], v151 offset:16384\n\t"
    "v_mfma_f32_16x16x32_bf16 v[16:19], a[112:115], v[208:211], v[16:19]\n\t"
    "s_waitcnt lgkmcnt(15)\n\t"
    "ds_read_b64_tr_b16 a[94:95], v151 offset:17408\n\t"
    "v_mfma_f32_16x16x32_bf16 v[48:51], a[112:115], v[212:215], v[48:51]\n\t"
    "buffer_load_dwordx4 v132, s[36:39], s69 offen lds\n\t"
    "v_mfma_f32_16x16x32_bf16 v[80:83], a[112:115], v[216:219], v[80:83]\n\t"
    "v_mfma_f32_16x16x32_bf16 v[84:87], a[116:119], v[216:219], v[84:87]\n\t"
    "v_mfma_f32_16x16x32_bf16 v[52:55], a[116:119], v[212:215], v[52:55]\n\t"
    "s_waitcnt vmcnt(23)\n\t"
    "ds_read_b128 v[224:227], v138 offset:40960\n\t"
    "ds_read_b128 v[236:239], v139 offset:40960\n\t"
    "v_mfma_f32_16x16x32_bf16 v[20:23], a[116:119], v[208:211], v[20:23]\n\t"
    "v_mfma_f32_16x16x32_bf16 v[24:27], a[120:123], v[208:211], v[24:27]\n\t"
    "s_waitcnt vmcnt(21)\n\t"
    "buffer_load_dwordx4 v133, s[36:39], s69 offen offset:1024 lds\n\t"
    "v_mfma_f32_16x16x32_bf16 v[56:59], a[120:123], v[212:215], v[56:59]\n\t"
    "ds_read_b128 v[228:231], v138 offset:57344\n\t"
    "ds_read_b128 v[240:243], v139 offset:57344\n\t"
    "v_mfma_f32_16x16x32_bf16 v[88:91], a[120:123], v[216:219], v[88:91]\n\t"
    "v_mfma_f32_16x16x32_bf16 v[92:95], a[124:127], v[216:219], v[92:95]\n\t"
    "v_mfma_f32_16x16x32_bf16 v[60:63], a[124:127], v[212:215], v[60:63]\n\t"
    "v_mfma_f32_16x16x32_bf16 v[28:31], a[124:127], v[208:211], v[28:31]\n\t"
    "s_waitcnt vmcnt(20) lgkmcnt(4)\n\t"
    "s_barrier\n\t"
    "v_mfma_f32_16x16x32_bf16 v[0:3], a[64:67], v[220:223], v[0:3]\n\t"
    "s_waitcnt lgkmcnt(3)\n\t"
    "s_add_u32 m0, s68, 0x24000\n\t"
    "ds_read_b64_tr_b16 a[96:97], v144 offset:24576\n\t"
    "v_mfma_f32_16x16x32_bf16 v[32:35], a[64:67], v[224:227], v[32:35]\n\t"
    "s_waitcnt lgkmcnt(2)\n\t"
    "ds_read_b64_tr_b16 a[98:99], v144 offset:25600\n\t"
    "ds_read_b64_tr_b16 a[100:101], v145 offset:24576\n\t"
    "v_mfma_f32_16x16x32_bf16 v[64:67], a[64:67], v[228:231], v[64:67]\n\t"
    "buffer_load_dwordx4 v140, s[48:51], s67 offen lds\n\t"
    "v_mfma_f32_16x16x32_bf16 v[68:71], a[68:71], v[228:231], v[68:71]\n\t"
    "ds_read_b64_tr_b16 a[102:103], v145 offset:25600\n\t"
    "ds_read_b64_tr_b16 a[104:105], v146 offset:24576\n\t"
    "v_mfma_f32_16x16x32_bf16 v[36:39], a[68:71], v[224:227], v[36:39]\n\t"
    "ds_read_b64_tr_b16 a[106:107], v146 offset:25600\n\t"
    "ds_read_b64_tr_b16 a[108:109], v147 offset:24576\n\t"
    "v_mfma_f32_16x16x32_bf16 v[4:7], a[68:71], v[220:223], v[4:7]\n\t"
    "buffer_load_dwordx4 v141, s[48:51], s67 offen offset:1024 lds\n\t"
    "v_mfma_f32_16x16x32_bf16 v[8:11], a[72:75], v[220:223], v[8:11]\n\t"
    "s_add_u32 s67, s67, 0x40000\n\t"
    "ds_read_b64_tr_b16 a[110:111], v147 offset:25600\n\t"
    "ds_read_b64_tr_b16 a[112:113], v148 offset:24576\n\t"
    "v_mfma_f32_16x16x32_bf16 v[40:43], a[72:75], v[224:227], v[40:43]\n\t"
    "s_add_u32 m0, s68, 0x1a000\n\t"
    "ds_read_b64_tr_b16 a[114:115], v148 offset:25600\n\t"
    "ds_read_b64_tr_b16 a[116:117], v149 offset:24576\n\t"
    "v_mfma_f32_16x16x32_bf16 v[72:75], a[72:75], v[228:231], v[72:75]\n\t"
    "ds_read_b64_tr_b16 a[118:119], v149 offset:25600\n\t"
    "v_mfma_f32_16x16x32_bf16 v[76:79], a[76:79], v[228:231], v[76:79]\n\t"
    "buffer_load_dwordx4 v134, s[36:39], s69 offen lds\n\t"
    "v_mfma_f32_16x16x32_bf16 v[44:47], a[76:79], v[224:227], v[44:47]\n\t"
    "ds_read_b64_tr_b16 a[120:121], v150 offset:24576\n\t"
    "ds_read_b64_tr_b16 a[122:123], v150 offset:25600\n\t"
    "v_mfma_f32_16x16x32_bf16 v[12:15], a[76:79], v[220:223], v[12:15]\n\t"
    "ds_read_b64_tr_b16 a[124:125], v151 offset:24576\n\t"
    "v_mfma_f32_16x16x32_bf16 v[16:19], a[80:83], v[220:223], v[16:19]\n\t"
    "s_waitcnt lgkmcnt(15)\n\t"
    "ds_read_b64_tr_b16 a[126:127], v151 offset:25600\n\t"
    "v_mfma_f32_16x16x32_bf16 v[48:51], a[80:83], v[224:227], v[48:51]\n\t"
    "buffer_load_dwordx4 v135, s[36:39], s69 offen offset:1024 lds\n\t"
    "v_mfma_f32_16x16x32_bf16 v[80:83], a[80:83], v[228:231], v[80:83]\n\t"
    "s_add_u32 m0, s68, 0x1e000\n\t"
    "v_mfma_f32_16x16x32_bf16 v[84:87], a[84:87], v[228:231], v[84:87]\n\t"
    "v_mfma_f32_16x16x32_bf16 v[52:55], a[84:87], v[224:227], v[52:55]\n\t"
    "s_waitcnt vmcnt(22)\n\t"
    "buffer_load_dwordx4 v136, s[36:39], s69 offen lds\n\t"
    "v_mfma_f32_16x16x32_bf16 v[20:23], a[84:87], v[220:223], v[20:23]\n\t"
    "ds_read_b128 v[196:199], v138\n\t"
    "ds_read_b128 v[208:211], v139\n\t"
    "v_mfma_f32_16x16x32_bf16 v[24:27], a[88:91], v[220:223], v[24:27]\n\t"
    "v_mfma_f32_16x16x32_bf16 v[56:59], a[88:91], v[224:227], v[56:59]\n\t"
    "v_mfma_f32_16x16x32_bf16 v[88:91], a[88:91], v[228:231], v[88:91]\n\t"
    "buffer_load_dwordx4 v137, s[36:39], s69 offen offset:1024 lds\n\t"
    "v_mfma_f32_16x16x32_bf16 v[92:95], a[92:95], v[228:231], v[92:95]\n\t"
    "s_add_u32 s69, s69, 0x80\n\t"
    "s_cmp_eq_u32 s69, 0x500\n\t"
    "s_cselect_b32 s69, 0, s69\n\t"
    "v_mfma_f32_16x16x32_bf16 v[60:63], a[92:95], v[224:227], v[60:63]\n\t"
    "v_mfma_f32_16x16x32_bf16 v[28:31], a[92:95], v[220:223], v[28:31]\n\t"
    "s_waitcnt vmcnt(22) lgkmcnt(2)\n\t"
    "s_barrier\n\t"
    "v_mfma_f32_16x16x32_bf16 v[0:3], a[96:99], v[232:235], v[0:3]\n\t"
    "s_add_u32 m0, s68, 0x26000\n\t"
    "ds_read_b64_tr_b16 a[64:65], v144\n\t"
    "v_mfma_f32_16x16x32_bf16 v[32:35], a[96:99], v[236:239], v[32:35]\n\t"
    "ds_read_b64_tr_b16 a[66:67], v144 offset:1024\n\t"
    "ds_read_b64_tr_b16 a[68:69], v145\n\t"
    "v_mfma_f32_16x16x32_bf16 v[64:67], a[96:99], v[240:243], v[64:67]\n\t"
    "buffer_load_dwordx4 v140, s[48:51], s67 offen lds\n\t"
    "v_mfma_f32_16x16x32_bf16 v[68:71], a[100:103], v[240:243], v[68:71]\n\t"
    "ds_read_b64_tr_b16 a[70:71], v145 offset:1024\n\t"
    "ds_read_b64_tr_b16 a[72:73], v146\n\t"
    "v_mfma_f32_16x16x32_bf16 v[36:39], a[100:103], v[236:239], v[36:39]\n\t"
    "ds_read_b64_tr_b16 a[74:75], v146 offset:1024\n\t"
    "ds_read_b64_tr_b16 a[76:77], v147\n\t"
    "v_mfma_f32_16x16x32_bf16 v[4:7], a[100:103], v[232:235], v[4:7]\n\t"
    "ds_read_b64_tr_b16 a[78:79], v147 offset:1024\n\t"
    "ds_read_b64_tr_b16 a[80:81], v148\n\t"
    "v_mfma_f32_16x16x32_bf16 v[8:11], a[104:107], v[232:235], v[8:11]\n\t"
    "ds_read_b64_tr_b16 a[82:83], v148 offset:1024\n\t"
    "v_mfma_f32_16x16x32_bf16 v[40:43], a[104:107], v[236:239], v[40:43]\n\t"
    "buffer_load_dwordx4 v141, s[48:51], s67 offen offset:1024 lds\n\t"
    "v_mfma_f32_16x16x32_bf16 v[72:75], a[104:107], v[240:243], v[72:75]\n\t"
    "s_add_u32 s67, s67, 0x40000\n\t"
    "ds_read_b64_tr_b16 a[84:85], v149\n\t"
    "ds_read_b64_tr_b16 a[86:87], v149 offset:1024\n\t"
    "v_mfma_f32_16x16x32_bf16 v[76:79], a[108:111], v[240:243], v[76:79]\n\t"
    "s_cmp_eq_u32 s67, 0x500000\n\t"
    "ds_read_b64_tr_b16 a[88:89], v150\n\t"
    "v_mfma_f32_16x16x32_bf16 v[44:47], a[108:111], v[236:239], v[44:47]\n\t"
    "s_cselect_b32 s67, 0, s67\n\t"
    "ds_read_b64_tr_b16 a[90:91], v150 offset:1024\n\t"
    "v_mfma_f32_16x16x32_bf16 v[12:15], a[108:111], v[232:235], v[12:15]\n\t"
    "s_add_u32 m0, s68, 0x10000\n\t"
    "ds_read_b64_tr_b16 a[92:93], v151\n\t"
    "v_mfma_f32_16x16x32_bf16 v[16:19], a[112:115], v[232:235], v[16:19]\n\t"
    "s_waitcnt lgkmcnt(15)\n\t"
    "ds_read_b64_tr_b16 a[94:95], v151 offset:1024\n\t"
    "v_mfma_f32_16x16x32_bf16 v[48:51], a[112:115], v[236:239], v[48:51]\n\t"
    "buffer_load_dwordx4 v132, s[36:39], s69 offen lds\n\t"
    "v_mfma_f32_16x16x32_bf16 v[80:83], a[112:115], v[240:243], v[80:83]\n\t"
    "v_mfma_f32_16x16x32_bf16 v[84:87], a[116:119], v[240:243], v[84:87]\n\t"
    "v_mfma_f32_16x16x32_bf16 v[52:55], a[116:119], v[236:239], v[52:55]\n\t"
    "s_waitcnt vmcnt(23)\n\t"
    "ds_read_b128 v[200:203], v138 offset:8192\n\t"
    "ds_read_b128 v[212:215], v139 offset:8192\n\t"
    "v_mfma_f32_16x16x32_bf16 v[20:23], a[116:119], v[232:235], v[20:23]\n\t"
    "v_mfma_f32_16x16x32_bf16 v[24:27], a[120:123], v[232:235], v[24:27]\n\t"
    "s_waitcnt vmcnt(21)\n\t"
    "buffer_load_dwordx4 v133, s[36:39], s69 offen offset:1024 lds\n\t"
    "v_mfma_f32_16x16x32_bf16 v[56:59], a[120:123], v[236:239], v[56:59]\n\t"
    "ds_read_b128 v[204:207], v138 offset:16384\n\t"
    "ds_read_b128 v[216:219], v139 offset:16384\n\t"
    "v_mfma_f32_16x16x32_bf16 v[88:91], a[120:123], v[240:243], v[88:91]\n\t"
    "v_mfma_f32_16x16x32_bf16 v[92:95], a[124:127], v[240:243], v[92:95]\n\t"
    "v_mfma_f32_16x16x32_bf16 v[60:63], a[124:127], v[236:239], v[60:63]\n\t"
    "v_mfma_f32_16x16x32_bf16 v[28:31], a[124:127], v[232:235], v[28:31]\n\t"
    "s_waitcnt vmcnt(12) lgkmcnt(4)\n\t"
    "s_barrier\n\t"
    "v_mfma_f32_16x16x32_bf16 v[0:3], a[64:67], v[196:199], v[0:3]\n\t"
    "s_waitcnt lgkmcnt(3)\n\t"
    "s_add_u32 m0, s68, 0x20000\n\t"
    "ds_read_b64_tr_b16 a[96:97], v144 offset:8192\n\t"
    "v_mfma_f32_16x16x32_bf16 v[32:35], a[64:67], v[200:203], v[32:35]\n\t"
    "s_waitcnt lgkmcnt(2)\n\t"
    "ds_read_b64_tr_b16 a[98:99], v144 offset:9216\n\t"
    "ds_read_b64_tr_b16 a[100:101], v145 offset:8192\n\t"
    "v_mfma_f32_16x16x32_bf16 v[64:67], a[64:67], v[204:207], v[64:67]\n\t"
    "buffer_load_dwordx4 v140, s[48:51], s67 offen lds\n\t"
    "v_mfma_f32_16x16x32_bf16 v[68:71], a[68:71], v[204:207], v[68:71]\n\t"
    "buffer_load_dwordx4 v141, s[48:51], s67 offen offset:1024 lds\n\t"
    "v_mfma_f32_16x16x32_bf16 v[36:39], a[68:71], v[200:203], v[36:39]\n\t"
    "s_add_u32 s67, s67, 0x40000\n\t"
    "s_add_u32 m0, s68, 0x12000\n\t"
    "v_mfma_f32_16x16x32_bf16 v[4:7], a[68:71], v[196:199], v[4:7]\n\t"
    "buffer_load_dwordx4 v134, s[36:39], s69 offen lds\n\t"
    "v_mfma_f32_16x16x32_bf16 v[8:11], a[72:75], v[196:199], v[8:11]\n\t"
    "s_waitcnt lgkmcnt(3)\n\t"
    "ds_read_b64_tr_b16 a[102:103], v145 offset:9216\n\t"
    "ds_read_b64_tr_b16 a[104:105], v146 offset:8192\n\t"
    "v_mfma_f32_16x16x32_bf16 v[40:43], a[72:75], v[200:203], v[40:43]\n\t"
    "buffer_load_dwordx4 v135, s[36:39], s69 offen offset:1024 lds\n\t"
    "v_mfma_f32_16x16x32_bf16 v[72:75], a[72:75], v[204:207], v[72:75]\n\t"
    "s_add_u32 m0, s68, 0x14000\n\t"
    "ds_read_b64_tr_b16 a[106:107], v146 offset:9216\n\t"
    "ds_read_b64_tr_b16 a[108:109], v147 offset:8192\n\t"
    "v_mfma_f32_16x16x32_bf16 v[76:79], a[76:79], v[204:207], v[76:79]\n\t"
    "buffer_load_dwordx4 v136, s[36:39], s69 offen lds\n\t"
    "v_mfma_f32_16x16x32_bf16 v[44:47], a[76:79], v[200:203], v[44:47]\n\t"
    "s_mov_b32 s74, 0x2000\n\t"
    "ds_read_b64_tr_b16 a[110:111], v147 offset:9216\n\t"
    "ds_read_b64_tr_b16 a[112:113], v148 offset:8192\n\t"
    "v_mfma_f32_16x16x32_bf16 v[12:15], a[76:79], v[196:199], v[12:15]\n\t"
    "buffer_load_dwordx4 v137, s[36:39], s69 offen offset:1024 lds\n\t"
    "v_mfma_f32_16x16x32_bf16 v[16:19], a[80:83], v[196:199], v[16:19]\n\t"
    "s_add_u32 s69, s69, 0x80\n\t"
    "s_cmp_eq_u32 s69, 0x500\n\t"
    "s_cselect_b32 s69, 0, s69\n\t"
    "v_mfma_f32_16x16x32_bf16 v[48:51], a[80:83], v[200:203], v[48:51]\n\t"
    "s_add_u32 m0, s68, 0x6000\n\t"
    "ds_read_b64_tr_b16 a[114:115], v148 offset:9216\n\t"
    "ds_read_b64_tr_b16 a[116:117], v149 offset:8192\n\t"
    "v_mfma_f32_16x16x32_bf16 v[80:83], a[80:83], v[204:207], v[80:83]\n\t"
    "buffer_load_dwordx4 v194, s[44:47], s74 offen nt lds\n\t"
    "v_mfma_f32_16x16x32_bf16 v[84:87], a[84:87], v[204:207], v[84:87]\n\t"
    "buffer_load_dwordx4 v195, s[44:47], s74 offen offset:1024 nt lds\n\t"
    "v_mfma_f32_16x16x32_bf16 v[52:55], a[84:87], v[200:203], v[52:55]\n\t"
    "s_add_u32 m0, s68, 0x4000\n\t"
    "ds_read_b64_tr_b16 a[118:119], v149 offset:9216\n\t"
    "ds_read_b64_tr_b16 a[120:121], v150 offset:8192\n\t"
    "v_mfma_f32_16x16x32_bf16 v[20:23], a[84:87], v[196:199], v[20:23]\n\t"
    "s_mov_b32 s74, 0x6000\n\t"
    "buffer_load_dwordx4 v192, s[40:43], s74 offen nt lds\n\t"
    "v_mfma_f32_16x16x32_bf16 v[24:27], a[88:91], v[196:199], v[24:27]\n\t"
    "s_waitcnt vmcnt(19)\n\t"
    "ds_read_b64_tr_b16 a[122:123], v150 offset:9216\n\t"
    "ds_read_b64_tr_b16 a[124:125], v151 offset:8192\n\t"
    "v_mfma_f32_16x16x32_bf16 v[56:59], a[88:91], v[200:203], v[56:59]\n\t"
    "buffer_load_dwordx4 v193, s[40:43], s74 offen offset:1024 nt lds\n\t"
    "v_mfma_f32_16x16x32_bf16 v[88:91], a[88:91], v[204:207], v[88:91]\n\t"
    "s_add_u32 m0, s68, 0xa000\n\t"
    "ds_read_b64_tr_b16 a[126:127], v151 offset:9216\n\t"
    "ds_read_b128 v[220:223], v138 offset:24576\n\t"
    "v_mfma_f32_16x16x32_bf16 v[92:95], a[92:95], v[204:207], v[92:95]\n\t"
    "s_mov_b32 s74, 0x4000\n\t"
    "buffer_load_dwordx4 v194, s[44:47], s74 offen nt lds\n\t"
    "v_mfma_f32_16x16x32_bf16 v[60:63], a[92:95], v[200:203], v[60:63]\n\t"
    "ds_read_b128 v[232:235], v139 offset:24576\n\t"
    "buffer_load_dwordx4 v195, s[44:47], s74 offen offset:1024 nt lds\n\t"
    "v_mfma_f32_16x16x32_bf16 v[28:31], a[92:95], v[196:199], v[28:31]\n\t"
    "s_waitcnt vmcnt(20) lgkmcnt(2)\n\t"
    "s_barrier\n\t"
    "v_mfma_f32_16x16x32_bf16 v[0:3], a[96:99], v[208:211], v[0:3]\n\t"
    "s_add_u32 m0, s68, 0x22000\n\t"
    "ds_read_b64_tr_b16 a[64:65], v144 offset:16384\n\t"
    "v_mfma_f32_16x16x32_bf16 v[32:35], a[96:99], v[212:215], v[32:35]\n\t"
    "ds_read_b64_tr_b16 a[66:67], v144 offset:17408\n\t"
    "ds_read_b64_tr_b16 a[68:69], v145 offset:16384\n\t"
    "v_mfma_f32_16x16x32_bf16 v[64:67], a[96:99], v[216:219], v[64:67]\n\t"
    "buffer_load_dwordx4 v140, s[48:51], s67 offen lds\n\t"
    "v_mfma_f32_16x16x32_bf16 v[68:71], a[100:103], v[216:219], v[68:71]\n\t"
    "ds_read_b64_tr_b16 a[70:71], v145 offset:17408\n\t"
    "ds_read_b64_tr_b16 a[72:73], v146 offset:16384\n\t"
    "v_mfma_f32_16x16x32_bf16 v[36:39], a[100:103], v[212:215], v[36:39]\n\t"
    "ds_read_b64_tr_b16 a[74:75], v146 offset:17408\n\t"
    "ds_read_b64_tr_b16 a[76:77], v147 offset:16384\n\t"
    "v_mfma_f32_16x16x32_bf16 v[4:7], a[100:103], v[208:211], v[4:7]\n\t"
    "ds_read_b64_tr_b16 a[78:79], v147 offset:17408\n\t"
    "ds_read_b64_tr_b16 a[80:81], v148 offset:16384\n\t"
    "v_mfma_f32_16x16x32_bf16 v[8:11], a[104:107], v[208:211], v[8:11]\n\t"
    "ds_read_b64_tr_b16 a[82:83], v148 offset:17408\n\t"
    "v_mfma_f32_16x16x32_bf16 v[40:43], a[104:107], v[212:215], v[40:43]\n\t"
    "buffer_load_dwordx4 v141, s[48:51], s67 offen offset:1024 lds\n\t"
    "v_mfma_f32_16x16x32_bf16 v[72:75], a[104:107], v[216:219], v[72:75]\n\t"
    "s_add_u32 s67, s67, 0x40000\n\t"
    "ds_read_b64_tr_b16 a[84:85], v149 offset:16384\n\t"
    "ds_read_b64_tr_b16 a[86:87], v149 offset:17408\n\t"
    "v_mfma_f32_16x16x32_bf16 v[76:79], a[108:111], v[216:219], v[76:79]\n\t"
    "s_cmp_eq_u32 s67, 0x500000\n\t"
    "ds_read_b64_tr_b16 a[88:89], v150 offset:16384\n\t"
    "v_mfma_f32_16x16x32_bf16 v[44:47], a[108:111], v[212:215], v[44:47]\n\t"
    "s_cselect_b32 s67, 0, s67\n\t"
    "ds_read_b64_tr_b16 a[90:91], v150 offset:17408\n\t"
    "v_mfma_f32_16x16x32_bf16 v[12:15], a[108:111], v[208:211], v[12:15]\n\t"
    "s_add_u32 m0, s68, 0x16000\n\t"
    "ds_read_b64_tr_b16 a[92:93], v151 offset:16384\n\t"
    "v_mfma_f32_16x16x32_bf16 v[16:19], a[112:115], v[208:211], v[16:19]\n\t"
    "s_waitcnt lgkmcnt(15)\n\t"
    "ds_read_b64_tr_b16 a[94:95], v151 offset:17408\n\t"
    "v_mfma_f32_16x16x32_bf16 v[48:51], a[112:115], v[212:215], v[48:51]\n\t"
    "buffer_load_dwordx4 v132, s[36:39], s69 offen lds\n\t"
    "v_mfma_f32_16x16x32_bf16 v[80:83], a[112:115], v[216:219], v[80:83]\n\t"
    "v_mfma_f32_16x16x32_bf16 v[84:87], a[116:119], v[216:219], v[84:87]\n\t"
    "v_mfma_f32_16x16x32_bf16 v[52:55], a[116:119], v[212:215], v[52:55]\n\t"
    "s_waitcnt vmcnt(21)\n\t"
    "ds_read_b128 v[224:227], v138 offset:40960\n\t"
    "ds_read_b128 v[236:239], v139 offset:40960\n\t"
    "v_mfma_f32_16x16x32_bf16 v[20:23], a[116:119], v[208:211], v[20:23]\n\t"
    "v_mfma_f32_16x16x32_bf16 v[24:27], a[120:123], v[208:211], v[24:27]\n\t"
    "s_waitcnt vmcnt(19)\n\t"
    "buffer_load_dwordx4 v133, s[36:39], s69 offen offset:1024 lds\n\t"
    "v_mfma_f32_16x16x32_bf16 v[56:59], a[120:123], v[212:215], v[56:59]\n\t"
    "ds_read_b128 v[228:231], v138 offset:57344\n\t"
    "ds_read_b128 v[240:243], v139 offset:57344\n\t"
    "v_mfma_f32_16x16x32_bf16 v[88:91], a[120:123], v[216:219], v[88:91]\n\t"
    "v_mfma_f32_16x16x32_bf16 v[92:95], a[124:127], v[216:219], v[92:95]\n\t"
    "v_mfma_f32_16x16x32_bf16 v[60:63], a[124:127], v[212:215], v[60:63]\n\t"
    "v_mfma_f32_16x16x32_bf16 v[28:31], a[124:127], v[208:211], v[28:31]\n\t"
    "s_waitcnt vmcnt(18) lgkmcnt(4)\n\t"
    "s_barrier\n\t"
    "v_mfma_f32_16x16x32_bf16 v[0:3], a[64:67], v[220:223], v[0:3]\n\t"
    "s_waitcnt lgkmcnt(3)\n\t"
    "s_add_u32 m0, s68, 0x24000\n\t"
    "ds_read_b64_tr_b16 a[96:97], v144 offset:24576\n\t"
    "v_mfma_f32_16x16x32_bf16 v[32:35], a[64:67], v[224:227], v[32:35]\n\t"
    "s_waitcnt lgkmcnt(2)\n\t"
    "ds_read_b64_tr_b16 a[98:99], v144 offset:25600\n\t"
    "ds_read_b64_tr_b16 a[100:101], v145 offset:24576\n\t"
    "v_mfma_f32_16x16x32_bf16 v[64:67], a[64:67], v[228:231], v[64:67]\n\t"
    "buffer_load_dwordx4 v140, s[48:51], s67 offen lds\n\t"
    "v_mfma_f32_16x16x32_bf16 v[68:71], a[68:71], v[228:231], v[68:71]\n\t"
    "ds_read_b64_tr_b16 a[102:103], v145 offset:25600\n\t"
    "ds_read_b64_tr_b16 a[104:105], v146 offset:24576\n\t"
    "v_mfma_f32_16x16x32_bf16 v[36:39], a[68:71], v[224:227], v[36:39]\n\t"
    "ds_read_b64_tr_b16 a[106:107], v146 offset:25600\n\t"
    "ds_read_b64_tr_b16 a[108:109], v147 offset:24576\n\t"
    "v_mfma_f32_16x16x32_bf16 v[4:7], a[68:71], v[220:223], v[4:7]\n\t"
    "buffer_load_dwordx4 v141, s[48:51], s67 offen offset:1024 lds\n\t"
    "v_mfma_f32_16x16x32_bf16 v[8:11], a[72:75], v[220:223], v[8:11]\n\t"
    "s_add_u32 s67, s67, 0x40000\n\t"
    "ds_read_b64_tr_b16 a[110:111], v147 offset:25600\n\t"
    "ds_read_b64_tr_b16 a[112:113], v148 offset:24576\n\t"
    "v_mfma_f32_16x16x32_bf16 v[40:43], a[72:75], v[224:227], v[40:43]\n\t"
    "s_add_u32 m0, s68, 0x1a000\n\t"
    "ds_read_b64_tr_b16 a[114:115], v148 offset:25600\n\t"
    "ds_read_b64_tr_b16 a[116:117], v149 offset:24576\n\t"
    "v_mfma_f32_16x16x32_bf16 v[72:75], a[72:75], v[228:231], v[72:75]\n\t"
    "ds_read_b64_tr_b16 a[118:119], v149 offset:25600\n\t"
    "v_mfma_f32_16x16x32_bf16 v[76:79], a[76:79], v[228:231], v[76:79]\n\t"
    "buffer_load_dwordx4 v134, s[36:39], s69 offen lds\n\t"
    "v_mfma_f32_16x16x32_bf16 v[44:47], a[76:79], v[224:227], v[44:47]\n\t"
    "ds_read_b64_tr_b16 a[120:121], v150 offset:24576\n\t"
    "ds_read_b64_tr_b16 a[122:123], v150 offset:25600\n\t"
    "v_mfma_f32_16x16x32_bf16 v[12:15], a[76:79], v[220:223], v[12:15]\n\t"
    "ds_read_b64_tr_b16 a[124:125], v151 offset:24576\n\t"
    "v_mfma_f32_16x16x32_bf16 v[16:19], a[80:83], v[220:223], v[16:19]\n\t"
    "s_waitcnt lgkmcnt(15)\n\t"
    "ds_read_b64_tr_b16 a[126:127], v151 offset:25600\n\t"
    "v_mfma_f32_16x16x32_bf16 v[48:51], a[80:83], v[224:227], v[48:51]\n\t"
    "buffer_load_dwordx4 v135, s[36:39], s69 offen offset:1024 lds\n\t"
    "v_mfma_f32_16x16x32_bf16 v[80:83], a[80:83], v[228:231], v[80:83]\n\t"
    "s_add_u32 m0, s68, 0x1e000\n\t"
    "v_mfma_f32_16x16x32_bf16 v[84:87], a[84:87], v[228:231], v[84:87]\n\t"
    "v_mfma_f32_16x16x32_bf16 v[52:55], a[84:87], v[224:227], v[52:55]\n\t"
    "s_waitcnt vmcnt(20)\n\t"
    "buffer_load_dwordx4 v136, s[36:39], s69 offen lds\n\t"
    "v_mfma_f32_16x16x32_bf16 v[20:23], a[84:87], v[220:223], v[20:23]\n\t"
    "ds_read_b128 v[196:199], v138\n\t"
    "ds_read_b128 v[208:211], v139\n\t"
    "v_mfma_f32_16x16x32_bf16 v[24:27], a[88:91], v[220:223], v[24:27]\n\t"
    "v_mfma_f32_16x16x32_bf16 v[56:59], a[88:91], v[224:227], v[56:59]\n\t"
    "v_mfma_f32_16x16x32_bf16 v[88:91], a[88:91], v[228:231], v[88:91]\n\t"
    "buffer_load_dwordx4 v137, s[36:39], s69 offen offset:1024 lds\n\t"
    "v_mfma_f32_16x16x32_bf16 v[92:95], a[92:95], v[228:231], v[92:95]\n\t"
    "s_add_u32 s69, s69, 0x80\n\t"
    "s_cmp_eq_u32 s69, 0x500\n\t"
    "s_cselect_b32 s69, 0, s69\n\t"
    "v_mfma_f32_16x16x32_bf16 v[60:63], a[92:95], v[224:227], v[60:63]\n\t"
    "v_mfma_f32_16x16x32_bf16 v[28:31], a[92:95], v[220:223], v[28:31]\n\t"
    "s_cmp_lg_u32 s64, 0\n\t"
    "s_cbranch_scc1 Luq100%=\n\t"
    "s_call_b64 s[22:23], Lc10%=\n\t"
    "Luq100%=:\n\t"
    "s_cmp_lg_u32 s64, 1\n\t"
    "s_cbranch_scc1 Luq101%=\n\t"
    "s_call_b64 s[22:23], Lc11%=\n\t"
    "Luq101%=:\n\t"
    "s_cmp_lg_u32 s64, 2\n\t"
    "s_cbranch_scc1 Luq102%=\n\t"
    "s_call_b64 s[22:23], Lc4%=\n\t"
    "Luq102%=:\n\t"
    "s_cmp_lg_u32 s64, 3\n\t"
    "s_cbranch_scc1 Luq103%=\n\t"
    "s_call_b64 s[22:23], Lc5%=\n\t"
    "Luq103%=:\n\t"
    "s_waitcnt vmcnt(20) lgkmcnt(2)\n\t"
    "s_barrier\n\t"
    "v_mfma_f32_16x16x32_bf16 v[0:3], a[96:99], v[232:235], v[0:3]\n\t"
    "s_add_u32 m0, s68, 0x26000\n\t"
    "ds_read_b64_tr_b16 a[64:65], v144\n\t"
    "v_mfma_f32_16x16x32_bf16 v[32:35], a[96:99], v[236:239], v[32:35]\n\t"
    "ds_read_b64_tr_b16 a[66:67], v144 offset:1024\n\t"
    "ds_read_b64_tr_b16 a[68:69], v145\n\t"
    "v_mfma_f32_16x16x32_bf16 v[64:67], a[96:99], v[240:243], v[64:67]\n\t"
    "buffer_load_dwordx4 v140, s[48:51], s67 offen lds\n\t"
    "v_mfma_f32_16x16x32_bf16 v[68:71], a[100:103], v[240:243], v[68:71]\n\t"
    "ds_read_b64_tr_b16 a[70:71], v145 offset:1024\n\t"
    "ds_read_b64_tr_b16 a[72:73], v146\n\t"
    "v_mfma_f32_16x16x32_bf16 v[36:39], a[100:103], v[236:239], v[36:39]\n\t"
    "ds_read_b64_tr_b16 a[74:75], v146 offset:1024\n\t"
    "ds_read_b64_tr_b16 a[76:77], v147\n\t"
    "v_mfma_f32_16x16x32_bf16 v[4:7], a[100:103], v[232:235], v[4:7]\n\t"
    "ds_read_b64_tr_b16 a[78:79], v147 offset:1024\n\t"
    "ds_read_b64_tr_b16 a[80:81], v148\n\t"
    "v_mfma_f32_16x16x32_bf16 v[8:11], a[104:107], v[232:235], v[8:11]\n\t"
    "ds_read_b64_tr_b16 a[82:83], v148 offset:1024\n\t"
    "v_mfma_f32_16x16x32_bf16 v[40:43], a[104:107], v[236:239], v[40:43]\n\t"
    "buffer_load_dwordx4 v141, s[48:51], s67 offen offset:1024 lds\n\t"
    "v_mfma_f32_16x16x32_bf16 v[72:75], a[104:107], v[240:243], v[72:75]\n\t"
    "s_add_u32 s67, s67, 0x40000\n\t"
    "ds_read_b64_tr_b16 a[84:85], v149\n\t"
    "ds_read_b64_tr_b16 a[86:87], v149 offset:1024\n\t"
    "v_mfma_f32_16x16x32_bf16 v[76:79], a[108:111], v[240:243], v[76:79]\n\t"
    "s_cmp_eq_u32 s67, 0x500000\n\t"
    "ds_read_b64_tr_b16 a[88:89], v150\n\t"
    "v_mfma_f32_16x16x32_bf16 v[44:47], a[108:111], v[236:239], v[44:47]\n\t"
    "s_cselect_b32 s67, 0, s67\n\t"
    "ds_read_b64_tr_b16 a[90:91], v150 offset:1024\n\t"
    "v_mfma_f32_16x16x32_bf16 v[12:15], a[108:111], v[232:235], v[12:15]\n\t"
    "s_add_u32 m0, s68, 0x10000\n\t"
    "ds_read_b64_tr_b16 a[92:93], v151\n\t"
    "v_mfma_f32_16x16x32_bf16 v[16:19], a[112:115], v[232:235], v[16:19]\n\t"
    "s_waitcnt lgkmcnt(15)\n\t"
    "ds_read_b64_tr_b16 a[94:95], v151 offset:1024\n\t"
    "v_mfma_f32_16x16x32_bf16 v[48:51], a[112:115], v[236:239], v[48:51]\n\t"
    "buffer_load_dwordx4 v132, s[36:39], s69 offen lds\n\t"
    "v_mfma_f32_16x16x32_bf16 v[80:83], a[112:115], v[240:243], v[80:83]\n\t"
    "v_mfma_f32_16x16x32_bf16 v[84:87], a[116:119], v[240:243], v[84:87]\n\t"
    "v_mfma_f32_16x16x32_bf16 v[52:55], a[116:119], v[236:239], v[52:55]\n\t"
    "s_waitcnt vmcnt(21)\n\t"
    "ds_read_b128 v[200:203], v138 offset:8192\n\t"
    "ds_read_b128 v[212:215], v139 offset:8192\n\t"
    "v_mfma_f32_16x16x32_bf16 v[20:23], a[116:119], v[232:235], v[20:23]\n\t"
    "v_mfma_f32_16x16x32_bf16 v[24:27], a[120:123], v[232:235], v[24:27]\n\t"
    "s_waitcnt vmcnt(19)\n\t"
    "buffer_load_dwordx4 v133, s[36:39], s69 offen offset:1024 lds\n\t"
    "v_mfma_f32_16x16x32_bf16 v[56:59], a[120:123], v[236:239], v[56:59]\n\t"
    "ds_read_b128 v[204:207], v138 offset:16384\n\t"
    "ds_read_b128 v[216:219], v139 offset:16384\n\t"
    "v_mfma_f32_16x16x32_bf16 v[88:91], a[120:123], v[240:243], v[88:91]\n\t"
    "v_mfma_f32_16x16x32_bf16 v[92:95], a[124:127], v[240:243], v[92:95]\n\t"
    "v_mfma_f32_16x16x32_bf16 v[60:63], a[124:127], v[236:239], v[60:63]\n\t"
    "v_mfma_f32_16x16x32_bf16 v[28:31], a[124:127], v[232:235], v[28:31]\n\t"
    "s_waitcnt vmcnt(12) lgkmcnt(4)\n\t"
    "s_barrier\n\t"
    "v_mfma_f32_16x16x32_bf16 v[0:3], a[64:67], v[196:199], v[0:3]\n\t"
    "s_waitcnt lgkmcnt(3)\n\t"
    "s_add_u32 m0, s68, 0x20000\n\t"
    "ds_read_b64_tr_b16 a[96:97], v144 offset:8192\n\t"
    "v_mfma_f32_16x16x32_bf16 v[32:35], a[64:67], v[200:203], v[32:35]\n\t"
    "s_waitcnt lgkmcnt(2)\n\t"
    "ds_read_b64_tr_b16 a[98:99], v144 offset:9216\n\t"
    "ds_read_b64_tr_b16 a[100:101], v145 offset:8192\n\t"
    "v_mfma_f32_16x16x32_bf16 v[64:67], a[64:67], v[204:207], v[64:67]\n\t"
    "buffer_load_dwordx4 v140, s[48:51], s67 offen lds\n\t"
    "v_mfma_f32_16x16x32_bf16 v[68:71], a[68:71], v[204:207], v[68:71]\n\t"
    "buffer_load_dwordx4 v141, s[48:51], s67 offen offset:1024 lds\n\t"
    "v_mfma_f32_16x16x32_bf16 v[36:39], a[68:71], v[200:203], v[36:39]\n\t"
    "s_add_u32 s67, s67, 0x40000\n\t"
    "s_add_u32 m0, s68, 0x12000\n\t"
    "v_mfma_f32_16x16x32_bf16 v[4:7], a[68:71], v[196:199], v[4:7]\n\t"
    "buffer_load_dwordx4 v134, s[36:39], s69 offen lds\n\t"
    "v_mfma_f32_16x16x32_bf16 v[8:11], a[72:75], v[196:199], v[8:11]\n\t"
    "s_waitcnt lgkmcnt(3)\n\t"
    "ds_read_b64_tr_b16 a[102:103], v145 offset:9216\n\t"
    "ds_read_b64_tr_b16 a[104:105], v146 offset:8192\n\t"
    "v_mfma_f32_16x16x32_bf16 v[40:43], a[72:75], v[200:203], v[40:43]\n\t"
    "buffer_load_dwordx4 v135, s[36:39], s69 offen offset:1024 lds\n\t"
    "v_mfma_f32_16x16x32_bf16 v[72:75], a[72:75], v[204:207], v[72:75]\n\t"
    "s_add_u32 m0, s68, 0x14000\n\t"
    "ds_read_b64_tr_b16 a[106:107], v146 offset:9216\n\t"
    "ds_read_b64_tr_b16 a[108:109], v147 offset:8192\n\t"
    "v_mfma_f32_16x16x32_bf16 v[76:79], a[76:79], v[204:207], v[76:79]\n\t"
    "buffer_load_dwordx4 v136, s[36:39], s69 offen lds\n\t"
    "v_mfma_f32_16x16x32_bf16 v[44:47], a[76:79], v[200:203], v[44:47]\n\t"
    "s_mov_b32 s74, 0x8000\n\t"
    "ds_read_b64_tr_b16 a[110:111], v147 offset:9216\n\t"
    "ds_read_b64_tr_b16 a[112:113], v148 offset:8192\n\t"
    "v_mfma_f32_16x16x32_bf16 v[12:15], a[76:79], v[196:199], v[12:15]\n\t"
    "buffer_load_dwordx4 v137, s[36:39], s69 offen offset:1024 lds\n\t"
    "v_mfma_f32_16x16x32_bf16 v[16:19], a[80:83], v[196:199], v[16:19]\n\t"
    "s_add_u32 s69, s69, 0x80\n\t"
    "s_cmp_eq_u32 s69, 0x500\n\t"
    "s_cselect_b32 s69, 0, s69\n\t"
    "v_mfma_f32_16x16x32_bf16 v[48:51], a[80:83], v[200:203], v[48:51]\n\t"
    "s_add_u32 m0, s68, 0x8000\n\t"
    "ds_read_b64_tr_b16 a[114:115], v148 offset:9216\n\t"
    "ds_read_b64_tr_b16 a[116:117], v149 offset:8192\n\t"
    "v_mfma_f32_16x16x32_bf16 v[80:83], a[80:83], v[204:207], v[80:83]\n\t"
    "buffer_load_dwordx4 v192, s[40:43], s74 offen nt lds\n\t"
    "v_mfma_f32_16x16x32_bf16 v[84:87], a[84:87], v[204:207], v[84:87]\n\t"
    "buffer_load_dwordx4 v193, s[40:43], s74 offen offset:1024 nt lds\n\t"
    "v_mfma_f32_16x16x32_bf16 v[52:55], a[84:87], v[200:203], v[52:55]\n\t"
    "s_add_u32 m0, s68, 0xe000\n\t"
    "ds_read_b64_tr_b16 a[118:119], v149 offset:9216\n\t"
    "ds_read_b64_tr_b16 a[120:121], v150 offset:8192\n\t"
    "v_mfma_f32_16x16x32_bf16 v[20:23], a[84:87], v[196:199], v[20:23]\n\t"
    "s_mov_b32 s74, 0x6000\n\t"
    "buffer_load_dwordx4 v194, s[44:47], s74 offen nt lds\n\t"
    "v_mfma_f32_16x16x32_bf16 v[24:27], a[88:91], v[196:199], v[24:27]\n\t"
    "s_waitcnt vmcnt(19)\n\t"
    "ds_read_b64_tr_b16 a[122:123], v150 offset:9216\n\t"
    "ds_read_b64_tr_b16 a[124:125], v151 offset:8192\n\t"
    "v_mfma_f32_16x16x32_bf16 v[56:59], a[88:91], v[200:203], v[56:59]\n\t"
    "buffer_load_dwordx4 v195, s[44:47], s74 offen offset:1024 nt lds\n\t"
    "v_mfma_f32_16x16x32_bf16 v[88:91], a[88:91], v[204:207], v[88:91]\n\t"
    "s_add_u32 m0, s68, 0xc000\n\t"
    "ds_read_b64_tr_b16 a[126:127], v151 offset:9216\n\t"
    "ds_read_b128 v[220:223], v138 offset:24576\n\t"
    "v_mfma_f32_16x16x32_bf16 v[92:95], a[92:95], v[204:207], v[92:95]\n\t"
    "s_mov_b32 s74, 0xa000\n\t"
    "buffer_load_dwordx4 v192, s[40:43], s74 offen nt lds\n\t"
    "v_mfma_f32_16x16x32_bf16 v[60:63], a[92:95], v[200:203], v[60:63]\n\t"
    "ds_read_b128 v[232:235], v139 offset:24576\n\t"
    "buffer_load_dwordx4 v193, s[40:43], s74 offen offset:1024 nt lds\n\t"
    "v_mfma_f32_16x16x32_bf16 v[28:31], a[92:95], v[196:199], v[28:31]\n\t"
    "s_waitcnt vmcnt(20) lgkmcnt(2)\n\t"
    "s_barrier\n\t"
    "v_mfma_f32_16x16x32_bf16 v[0:3], a[96:99], v[208:211], v[0:3]\n\t"
    "s_add_u32 m0, s68, 0x22000\n\t"
    "ds_read_b64_tr_b16 a[64:65], v144 offset:16384\n\t"
    "v_mfma_f32_16x16x32_bf16 v[32:35], a[96:99], v[212:215], v[32:35]\n\t"
    "ds_read_b64_tr_b16 a[66:67], v144 offset:17408\n\t"
    "ds_read_b64_tr_b16 a[68:69], v145 offset:16384\n\t"
    "v_mfma_f32_16x16x32_bf16 v[64:67], a[96:99], v[216:219], v[64:67]\n\t"
    "buffer_load_dwordx4 v140, s[48:51], s67 offen lds\n\t"
    "v_mfma_f32_16x16x32_bf16 v[68:71], a[100:103], v[216:219], v[68:71]\n\t"
    "ds_read_b64_tr_b16 a[70:71], v145 offset:17408\n\t"
    "ds_read_b64_tr_b16 a[72:73], v146 offset:16384\n\t"
    "v_mfma_f32_16x16x32_bf16 v[36:39], a[100:103], v[212:215], v[36:39]\n\t"
    "ds_read_b64_tr_b16 a[74:75], v146 offset:17408\n\t"
    "ds_read_b64_tr_b16 a[76:77], v147 offset:16384\n\t"
    "v_mfma_f32_16x16x32_bf16 v[4:7], a[100:103], v[208:211], v[4:7]\n\t"
    "ds_read_b64_tr_b16 a[78:79], v147 offset:17408\n\t"
    "ds_read_b64_tr_b16 a[80:81], v148 offset:16384\n\t"
    "v_mfma_f32_16x16x32_bf16 v[8:11], a[104:107], v[208:211], v[8:11]\n\t"
    "ds_read_b64_tr_b16 a[82:83], v148 offset:17408\n\t"
    "v_mfma_f32_16x16x32_bf16 v[40:43], a[104:107], v[212:215], v[40:43]\n\t"
    "buffer_load_dwordx4 v141, s[48:51], s67 offen offset:1024 lds\n\t"
    "v_mfma_f32_16x16x32_bf16 v[72:75], a[104:107], v[216:219], v[72:75]\n\t"
    "s_add_u32 s67, s67, 0x40000\n\t"
    "ds_read_b64_tr_b16 a[84:85], v149 offset:16384\n\t"
    "ds_read_b64_tr_b16 a[86:87], v149 offset:17408\n\t"
    "v_mfma_f32_16x16x32_bf16 v[76:79], a[108:111], v[216:219], v[76:79]\n\t"
    "s_cmp_eq_u32 s67, 0x500000\n\t"
    "ds_read_b64_tr_b16 a[88:89], v150 offset:16384\n\t"
    "v_mfma_f32_16x16x32_bf16 v[44:47], a[108:111], v[212:215], v[44:47]\n\t"
    "s_cselect_b32 s67, 0, s67\n\t"
    "ds_read_b64_tr_b16 a[90:91], v150 offset:17408\n\t"
    "v_mfma_f32_16x16x32_bf16 v[12:15], a[108:111], v[208:211], v[12:15]\n\t"
    "s_add_u32 m0, s68, 0x16000\n\t"
    "ds_read_b64_tr_b16 a[92:93], v151 offset:16384\n\t"
    "v_mfma_f32_16x16x32_bf16 v[16:19], a[112:115], v[208:211], v[16:19]\n\t"
    "s_waitcnt lgkmcnt(15)\n\t"
    "ds_read_b64_tr_b16 a[94:95], v151 offset:17408\n\t"
    "v_mfma_f32_16x16x32_bf16 v[48:51], a[112:115], v[212:215], v[48:51]\n\t"
    "buffer_load_dwordx4 v132, s[36:39], s69 offen lds\n\t"
    "v_mfma_f32_16x16x32_bf16 v[80:83], a[112:115], v[216:219], v[80:83]\n\t"
    "v_mfma_f32_16x16x32_bf16 v[84:87], a[116:119], v[216:219], v[84:87]\n\t"
    "v_mfma_f32_16x16x32_bf16 v[52:55], a[116:119], v[212:215], v[52:55]\n\t"
    "s_waitcnt vmcnt(21)\n\t"
    "ds_read_b128 v[224:227], v138 offset:40960\n\t"
    "ds_read_b128 v[236:239], v139 offset:40960\n\t"
    "v_mfma_f32_16x16x32_bf16 v[20:23], a[116:119], v[208:211], v[20:23]\n\t"
    "v_mfma_f32_16x16x32_bf16 v[24:27], a[120:123], v[208:211], v[24:27]\n\t"
    "s_waitcnt vmcnt(19)\n\t"
    "buffer_load_dwordx4 v133, s[36:39], s69 offen offset:1024 lds\n\t"
    "v_mfma_f32_16x16x32_bf16 v[56:59], a[120:123], v[212:215], v[56:59]\n\t"
    "ds_read_b128 v[228:231], v138 offset:57344\n\t"
    "ds_read_b128 v[240:243], v139 offset:57344\n\t"
    "v_mfma_f32_16x16x32_bf16 v[88:91], a[120:123], v[216:219], v[88:91]\n\t"
    "v_mfma_f32_16x16x32_bf16 v[92:95], a[124:127], v[216:219], v[92:95]\n\t"
    "v_mfma_f32_16x16x32_bf16 v[60:63], a[124:127], v[212:215], v[60:63]\n\t"
    "v_mfma_f32_16x16x32_bf16 v[28:31], a[124:127], v[208:211], v[28:31]\n\t"
    "s_waitcnt vmcnt(18) lgkmcnt(4)\n\t"
    "s_barrier\n\t"
    "v_mfma_f32_16x16x32_bf16 v[0:3], a[64:67], v[220:223], v[0:3]\n\t"
    "s_waitcnt lgkmcnt(3)\n\t"
    "s_add_u32 m0, s68, 0x24000\n\t"
    "ds_read_b64_tr_b16 a[96:97], v144 offset:24576\n\t"
    "v_mfma_f32_16x16x32_bf16 v[32:35], a[64:67], v[224:227], v[32:35]\n\t"
    "s_waitcnt lgkmcnt(2)\n\t"
    "ds_read_b64_tr_b16 a[98:99], v144 offset:25600\n\t"
    "ds_read_b64_tr_b16 a[100:101], v145 offset:24576\n\t"
    "v_mfma_f32_16x16x32_bf16 v[64:67], a[64:67], v[228:231], v[64:67]\n\t"
    "buffer_load_dwordx4 v140, s[48:51], s67 offen lds\n\t"
    "v_mfma_f32_16x16x32_bf16 v[68:71], a[68:71], v[228:231], v[68:71]\n\t"
    "ds_read_b64_tr_b16 a[102:103], v145 offset:25600\n\t"
    "ds_read_b64_tr_b16 a[104:105], v146 offset:24576\n\t"
    "v_mfma_f32_16x16x32_bf16 v[36:39], a[68:71], v[224:227], v[36:39]\n\t"
    "ds_read_b64_tr_b16 a[106:107], v146 offset:25600\n\t"
    "ds_read_b64_tr_b16 a[108:109], v147 offset:24576\n\t"
    "v_mfma_f32_16x16x32_bf16 v[4:7], a[68:71], v[220:223], v[4:7]\n\t"
    "buffer_load_dwordx4 v141, s[48:51], s67 offen offset:1024 lds\n\t"
    "v_mfma_f32_16x16x32_bf16 v[8:11], a[72:75], v[220:223], v[8:11]\n\t"
    "s_add_u32 s67, s67, 0x40000\n\t"
    "ds_read_b64_tr_b16 a[110:111], v147 offset:25600\n\t"
    "ds_read_b64_tr_b16 a[112:113], v148 offset:24576\n\t"
    "v_mfma_f32_16x16x32_bf16 v[40:43], a[72:75], v[224:227], v[40:43]\n\t"
    "s_add_u32 m0, s68, 0x1a000\n\t"
    "ds_read_b64_tr_b16 a[114:115], v148 offset:25600\n\t"
    "ds_read_b64_tr_b16 a[116:117], v149 offset:24576\n\t"
    "v_mfma_f32_16x16x32_bf16 v[72:75], a[72:75], v[228:231], v[72:75]\n\t"
    "ds_read_b64_tr_b16 a[118:119], v149 offset:25600\n\t"
    "v_mfma_f32_16x16x32_bf16 v[76:79], a[76:79], v[228:231], v[76:79]\n\t"
    "buffer_load_dwordx4 v134, s[36:39], s69 offen lds\n\t"
    "v_mfma_f32_16x16x32_bf16 v[44:47], a[76:79], v[224:227], v[44:47]\n\t"
    "ds_read_b64_tr_b16 a[120:121], v150 offset:24576\n\t"
    "ds_read_b64_tr_b16 a[122:123], v150 offset:25600\n\t"
    "v_mfma_f32_16x16x32_bf16 v[12:15], a[76:79], v[220:223], v[12:15]\n\t"
    "ds_read_b64_tr_b16 a[124:125], v151 offset:24576\n\t"
    "v_mfma_f32_16x16x32_bf16 v[16:19], a[80:83], v[220:223], v[16:19]\n\t"
    "s_waitcnt lgkmcnt(15)\n\t"
    "ds_read_b64_tr_b16 a[126:127], v151 offset:25600\n\t"
    "v_mfma_f32_16x16x32_bf16 v[48:51], a[80:83], v[224:227], v[48:51]\n\t"
    "buffer_load_dwordx4 v135, s[36:39], s69 offen offset:1024 lds\n\t"
    "v_mfma_f32_16x16x32_bf16 v[80:83], a[80:83], v[228:231], v[80:83]\n\t"
    "s_add_u32 m0, s68, 0x1e000\n\t"
    "v_mfma_f32_16x16x32_bf16 v[84:87], a[84:87], v[228:231], v[84:87]\n\t"
    "v_mfma_f32_16x16x32_bf16 v[52:55], a[84:87], v[224:227], v[52:55]\n\t"
    "s_waitcnt vmcnt(20)\n\t"
    "buffer_load_dwordx4 v136, s[36:39], s69 offen lds\n\t"
    "v_mfma_f32_16x16x32_bf16 v[20:23], a[84:87], v[220:223], v[20:23]\n\t"
    "ds_read_b128 v[196:199], v138\n\t"
    "ds_read_b128 v[208:211], v139\n\t"
    "v_mfma_f32_16x16x32_bf16 v[24:27], a[88:91], v[220:223], v[24:27]\n\t"
    "v_mfma_f32_16x16x32_bf16 v[56:59], a[88:91], v[224:227], v[56:59]\n\t"
    "v_mfma_f32_16x16x32_bf16 v[88:91], a[88:91], v[228:231], v[88:91]\n\t"
    "buffer_load_dwordx4 v137, s[36:39], s69 offen offset:1024 lds\n\t"
    "v_mfma_f32_16x16x32_bf16 v[92:95], a[92:95], v[228:231], v[92:95]\n\t"
    "v_mfma_f32_16x16x32_bf16 v[60:63], a[92:95], v[224:227], v[60:63]\n\t"
    "v_mfma_f32_16x16x32_bf16 v[28:31], a[92:95], v[220:223], v[28:31]\n\t"
    "s_cmp_lg_u32 s64, 0\n\t"
    "s_cbranch_scc1 Luq110%=\n\t"
    "s_call_b64 s[22:23], Lc6%=\n\t"
    "Luq110%=:\n\t"
    "s_cmp_lg_u32 s64, 1\n\t"
    "s_cbranch_scc1 Luq111%=\n\t"
    "s_call_b64 s[22:23], Lc7%=\n\t"
    "Luq111%=:\n\t"
    "s_cmp_lg_u32 s64, 2\n\t"
    "s_cbranch_scc1 Luq112%=\n\t"
    "s_call_b64 s[22:23], Lc8%=\n\t"
    "Luq112%=:\n\t"
    "s_cmp_lg_u32 s64, 3\n\t"
    "s_cbranch_scc1 Luq113%=\n\t"
    "s_call_b64 s[22:23], Lc9%=\n\t"
    "Luq113%=:\n\t"
    "s_waitcnt vmcnt(20) lgkmcnt(2)\n\t"
    "s_barrier\n\t"
    "v_mfma_f32_16x16x32_bf16 v[0:3], a[96:99], v[232:235], v[0:3]\n\t"
    "s_add_u32 m0, s68, 0x26000\n\t"
    "ds_read_b64_tr_b16 a[64:65], v144\n\t"
    "v_mfma_f32_16x16x32_bf16 v[32:35], a[96:99], v[236:239], v[32:35]\n\t"
    "ds_read_b64_tr_b16 a[66:67], v144 offset:1024\n\t"
    "ds_read_b64_tr_b16 a[68:69], v145\n\t"
    "v_mfma_f32_16x16x32_bf16 v[64:67], a[96:99], v[240:243], v[64:67]\n\t"
    "buffer_load_dwordx4 v140, s[48:51], s67 offen lds\n\t"
    "v_mfma_f32_16x16x32_bf16 v[68:71], a[100:103], v[240:243], v[68:71]\n\t"
    "ds_read_b64_tr_b16 a[70:71], v145 offset:1024\n\t"
    "ds_read_b64_tr_b16 a[72:73], v146\n\t"
    "v_mfma_f32_16x16x32_bf16 v[36:39], a[100:103], v[236:239], v[36:39]\n\t"
    "ds_read_b64_tr_b16 a[74:75], v146 offset:1024\n\t"
    "ds_read_b64_tr_b16 a[76:77], v147\n\t"
    "v_mfma_f32_16x16x32_bf16 v[4:7], a[100:103], v[232:235], v[4:7]\n\t"
    "ds_read_b64_tr_b16 a[78:79], v147 offset:1024\n\t"
    "ds_read_b64_tr_b16 a[80:81], v148\n\t"
    "v_mfma_f32_16x16x32_bf16 v[8:11], a[104:107], v[232:235], v[8:11]\n\t"
    "ds_read_b64_tr_b16 a[82:83], v148 offset:1024\n\t"
    "v_mfma_f32_16x16x32_bf16 v[40:43], a[104:107], v[236:239], v[40:43]\n\t"
    "buffer_load_dwordx4 v141, s[48:51], s67 offen offset:1024 lds\n\t"
    "v_mfma_f32_16x16x32_bf16 v[72:75], a[104:107], v[240:243], v[72:75]\n\t"
    "ds_read_b64_tr_b16 a[84:85], v149\n\t"
    "ds_read_b64_tr_b16 a[86:87], v149 offset:1024\n\t"
    "v_mfma_f32_16x16x32_bf16 v[76:79], a[108:111], v[240:243], v[76:79]\n\t"
    "s_add_u32 m0, s68, 0x10000\n\t"
    "ds_read_b64_tr_b16 a[88:89], v150\n\t"
    "v_mfma_f32_16x16x32_bf16 v[44:47], a[108:111], v[236:239], v[44:47]\n\t"
    "s_waitcnt lgkmcnt(13)\n\t"
    "ds_read_b64_tr_b16 a[90:91], v150 offset:1024\n\t"
    "v_mfma_f32_16x16x32_bf16 v[12:15], a[108:111], v[232:235], v[12:15]\n\t"
    "s_mov_b32 s74, 0xc000\n\t"
    "ds_read_b64_tr_b16 a[92:93], v151\n\t"
    "v_mfma_f32_16x16x32_bf16 v[16:19], a[112:115], v[232:235], v[16:19]\n\t"
    "ds_read_b64_tr_b16 a[94:95], v151 offset:1024\n\t"
    "v_mfma_f32_16x16x32_bf16 v[48:51], a[112:115], v[236:239], v[48:51]\n\t"
    "buffer_load_dwordx4 v192, s[40:43], s74 offen nt lds\n\t"
    "v_mfma_f32_16x16x32_bf16 v[80:83], a[112:115], v[240:243], v[80:83]\n\t"
    "v_mfma_f32_16x16x32_bf16 v[84:87], a[116:119], v[240:243], v[84:87]\n\t"
    "v_mfma_f32_16x16x32_bf16 v[52:55], a[116:119], v[236:239], v[52:55]\n\t"
    "s_waitcnt vmcnt(21)\n\t"
    "ds_read_b128 v[200:203], v138 offset:8192\n\t"
    "ds_read_b128 v[212:215], v139 offset:8192\n\t"
    "v_mfma_f32_16x16x32_bf16 v[20:23], a[116:119], v[232:235], v[20:23]\n\t"
    "v_mfma_f32_16x16x32_bf16 v[24:27], a[120:123], v[232:235], v[24:27]\n\t"
    "s_waitcnt vmcnt(19)\n\t"
    "buffer_load_dwordx4 v193, s[40:43], s74 offen offset:1024 nt lds\n\t"
    "v_mfma_f32_16x16x32_bf16 v[56:59], a[120:123], v[236:239], v[56:59]\n\t"
    "ds_read_b128 v[204:207], v138 offset:16384\n\t"
    "ds_read_b128 v[216:219], v139 offset:16384\n\t"
    "v_mfma_f32_16x16x32_bf16 v[88:91], a[120:123], v[240:243], v[88:91]\n\t"
    "v_mfma_f32_16x16x32_bf16 v[92:95], a[124:127], v[240:243], v[92:95]\n\t"
    "v_mfma_f32_16x16x32_bf16 v[60:63], a[124:127], v[236:239], v[60:63]\n\t"
    "v_mfma_f32_16x16x32_bf16 v[28:31], a[124:127], v[232:235], v[28:31]\n\t"
    "s_waitcnt vmcnt(12) lgkmcnt(4)\n\t"
    "s_barrier\n\t"
    "v_mfma_f32_16x16x32_bf16 v[0:3], a[64:67], v[196:199], v[0:3]\n\t"
    "s_waitcnt lgkmcnt(3)\n\t"
    "s_add_u32 m0, s68, 0x12000\n\t"
    "ds_read_b64_tr_b16 a[96:97], v144 offset:8192\n\t"
    "v_mfma_f32_16x16x32_bf16 v[32:35], a[64:67], v[200:203], v[32:35]\n\t"
    "s_waitcnt lgkmcnt(2)\n\t"
    "s_mov_b32 s74, 0x8000\n\t"
    "v_mfma_f32_16x16x32_bf16 v[64:67], a[64:67], v[204:207], v[64:67]\n\t"
    "buffer_load_dwordx4 v194, s[44:47], s74 offen nt lds\n\t"
    "v_mfma_f32_16x16x32_bf16 v[68:71], a[68:71], v[204:207], v[68:71]\n\t"
    "ds_read_b64_tr_b16 a[98:99], v144 offset:9216\n\t"
    "ds_read_b64_tr_b16 a[100:101], v145 offset:8192\n\t"
    "v_mfma_f32_16x16x32_bf16 v[36:39], a[68:71], v[200:203], v[36:39]\n\t"
    "ds_read_b64_tr_b16 a[102:103], v145 offset:9216\n\t"
    "ds_read_b64_tr_b16 a[104:105], v146 offset:8192\n\t"
    "v_mfma_f32_16x16x32_bf16 v[4:7], a[68:71], v[196:199], v[4:7]\n\t"
    "ds_read_b64_tr_b16 a[106:107], v146 offset:9216\n\t"
    "ds_read_b64_tr_b16 a[108:109], v147 offset:8192\n\t"
    "v_mfma_f32_16x16x32_bf16 v[8:11], a[72:75], v[196:199], v[8:11]\n\t"
    "ds_read_b64_tr_b16 a[110:111], v147 offset:9216\n\t"
    "ds_read_b64_tr_b16 a[112:113], v148 offset:8192\n\t"
    "v_mfma_f32_16x16x32_bf16 v[40:43], a[72:75], v[200:203], v[40:43]\n\t"
    "buffer_load_dwordx4 v195, s[44:47], s74 offen offset:1024 nt lds\n\t"
    "v_mfma_f32_16x16x32_bf16 v[72:75], a[72:75], v[204:207], v[72:75]\n\t"
    "ds_read_b64_tr_b16 a[114:115], v148 offset:9216\n\t"
    "ds_read_b64_tr_b16 a[116:117], v149 offset:8192\n\t"
    "v_mfma_f32_16x16x32_bf16 v[76:79], a[76:79], v[204:207], v[76:79]\n\t"
    "s_add_u32 m0, s68, 0x14000\n\t"
    "ds_read_b64_tr_b16 a[118:119], v149 offset:9216\n\t"
    "ds_read_b64_tr_b16 a[120:121], v150 offset:8192\n\t"
    "v_mfma_f32_16x16x32_bf16 v[44:47], a[76:79], v[200:203], v[44:47]\n\t"
    "s_waitcnt lgkmcnt(13)\n\t"
    "ds_read_b64_tr_b16 a[122:123], v150 offset:9216\n\t"
    "v_mfma_f32_16x16x32_bf16 v[12:15], a[76:79], v[196:199], v[12:15]\n\t"
    "s_mov_b32 s74, 0xe000\n\t"
    "ds_read_b64_tr_b16 a[124:125], v151 offset:8192\n\t"
    "v_mfma_f32_16x16x32_bf16 v[16:19], a[80:83], v[196:199], v[16:19]\n\t"
    "ds_read_b64_tr_b16 a[126:127], v151 offset:9216\n\t"
    "v_mfma_f32_16x16x32_bf16 v[48:51], a[80:83], v[200:203], v[48:51]\n\t"
    "buffer_load_dwordx4 v192, s[40:43], s74 offen nt lds\n\t"
    "v_mfma_f32_16x16x32_bf16 v[80:83], a[80:83], v[204:207], v[80:83]\n\t"
    "v_mfma_f32_16x16x32_bf16 v[84:87], a[84:87], v[204:207], v[84:87]\n\t"
    "v_mfma_f32_16x16x32_bf16 v[52:55], a[84:87], v[200:203], v[52:55]\n\t"
    "s_waitcnt vmcnt(13)\n\t"
    "ds_read_b128 v[220:223], v138 offset:24576\n\t"
    "ds_read_b128 v[232:235], v139 offset:24576\n\t"
    "v_mfma_f32_16x16x32_bf16 v[20:23], a[84:87], v[196:199], v[20:23]\n\t"
    "s_memtime s[20:21]\n\t"
    "v_mfma_f32_16x16x32_bf16 v[24:27], a[88:91], v[196:199], v[24:27]\n\t"
    "s_memrealtime s[70:71]\n\t"
    "buffer_load_dwordx4 v193, s[40:43], s74 offen offset:1024 nt lds\n\t"
    "v_mfma_f32_16x16x32_bf16 v[56:59], a[88:91], v[200:203], v[56:59]\n\t"
    "v_mfma_f32_16x16x32_bf16 v[88:91], a[88:91], v[204:207], v[88:91]\n\t"
    "v_mfma_f32_16x16x32_bf16 v[92:95], a[92:95], v[204:207], v[92:95]\n\t"
    "v_mfma_f32_16x16x32_bf16 v[60:63], a[92:95], v[200:203], v[60:63]\n\t"
    "v_mfma_f32_16x16x32_bf16 v[28:31], a[92:95], v[196:199], v[28:31]\n\t"
    "s_waitcnt vmcnt(12) lgkmcnt(0)\n\t"
    "s_barrier\n\t"
    "v_mfma_f32_16x16x32_bf16 v[0:3], a[96:99], v[208:211], v[0:3]\n\t"
    "s_add_u32 m0, s68, 0x16000\n\t"
    "ds_read_b64_tr_b16 a[64:65], v144 offset:16384\n\t"
    "v_mfma_f32_16x16x32_bf16 v[32:35], a[96:99], v[212:215], v[32:35]\n\t"
    "s_mov_b32 s74, 0xa000\n\t"
    "v_mfma_f32_16x16x32_bf16 v[64:67], a[96:99], v[216:219], v[64:67]\n\t"
    "buffer_load_dwordx4 v194, s[44:47], s74 offen nt lds\n\t"
    "v_mfma_f32_16x16x32_bf16 v[68:71], a[100:103], v[216:219], v[68:71]\n\t"
    "ds_read_b64_tr_b16 a[66:67], v144 offset:17408\n\t"
    "ds_read_b64_tr_b16 a[68:69], v145 offset:16384\n\t"
    "v_mfma_f32_16x16x32_bf16 v[36:39], a[100:103], v[212:215], v[36:39]\n\t"
    "ds_read_b64_tr_b16 a[70:71], v145 offset:17408\n\t"
    "ds_read_b64_tr_b16 a[72:73], v146 offset:16384\n\t"
    "v_mfma_f32_16x16x32_bf16 v[4:7], a[100:103], v[208:211], v[4:7]\n\t"
    "ds_read_b64_tr_b16 a[74:75], v146 offset:17408\n\t"
    "ds_read_b64_tr_b16 a[76:77], v147 offset:16384\n\t"
    "v_mfma_f32_16x16x32_bf16 v[8:11], a[104:107], v[208:211], v[8:11]\n\t"
    "ds_read_b64_tr_b16 a[78:79], v147 offset:17408\n\t"
    "ds_read_b64_tr_b16 a[80:81], v148 offset:16384\n\t"
    "v_mfma_f32_16x16x32_bf16 v[40:43], a[104:107], v[212:215], v[40:43]\n\t"
    "ds_read_b64_tr_b16 a[82:83], v148 offset:17408\n\t"
    "ds_read_b64_tr_b16 a[84:85], v149 offset:16384\n\t"
    "v_mfma_f32_16x16x32_bf16 v[72:75], a[104:107], v[216:219], v[72:75]\n\t"
    "ds_read_b64_tr_b16 a[86:87], v149 offset:17408\n\t"
    "v_mfma_f32_16x16x32_bf16 v[76:79], a[108:111], v[216:219], v[76:79]\n\t"
    "ds_read_b64_tr_b16 a[88:89], v150 offset:16384\n\t"
    "v_mfma_f32_16x16x32_bf16 v[44:47], a[108:111], v[212:215], v[44:47]\n\t"
    "ds_read_b64_tr_b16 a[90:91], v150 offset:17408\n\t"
    "v_mfma_f32_16x16x32_bf16 v[12:15], a[108:111], v[208:211], v[12:15]\n\t"
    "s_sub_u32 s20, s20, s18\n\t"
    "ds_read_b64_tr_b16 a[92:93], v151 offset:16384\n\t"
    "v_mfma_f32_16x16x32_bf16 v[16:19], a[112:115], v[208:211], v[16:19]\n\t"
    "s_sub_u32 s21, s70, s98\n\t"
    "ds_read_b64_tr_b16 a[94:95], v151 offset:17408\n\t"
    "v_mfma_f32_16x16x32_bf16 v[48:51], a[112:115], v[212:215], v[48:51]\n\t"
    "s_lshl_b32 s20, s20, 4\n\t"
    "buffer_load_dwordx4 v195, s[44:47], s74 offen offset:1024 nt lds\n\t"
    "v_mfma_f32_16x16x32_bf16 v[80:83], a[112:115], v[216:219], v[80:83]\n\t"
    "s_mul_i32 s21, s21, 0x113\n\t"
    "v_mfma_f32_16x16x32_bf16 v[84:87], a[116:119], v[216:219], v[84:87]\n\t"
    "s_cmp_lt_u32 s20, s21\n\t"
    "v_mfma_f32_16x16x32_bf16 v[52:55], a[116:119], v[212:215], v[52:55]\n\t"
    "s_waitcnt vmcnt(12)\n\t"
    "ds_read_b128 v[224:227], v138 offset:40960\n\t"
    "ds_read_b128 v[236:239], v139 offset:40960\n\t"
    "v_mfma_f32_16x16x32_bf16 v[20:23], a[116:119], v[208:211], v[20:23]\n\t"
    "s_cselect_b32 s19, 44, 57\n\t"
    "v_mfma_f32_16x16x32_bf16 v[24:27], a[120:123], v[208:211], v[24:27]\n\t"
    "s_waitcnt vmcnt(10)\n\t"
    "ds_read_b128 v[228:231], v138 offset:57344\n\t"
    "ds_read_b128 v[240:243], v139 offset:57344\n\t"
    "v_mfma_f32_16x16x32_bf16 v[56:59], a[120:123], v[212:215], v[56:59]\n\t"
    "s_cselect_b32 s20, 63, 0\n\t"
    "v_mfma_f32_16x16x32_bf16 v[88:91], a[120:123], v[216:219], v[88:91]\n\t"
    "v_mfma_f32_16x16x32_bf16 v[92:95], a[124:127], v[216:219], v[92:95]\n\t"
    "v_mfma_f32_16x16x32_bf16 v[60:63], a[124:127], v[212:215], v[60:63]\n\t"
    "v_mfma_f32_16x16x32_bf16 v[28:31], a[124:127], v[208:211], v[28:31]\n\t"
    "s_branch Lqt%=\n\t"
    "Lfb%=:\n\t"
    "s_waitcnt vmcnt(0) lgkmcnt(0)\n\t"
    "s_mov_b32 s35, 0\n\t"
    "v_lshlrev_b32 v172, 4, v246\n\t"
    "v_lshl_add_u32 v168, v245, 8, v172\n\t"
    "v_add_u32 v169, 0x1000, v168\n\t"
    "v_add_u32 v172, 0, v246\n\t"
    "v_lshlrev_b32 v172, 1, v172\n\t"
    "v_xor_b32 v172, v172, v245\n\t"
    "v_lshlrev_b32 v172, 4, v172\n\t"
    "v_lshl_add_u32 v170, v246, 8, v172\n\t"
    "v_add_u32 v172, 4, v246\n\t"
    "v_lshlrev_b32 v172, 1, v172\n\t"
    "v_xor_b32 v172, v172, v245\n\t"
    "v_lshlrev_b32 v172, 4, v172\n\t"
    "v_lshl_add_u32 v171, v246, 8, v172\n\t"
    "s_add_u32 s76, s72, 0x20000\n\t"
    "v_add_u32 v184, s76, v176\n\t"
    "v_add_u32 v185, s76, v177\n\t"
    "v_add_u32 v186, s76, v178\n\t"
    "v_add_u32 v187, s76, v179\n\t"
    "v_add_u32 v188, s76, v180\n\t"
    "v_add_u32 v189, s76, v181\n\t"
    "v_add_u32 v190, s76, v182\n\t"
    "v_add_u32 v191, s76, v183\n\t"
    "v_mov_b32 v156, 0xff800000\n\t"
    "v_mov_b32 v160, 0xff800000\n\t"
    "v_mov_b32 v164, 0xff800000\n\t"
    "s_mov_b32 s74, 0\n\t"
    "Lf1%=:\n\t"
    "buffer_load_dwordx4 a[0:3], v168, s[40:43], s74 offen\n\t"
    "buffer_load_dwordx4 a[4:7], v168, s[40:43], s74 offen offset:64\n\t"
    "buffer_load_dwordx4 a[8:11], v168, s[40:43], s74 offen offset:128\n\t"
    "buffer_load_dwordx4 a[12:15], v168, s[40:43], s74 offen offset:192\n\t"
    "buffer_load_dwordx4 a[16:19], v169, s[40:43], s74 offen\n\t"
    "buffer_load_dwordx4 a[20:23], v169, s[40:43], s74 offen offset:64\n\t"
    "buffer_load_dwordx4 a[24:27], v169, s[40:43], s74 offen offset:128\n\t"
    "buffer_load_dwordx4 a[28:31], v169, s[40:43], s74 offen offset:192\n\t"
    "s_waitcnt vmcnt(7)\n\t"
    "v_mfma_f32_16x16x32_bf16 v[108:111], a[0:3], v[196:199], 0\n\t"
    "s_waitcnt vmcnt(6)\n\t"
    "v_mfma_f32_16x16x32_bf16 v[108:111], a[4:7], v[200:203], v[108:111]\n\t"
    "s_waitcnt vmcnt(5)\n\t"
    "v_mfma_f32_16x16x32_bf16 v[108:111], a[8:11], v[204:207], v[108:111]\n\t"
    "s_waitcnt vmcnt(4)\n\t"
    "v_mfma_f32_16x16x32_bf16 v[108:111], a[12:15], v[208:211], v[108:111]\n\t"
    "v_mfma_f32_16x16x32_bf16 v[112:115], a[0:3], v[212:215], 0\n\t"
    "v_mfma_f32_16x16x32_bf16 v[112:115], a[4:7], v[216:219], v[112:115]\n\t"
    "v_mfma_f32_16x16x32_bf16 v[112:115], a[8:11], v[220:223], v[112:115]\n\t"
    "v_mfma_f32_16x16x32_bf16 v[112:115], a[12:15], v[224:227], v[112:115]\n\t"
    "v_mfma_f32_16x16x32_bf16 v[116:119], a[0:3], v[228:231], 0\n\t"
    "v_mfma_f32_16x16x32_bf16 v[116:119], a[4:7], v[232:235], v[116:119]\n\t"
    "v_mfma_f32_16x16x32_bf16 v[116:119], a[8:11], v[236:239], v[116:119]\n\t"
    "v_mfma_f32_16x16x32_bf16 v[116:119], a[12:15], v[240:243], v[116:119]\n\t"
    "s_waitcnt vmcnt(3)\n\t"
    "v_mfma_f32_16x16x32_bf16 v[120:123], a[16:19], v[196:199], 0\n\t"
    "s_waitcnt vmcnt(2)\n\t"
    "v_mfma_f32_16x16x32_bf16 v[120:123], a[20:23], v[200:203], v[120:123]\n\t"
    "s_waitcnt vmcnt(1)\n\t"
    "v_mfma_f32_16x16x32_bf16 v[120:123], a[24:27], v[204:207], v[120:123]\n\t"
    "s_waitcnt vmcnt(0)\n\t"
    "v_mfma_f32_16x16x32_bf16 v[120:123], a[28:31], v[208:211], v[120:123]\n\t"
    "v_mfma_f32_16x16x32_bf16 v[124:127], a[16:19], v[212:215], 0\n\t"
    "v_mfma_f32_16x16x32_bf16 v[124:127], a[20:23], v[216:219], v[124:127]\n\t"
    "v_mfma_f32_16x16x32_bf16 v[124:127], a[24:27], v[220:223], v[124:127]\n\t"
    "v_mfma_f32_16x16x32_bf16 v[124:127], a[28:31], v[224:227], v[124:127]\n\t"
    "v_mfma_f32_16x16x32_bf16 v[128:131], a[16:19], v[228:231], 0\n\t"
    "v_mfma_f32_16x16x32_bf16 v[128:131], a[20:23], v[232:235], v[128:131]\n\t"
    "v_mfma_f32_16x16x32_bf16 v[128:131], a[24:27], v[236:239], v[128:131]\n\t"
    "v_mfma_f32_16x16x32_bf16 v[128:131], a[28:31], v[240:243], v[128:131]\n\t"
    "v_max3_f32 v156, v156, v108, v109\n\t"
    "v_max3_f32 v156, v156, v110, v111\n\t"
    "v_max3_f32 v156, v156, v120, v121\n\t"
    "v_max3_f32 v156, v156, v122, v123\n\t"
    "v_max3_f32 v160, v160, v112, v113\n\t"
    "v_max3_f32 v160, v160, v114, v115\n\t"
    "v_max3_f32 v160, v160, v124, v125\n\t"
    "v_max3_f32 v160, v160, v126, v127\n\t"
    "v_max3_f32 v164, v164, v116, v117\n\t"
    "v_max3_f32 v164, v164, v118, v119\n\t"
    "v_max3_f32 v164, v164, v128, v129\n\t"
    "v_max3_f32 v164, v164, v130, v131\n\t"
    "s_add_u32 s74, s74, 0x2000\n\t"
    "s_cmp_lt_u32 s74, 0x80000\n\t"
    "s_cbranch_scc1 Lf1%=\n\t"
    "v_mov_b32 v157, v156\n\t"
    "v_mov_b32 v161, v160\n\t"
    "v_mov_b32 v165, v164\n\t"
    "v_permlane32_swap_b32 v156, v157\n\t"
    "v_permlane32_swap_b32 v160, v161\n\t"
    "v_permlane32_swap_b32 v164, v165\n\t"
    "v_max_f32 v156, v156, v157\n\t"
    "v_max_f32 v160, v160, v161\n\t"
    "v_max_f32 v164, v164, v165\n\t"
    "v_mov_b32 v157, v156\n\t"
    "v_mov_b32 v161, v160\n\t"
    "v_mov_b32 v165, v164\n\t"
    "v_permlane16_swap_b32 v156, v157\n\t"
    "v_permlane16_swap_b32 v160, v161\n\t"
    "v_permlane16_swap_b32 v164, v165\n\t"
    "v_max_f32 v156, v156, v157\n\t"
    "v_max_f32 v160, v160, v161\n\t"
    "v_max_f32 v164, v164, v165\n\t"
    "v_xor_b32 v159, 0x80000000, v156\n\t"
    "v_xor_b32 v158, 0x80000000, v156\n\t"
    "v_xor_b32 v157, 0x80000000, v156\n\t"
    "v_xor_b32 v156, 0x80000000, v156\n\t"
    "v_xor_b32 v163, 0x80000000, v160\n\t"
    "v_xor_b32 v162, 0x80000000, v160\n\t"
    "v_xor_b32 v161, 0x80000000, v160\n\t"
    "v_xor_b32 v160, 0x80000000, v160\n\t"
    "v_xor_b32 v167, 0x80000000, v164\n\t"
    "v_xor_b32 v166, 0x80000000, v164\n\t"
    "v_xor_b32 v165, 0x80000000, v164\n\t"
    "v_xor_b32 v164, 0x80000000, v164\n\t"
    "v_mov_b64 v[0:1], 0\n\t"
    "v_mov_b64 v[2:3], 0\n\t"
    "v_mov_b64 v[4:5], 0\n\t"
    "v_mov_b64 v[6:7], 0\n\t"
    "v_mov_b64 v[8:9], 0\n\t"
    "v_mov_b64 v[10:11], 0\n\t"
    "v_mov_b64 v[12:13], 0\n\t"
    "v_mov_b64 v[14:15], 0\n\t"
    "v_mov_b64 v[16:17], 0\n\t"
    "v_mov_b64 v[18:19], 0\n\t"
    "v_mov_b64 v[20:21], 0\n\t"
    "v_mov_b64 v[22:23], 0\n\t"
    "v_mov_b64 v[24:25], 0\n\t"
    "v_mov_b64 v[26:27], 0\n\t"
    "v_mov_b64 v[28:29], 0\n\t"
    "v_mov_b64 v[30:31], 0\n\t"
    "v_mov_b64 v[32:33], 0\n\t"
    "v_mov_b64 v[34:35], 0\n\t"
    "v_mov_b64 v[36:37], 0\n\t"
    "v_mov_b64 v[38:39], 0\n\t"
    "v_mov_b64 v[40:41], 0\n\t"
    "v_mov_b64 v[42:43], 0\n\t"
    "v_mov_b64 v[44:45], 0\n\t"
    "v_mov_b64 v[46:47], 0\n\t"
    "v_mov_b64 v[48:49], 0\n\t"
    "v_mov_b64 v[50:51], 0\n\t"
    "v_mov_b64 v[52:53], 0\n\t"
    "v_mov_b64 v[54:55], 0\n\t"
    "v_mov_b64 v[56:57], 0\n\t"
    "v_mov_b64 v[58:59], 0\n\t"
    "v_mov_b64 v[60:61], 0\n\t"
    "v_mov_b64 v[62:63], 0\n\t"
    "v_mov_b64 v[64:65], 0\n\t"
    "v_mov_b64 v[66:67], 0\n\t"
    "v_mov_b64 v[68:69], 0\n\t"
    "v_mov_b64 v[70:71], 0\n\t"
    "v_mov_b64 v[72:73], 0\n\t"
    "v_mov_b64 v[74:75], 0\n\t"
    "v_mov_b64 v[76:77], 0\n\t"
    "v_mov_b64 v[78:79], 0\n\t"
    "v_mov_b64 v[80:81], 0\n\t"
    "v_mov_b64 v[82:83], 0\n\t"
    "v_mov_b64 v[84:85], 0\n\t"
    "v_mov_b64 v[86:87], 0\n\t"
    "v_mov_b64 v[88:89], 0\n\t"
    "v_mov_b64 v[90:91], 0\n\t"
    "v_mov_b64 v[92:93], 0\n\t"
    "v_mov_b64 v[94:95], 0\n\t"
    "v_mov_b64 v[96:97], 0\n\t"
    "v_mov_b64 v[98:99], 0\n\t"
    "s_mov_b32 s74, 0\n\t"
    "Lf2%=:\n\t"
    "buffer_load_dwordx4 a[0:3], v168, s[40:43], s74 offen\n\t"
    "buffer_load_dwordx4 a[4:7], v168, s[40:43], s74 offen offset:64\n\t"
    "buffer_load_dwordx4 a[8:11], v168, s[40:43], s74 offen offset:128\n\t"
    "buffer_load_dwordx4 a[12:15], v168, s[40:43], s74 offen offset:192\n\t"
    "buffer_load_dwordx4 a[16:19], v169, s[40:43], s74 offen\n\t"
    "buffer_load_dwordx4 a[20:23], v169, s[40:43], s74 offen offset:64\n\t"
    "buffer_load_dwordx4 a[24:27], v169, s[40:43], s74 offen offset:128\n\t"
    "buffer_load_dwordx4 a[28:31], v169, s[40:43], s74 offen offset:192\n\t"
    "s_add_u32 s75, s74, 0x1000\n\t"
    "s_add_u32 m0, s76, 0\n\t"
    "s_nop 0\n\t"
    "buffer_load_dwordx4 v170, s[44:47], s74 offen lds\n\t"
    "buffer_load_dwordx4 v171, s[44:47], s74 offen offset:1024 lds\n\t"
    "buffer_load_dwordx4 v170, s[44:47], s74 offen offset:2048 lds\n\t"
    "buffer_load_dwordx4 v171, s[44:47], s74 offen offset:3072 lds\n\t"
    "s_add_u32 m0, s76, 0x4000\n\t"
    "s_nop 0\n\t"
    "buffer_load_dwordx4 v170, s[44:47], s75 offen lds\n\t"
    "buffer_load_dwordx4 v171, s[44:47], s75 offen offset:1024 lds\n\t"
    "buffer_load_dwordx4 v170, s[44:47], s75 offen offset:2048 lds\n\t"
    "buffer_load_dwordx4 v171, s[44:47], s75 offen offset:3072 lds\n\t"
    "s_waitcnt vmcnt(15)\n\t"
    "v_mfma_f32_16x16x32_bf16 v[108:111], a[0:3], v[196:199], v[156:159]\n\t"
    "s_waitcnt vmcnt(14)\n\t"
    "v_mfma_f32_16x16x32_bf16 v[108:111], a[4:7], v[200:203], v[108:111]\n\t"
    "s_waitcnt vmcnt(13)\n\t"
    "v_mfma_f32_16x16x32_bf16 v[108:111], a[8:11], v[204:207], v[108:111]\n\t"
    "s_waitcnt vmcnt(12)\n\t"
    "v_mfma_f32_16x16x32_bf16 v[108:111], a[12:15], v[208:211], v[108:111]\n\t"
    "v_mfma_f32_16x16x32_bf16 v[112:115], a[0:3], v[212:215], v[160:163]\n\t"
    "v_mfma_f32_16x16x32_bf16 v[112:115], a[4:7], v[216:219], v[112:115]\n\t"
    "v_mfma_f32_16x16x32_bf16 v[112:115], a[8:11], v[220:223], v[112:115]\n\t"
    "v_mfma_f32_16x16x32_bf16 v[112:115], a[12:15], v[224:227], v[112:115]\n\t"
    "v_mfma_f32_16x16x32_bf16 v[116:119], a[0:3], v[228:231], v[164:167]\n\t"
    "v_mfma_f32_16x16x32_bf16 v[116:119], a[4:7], v[232:235], v[116:119]\n\t"
    "v_mfma_f32_16x16x32_bf16 v[116:119], a[8:11], v[236:239], v[116:119]\n\t"
    "v_mfma_f32_16x16x32_bf16 v[116:119], a[12:15], v[240:243], v[116:119]\n\t"
    "s_waitcnt vmcnt(11)\n\t"
    "v_mfma_f32_16x16x32_bf16 v[120:123], a[16:19], v[196:199], v[156:159]\n\t"
    "s_waitcnt vmcnt(10)\n\t"
    "v_mfma_f32_16x16x32_bf16 v[120:123], a[20:23], v[200:203], v[120:123]\n\t"
    "s_waitcnt vmcnt(9)\n\t"
    "v_mfma_f32_16x16x32_bf16 v[120:123], a[24:27], v[204:207], v[120:123]\n\t"
    "s_waitcnt vmcnt(8)\n\t"
    "v_mfma_f32_16x16x32_bf16 v[120:123], a[28:31], v[208:211], v[120:123]\n\t"
    "v_mfma_f32_16x16x32_bf16 v[124:127], a[16:19], v[212:215], v[160:163]\n\t"
    "v_mfma_f32_16x16x32_bf16 v[124:127], a[20:23], v[216:219], v[124:127]\n\t"
    "v_mfma_f32_16x16x32_bf16 v[124:127], a[24:27], v[220:223], v[124:127]\n\t"
    "v_mfma_f32_16x16x32_bf16 v[124:127], a[28:31], v[224:227], v[124:127]\n\t"
    "v_mfma_f32_16x16x32_bf16 v[128:131], a[16:19], v[228:231], v[164:167]\n\t"
    "v_mfma_f32_16x16x32_bf16 v[128:131], a[20:23], v[232:235], v[128:131]\n\t"
    "v_mfma_f32_16x16x32_bf16 v[128:131], a[24:27], v[236:239], v[128:131]\n\t"
    "v_mfma_f32_16x16x32_bf16 v[128:131], a[28:31], v[240:243], v[128:131]\n\t"
    "v_exp_f32 v108, v108\n\t"
    "v_exp_f32 v109, v109\n\t"
    "v_exp_f32 v110, v110\n\t"
    "v_cvt_pk_bf16_f32 v132, v108, v109\n\t"
    "v_exp_f32 v111, v111\n\t"
    "v_exp_f32 v112, v112\n\t"
    "v_cvt_pk_bf16_f32 v133, v110, v111\n\t"
    "v_exp_f32 v113, v113\n\t"
    "v_exp_f32 v114, v114\n\t"
    "v_cvt_pk_bf16_f32 v136, v112, v113\n\t"
    "v_exp_f32 v115, v115\n\t"
    "v_exp_f32 v116, v116\n\t"
    "v_cvt_pk_bf16_f32 v137, v114, v115\n\t"
    "v_exp_f32 v117, v117\n\t"
    "v_exp_f32 v118, v118\n\t"
    "v_cvt_pk_bf16_f32 v140, v116, v117\n\t"
    "v_exp_f32 v119, v119\n\t"
    "v_exp_f32 v120, v120\n\t"
    "v_cvt_pk_bf16_f32 v141, v118, v119\n\t"
    "v_exp_f32 v121, v121\n\t"
    "v_exp_f32 v122, v122\n\t"
    "v_cvt_pk_bf16_f32 v134, v120, v121\n\t"
    "v_exp_f32 v123, v123\n\t"
    "v_exp_f32 v124, v124\n\t"
    "v_cvt_pk_bf16_f32 v135, v122, v123\n\t"
    "v_exp_f32 v125, v125\n\t"
    "v_exp_f32 v126, v126\n\t"
    "v_cvt_pk_bf16_f32 v138, v124, v125\n\t"
    "v_exp_f32 v127, v127\n\t"
    "v_exp_f32 v128, v128\n\t"
    "v_cvt_pk_bf16_f32 v139, v126, v127\n\t"
    "v_exp_f32 v129, v129\n\t"
    "v_exp_f32 v130, v130\n\t"
    "v_cvt_pk_bf16_f32 v142, v128, v129\n\t"
    "v_exp_f32 v131, v131\n\t"
    "s_nop 0\n\t"
    "v_cvt_pk_bf16_f32 v143, v130, v131\n\t"
    "s_waitcnt vmcnt(0)\n\t"
    "ds_read_b64_tr_b16 a[32:33], v184\n\t"
    "ds_read_b64_tr_b16 a[34:35], v184 offset:16384\n\t"
    "ds_read_b64_tr_b16 a[36:37], v185\n\t"
    "ds_read_b64_tr_b16 a[38:39], v185 offset:16384\n\t"
    "ds_read_b64_tr_b16 a[40:41], v186\n\t"
    "ds_read_b64_tr_b16 a[42:43], v186 offset:16384\n\t"
    "ds_read_b64_tr_b16 a[44:45], v187\n\t"
    "ds_read_b64_tr_b16 a[46:47], v187 offset:16384\n\t"
    "ds_read_b64_tr_b16 a[48:49], v188\n\t"
    "ds_read_b64_tr_b16 a[50:51], v188 offset:16384\n\t"
    "ds_read_b64_tr_b16 a[52:53], v189\n\t"
    "ds_read_b64_tr_b16 a[54:55], v189 offset:16384\n\t"
    "ds_read_b64_tr_b16 a[56:57], v190\n\t"
    "ds_read_b64_tr_b16 a[58:59], v190 offset:16384\n\t"
    "ds_read_b64_tr_b16 a[60:61], v191\n\t"
    "ds_read_b64_tr_b16 a[62:63], v191 offset:16384\n\t"
    "v_mfma_f32_16x16x32_bf16 v[96:99], a[240:243], v[132:135], v[96:99]\n\t"
    "v_mfma_f32_16x16x32_bf16 v[96:99], a[244:247], v[136:139], v[96:99]\n\t"
    "v_mfma_f32_16x16x32_bf16 v[96:99], a[248:251], v[140:143], v[96:99]\n\t"
    "s_waitcnt lgkmcnt(14)\n\t"
    "v_mfma_f32_16x16x32_bf16 v[64:67], a[32:35], v[140:143], v[64:67]\n\t"
    "v_mfma_f32_16x16x32_bf16 v[32:35], a[32:35], v[136:139], v[32:35]\n\t"
    "v_mfma_f32_16x16x32_bf16 v[0:3], a[32:35], v[132:135], v[0:3]\n\t"
    "s_waitcnt lgkmcnt(12)\n\t"
    "v_mfma_f32_16x16x32_bf16 v[4:7], a[36:39], v[132:135], v[4:7]\n\t"
    "v_mfma_f32_16x16x32_bf16 v[36:39], a[36:39], v[136:139], v[36:39]\n\t"
    "v_mfma_f32_16x16x32_bf16 v[68:71], a[36:39], v[140:143], v[68:71]\n\t"
    "s_waitcnt lgkmcnt(10)\n\t"
    "v_mfma_f32_16x16x32_bf16 v[72:75], a[40:43], v[140:143], v[72:75]\n\t"
    "v_mfma_f32_16x16x32_bf16 v[40:43], a[40:43], v[136:139], v[40:43]\n\t"
    "v_mfma_f32_16x16x32_bf16 v[8:11], a[40:43], v[132:135], v[8:11]\n\t"
    "s_waitcnt lgkmcnt(8)\n\t"
    "v_mfma_f32_16x16x32_bf16 v[12:15], a[44:47], v[132:135], v[12:15]\n\t"
    "v_mfma_f32_16x16x32_bf16 v[44:47], a[44:47], v[136:139], v[44:47]\n\t"
    "v_mfma_f32_16x16x32_bf16 v[76:79], a[44:47], v[140:143], v[76:79]\n\t"
    "s_waitcnt lgkmcnt(6)\n\t"
    "v_mfma_f32_16x16x32_bf16 v[80:83], a[48:51], v[140:143], v[80:83]\n\t"
    "v_mfma_f32_16x16x32_bf16 v[48:51], a[48:51], v[136:139], v[48:51]\n\t"
    "v_mfma_f32_16x16x32_bf16 v[16:19], a[48:51], v[132:135], v[16:19]\n\t"
    "s_waitcnt lgkmcnt(4)\n\t"
    "v_mfma_f32_16x16x32_bf16 v[20:23], a[52:55], v[132:135], v[20:23]\n\t"
    "v_mfma_f32_16x16x32_bf16 v[52:55], a[52:55], v[136:139], v[52:55]\n\t"
    "v_mfma_f32_16x16x32_bf16 v[84:87], a[52:55], v[140:143], v[84:87]\n\t"
    "s_waitcnt lgkmcnt(2)\n\t"
    "v_mfma_f32_16x16x32_bf16 v[88:91], a[56:59], v[140:143], v[88:91]\n\t"
    "v_mfma_f32_16x16x32_bf16 v[56:59], a[56:59], v[136:139], v[56:59]\n\t"
    "v_mfma_f32_16x16x32_bf16 v[24:27], a[56:59], v[132:135], v[24:27]\n\t"
    "s_waitcnt lgkmcnt(0)\n\t"
    "v_mfma_f32_16x16x32_bf16 v[28:31], a[60:63], v[132:135], v[28:31]\n\t"
    "v_mfma_f32_16x16x32_bf16 v[60:63], a[60:63], v[136:139], v[60:63]\n\t"
    "v_mfma_f32_16x16x32_bf16 v[92:95], a[60:63], v[140:143], v[92:95]\n\t"
    "s_add_u32 s74, s74, 0x2000\n\t"
    "s_cmp_lt_u32 s74, 0x80000\n\t"
    "s_cbranch_scc1 Lf2%=\n\t"
    "s_add_u32 s74, s34, 18\n\t"
    "s_and_b32 s74, s74, 31\n\t"
    "s_mul_i32 s74, s74, 0xa000\n\t"
    "s_add_u32 m0, s72, 0x20000\n\t"
    "s_nop 0\n\t"
    "buffer_load_dwordx4 v104, s[52:55], s74 offen lds\n\t"
    "buffer_load_dwordx4 v105, s[52:55], s74 offen offset:1024 lds\n\t"
    "buffer_load_dwordx4 v106, s[52:55], s74 offen offset:2048 lds\n\t"
    "buffer_load_dwordx4 v107, s[52:55], s74 offen offset:3072 lds\n\t"
    "s_add_u32 s74, s34, 13\n\t"
    "s_and_b32 s74, s74, 31\n\t"
    "s_mul_i32 s74, s74, 0xa000\n\t"
    "s_add_u32 m0, s72, 0x24000\n\t"
    "s_nop 0\n\t"
    "buffer_load_dwordx4 v104, s[52:55], s74 offen lds\n\t"
    "buffer_load_dwordx4 v105, s[52:55], s74 offen offset:1024 lds\n\t"
    "buffer_load_dwordx4 v106, s[52:55], s74 offen offset:2048 lds\n\t"
    "buffer_load_dwordx4 v107, s[52:55], s74 offen offset:3072 lds\n\t"
    "s_branch Lepi%=\n\t"
    "Lend%=:\n\t"
    "s_waitcnt vmcnt(0) lgkmcnt(0)\n\t"
    :
    : "s"(__builtin_amdgcn_kernarg_segment_ptr()), "s"(wg), "s"(wid)
    : "v0", "v1", "v2", "v3", "v4", "v5", "v6", "v7", "v8", "v9", "v10", "v11", "v12", "v13", "v14", "v15", "v16", "v17", "v18", "v19", "v20", "v21", "v22", "v23",
      "v24", "v25", "v26", "v27", "v28", "v29", "v30", "v31", "v32", "v33", "v34", "v35", "v36", "v37", "v38", "v39", "v40", "v41", "v42", "v43", "v44", "v45", "v46", "v47",
      "v48", "v49", "v50", "v51", "v52", "v53", "v54", "v55", "v56", "v57", "v58", "v59", "v60", "v61", "v62", "v63", "v64", "v65", "v66", "v67", "v68", "v69", "v70", "v71",
      "v72", "v73", "v74", "v75", "v76", "v77", "v78", "v79", "v80", "v81", "v82", "v83", "v84", "v85", "v86", "v87", "v88", "v89", "v90", "v91", "v92", "v93", "v94", "v95",
      "v96", "v97", "v98", "v99", "v100", "v101", "v102", "v103", "v104", "v105", "v106", "v107", "v108", "v109", "v110", "v111", "v112", "v113", "v114", "v115", "v116", "v117", "v118", "v119",
      "v120", "v121", "v122", "v123", "v124", "v125", "v126", "v127", "v128", "v129", "v130", "v131", "v132", "v133", "v134", "v135", "v136", "v137", "v138", "v139", "v140", "v141", "v142", "v143",
      "v144", "v145", "v146", "v147", "v148", "v149", "v150", "v151", "v152", "v153", "v154", "v155", "v156", "v157", "v158", "v159", "v160", "v161", "v162", "v163", "v164", "v165", "v166", "v167",
      "v168", "v169", "v170", "v171", "v172", "v173", "v174", "v175", "v176", "v177", "v178", "v179", "v180", "v181", "v182", "v183", "v184", "v185", "v186", "v187", "v188", "v189", "v190", "v191",
      "v192", "v193", "v194", "v195", "v196", "v197", "v198", "v199", "v200", "v201", "v202", "v203", "v204", "v205", "v206", "v207", "v208", "v209", "v210", "v211", "v212", "v213", "v214", "v215",
      "v216", "v217", "v218", "v219", "v220", "v221", "v222", "v223", "v224", "v225", "v226", "v227", "v228", "v229", "v230", "v231", "v232", "v233", "v234", "v235", "v236", "v237", "v238", "v239",
      "v240", "v241", "v242", "v243", "v244", "v245", "v246", "v247", "v248", "v249", "v250", "v251", "v252", "v253", "v254", "v255", "a0", "a1", "a2", "a3", "a4", "a5", "a6", "a7",
      "a8", "a9", "a10", "a11", "a12", "a13", "a14", "a15", "a16", "a17", "a18", "a19", "a20", "a21", "a22", "a23", "a24", "a25", "a26", "a27", "a28", "a29", "a30", "a31",
      "a32", "a33", "a34", "a35", "a36", "a37", "a38", "a39", "a40", "a41", "a42", "a43", "a44", "a45", "a46", "a47", "a48", "a49", "a50", "a51", "a52", "a53", "a54", "a55",
      "a56", "a57", "a58", "a59", "a60", "a61", "a62", "a63", "a64", "a65", "a66", "a67", "a68", "a69", "a70", "a71", "a72", "a73", "a74", "a75", "a76", "a77", "a78", "a79",
      "a80", "a81", "a82", "a83", "a84", "a85", "a86", "a87", "a88", "a89", "a90", "a91", "a92", "a93", "a94", "a95", "a96", "a97", "a98", "a99", "a100", "a101", "a102", "a103",
      "a104", "a105", "a106", "a107", "a108", "a109", "a110", "a111", "a112", "a113", "a114", "a115", "a116", "a117", "a118", "a119", "a120", "a121", "a122", "a123", "a124", "a125", "a126", "a127",
      "a128", "a129", "a130", "a131", "a132", "a133", "a134", "a135", "a136", "a137", "a138", "a139", "a140", "a141", "a142", "a143", "a144", "a145", "a146", "a147", "a148", "a149", "a150", "a151",
      "a152", "a153", "a154", "a155", "a156", "a157", "a158", "a159", "a160", "a161", "a162", "a163", "a164", "a165", "a166", "a167", "a168", "a169", "a170", "a171", "a172", "a173", "a174", "a175",
      "a176", "a177", "a178", "a179", "a180", "a181", "a182", "a183", "a184", "a185", "a186", "a187", "a188", "a189", "a190", "a191", "a192", "a193", "a194", "a195", "a196", "a197", "a198", "a199",
      "a200", "a201", "a202", "a203", "a204", "a205", "a206", "a207", "a208", "a209", "a210", "a211", "a212", "a213", "a214", "a215", "a216", "a217", "a218", "a219", "a220", "a221", "a222", "a223",
      "a224", "a225", "a226", "a227", "a228", "a229", "a230", "a231", "a232", "a233", "a234", "a235", "a236", "a237", "a238", "a239", "a240", "a241", "a242", "a243", "a244", "a245", "a246", "a247",
      "a248", "a249", "a250", "a251", "a252", "a253", "a254", "a255", "s4", "s5", "s6", "s7", "s8", "s9", "s10", "s11", "s12", "s13", "s14", "s15", "s16", "s17", "s18", "s19",
      "s20", "s21", "s22", "s23", "s24", "s25", "s26", "s27", "s28", "s29", "s30", "s31", "s32", "s33", "s34", "s35", "s36", "s37", "s38", "s39", "s40", "s41", "s42", "s43",
      "s44", "s45", "s46", "s47", "s48", "s49", "s50", "s51", "s52", "s53", "s54", "s55", "s56", "s57", "s58", "s59", "s60", "s61", "s62", "s63", "s64", "s65", "s66", "s67",
      "s68", "s69", "s70", "s71", "s72", "s73", "s74", "s75", "s76", "s77", "s78", "s79", "s80", "s81", "s82", "s83", "s84", "s85", "s86", "s87", "s88", "s89", "s90", "s91",
      "s92", "s93", "s94", "s95", "s96", "s97", "s98", "s99", "vcc", "scc", "memory");
}

extern "C" void kernel_launch(void* const* d_in, const int* in_sizes, int n_in, void* d_out, int out_size, void* d_ws, size_t ws_size, hipStream_t stream) {
  (void)hipFuncSetAttribute((const void*)attn_kernel, hipFuncAttributeMaxDynamicSharedMemorySize, 163840);
  attn_kernel<<<256, 256, 163840, stream>>>(d_in[0], d_in[1], d_in[2], d_in[3], d_in[4], d_out, d_ws);
}
```
